```python
import torch

def _softmax(x, axis=-1):
    return torch.softmax(x, dim=axis)

def _gelu(x):
    return torch.nn.functional.gelu(x, approximate='tanh')

def _silu(x):
    return torch.nn.functional.silu(x)

def _sigmoid(x):
    return torch.sigmoid(x)

def _layernorm(x, axis=-1, eps=1e-05):
    m = x.mean(dim=axis, keepdim=True)
    v = x.var(dim=axis, keepdim=True, unbiased=False)
    return (x - m) / torch.sqrt(v + eps)

def _scatter_add(tgt, idx, src, axis=0):
    return tgt.clone().index_add_(axis, idx, src)

def _segment_sum(src, seg, n):
    out = torch.zeros((n,) + src.shape[1:], dtype=src.dtype)
    return out.index_add_(0, seg, src)

def _pad(x, pw):
    flat = tuple((v for pair in reversed(pw) for v in pair))
    return torch.nn.functional.pad(x, flat)
kernel_name = 'recurrent_scan__memory_aug_recurrent_mem_tr__dense_activation_gelu__prompt'

def setup_inputs():

    _w = lambda a: (lambda t: t.to(torch.bfloat16) if t.is_floating_point() else t)(torch.as_tensor(a))

    def _A_setup():
        B, S, Dm = (1, 2048, 640)
        H, Dh = (32, 192)
        M, seg = (8, 32)
        seg = min(seg, S)
        S = S // seg * seg
        return {'x': _w((lambda *s: torch.randn(*s, dtype=torch.bfloat16) * 0.1)(B, S, Dm)), 'mem0': _w((lambda *s: torch.randn(*s, dtype=torch.bfloat16) * 1.6)(B, M, Dm)), 'Wq': _w((lambda *s: torch.randn(*s, dtype=torch.bfloat16) * 0.8)(Dm, H, Dh) * Dm ** (-0.5)), 'Wk': _w((lambda *s: torch.randn(*s, dtype=torch.bfloat16) * 0.8)(Dm, H, Dh) * Dm ** (-0.5)), 'Wv': _w((lambda *s: torch.randn(*s, dtype=torch.bfloat16) * 1.6)(Dm, H, Dh) * Dm ** (-0.5)), 'Wo': _w((lambda *s: torch.randn(*s, dtype=torch.bfloat16) * 1.6)(H, Dh, Dm) * (H * Dh) ** (-0.5)), 'seg_len': seg}
    _A_in = _A_setup()

    def _B_setup():
        B, S, D, F = (1, 2048, 640, 1536)
        return {'x': _w((lambda *s: torch.randn(*s, dtype=torch.bfloat16) * 0.1)(B, S, D)), 'w1': _w((lambda *s: torch.randn(*s, dtype=torch.bfloat16) * 0.4)(D, F) * D ** (-0.5)), 'b1': _w((lambda *s: torch.randn(*s, dtype=torch.bfloat16) * 0.4)(F) * 0.01), 'w2': _w((lambda *s: torch.randn(*s, dtype=torch.bfloat16) * 0.4)(F, D) * F ** (-0.5)), 'b2': _w((lambda *s: torch.randn(*s, dtype=torch.bfloat16) * 0.4)(D) * 0.01)}
    _B_in = _B_setup()
    return {'A_x': _A_in['x'], 'A_mem0': _A_in['mem0'], 'A_Wq': _A_in['Wq'], 'A_Wk': _A_in['Wk'], 'A_Wv': _A_in['Wv'], 'A_Wo': _A_in['Wo'], 'A_seg_len': _A_in['seg_len'], 'B_x': _B_in['x'], 'B_w1': _B_in['w1'], 'B_b1': _B_in['b1'], 'B_w2': _B_in['w2'], 'B_b2': _B_in['b2']}

def reference(A_x, A_mem0, A_Wq, A_Wk, A_Wv, A_Wo, A_seg_len, B_x, B_w1, B_b1, B_w2, B_b2):

    def _A_ref(x, mem0, Wq, Wk, Wv, Wo, seg_len):
        B, S, Dm = x.shape
        H, Dh = (Wq.shape[1], Wq.shape[2])
        M = mem0.shape[1]
        scale = Dh ** (-0.5)
        n_seg = S // seg_len
        L = M + seg_len
        idx = torch.arange(L)
        mask = (idx[:, None] < M) | (idx[:, None] >= idx[None, :])
        mem = mem0
        outs = []
        for s in range(n_seg):
            xs = x[:, s * seg_len:(s + 1) * seg_len, :].clone()
            h = torch.concatenate([mem, xs], axis=1)
            q = torch.einsum('btd,dhe->bhte', h, Wq)
            k = torch.einsum('btd,dhe->bhte', h, Wk)
            v = torch.einsum('btd,dhe->bhte', h, Wv)
            scores = torch.einsum('bhqe,bhke->bhqk', q, k) * scale
            scores = torch.where(mask[None, None], scores, -1000000000.0)
            p = _softmax(scores, axis=-1)
            o = torch.einsum('bhqk,bhke->bhqe', p, v)
            o = torch.einsum('bhte,hed->btd', o, Wo)
            mem = o[:, :M, :].clone()
            outs.append(o[:, M:, :])
        return torch.concatenate(outs, axis=1)

    def _B_ref(x, w1, b1, w2, b2):
        h = torch.einsum('bsd,df->bsf', x, w1) + b1
        h = _gelu(h)
        return torch.einsum('bsf,fd->bsd', h, w2) + b2
    x = A_x
    xn = _layernorm(x, axis=-1)
    s = _A_ref(xn, A_mem0, A_Wq, A_Wk, A_Wv, A_Wo, A_seg_len)
    g = _silu(_B_ref(xn, B_w1, B_b1, B_w2, B_b2))
    return x + s * g
```

```cpp
#include <hip/hip_runtime.h>
#include <hip/hip_bf16.h>
#include <stdint.h>

namespace rk {

typedef unsigned short u16;
typedef unsigned v4u __attribute__((ext_vector_type(4)));
typedef unsigned v2u __attribute__((ext_vector_type(2)));
typedef unsigned v8u __attribute__((ext_vector_type(8)));
typedef float f32x4 __attribute__((ext_vector_type(4)));
typedef float f32x2 __attribute__((ext_vector_type(2)));
typedef short s16x4 __attribute__((ext_vector_type(4)));
typedef __bf16 bf16x8 __attribute__((ext_vector_type(8)));
typedef __bf16 bf16x16 __attribute__((ext_vector_type(16)));
typedef __bf16 bf16x2 __attribute__((ext_vector_type(2)));
#define LAS __attribute__((address_space(3)))
#define GAS __attribute__((address_space(1)))
#define RLX_AGENT __ATOMIC_RELAXED, __HIP_MEMORY_SCOPE_AGENT

constexpr int S_ = 2048, D_ = 640, H_ = 32, E_ = 192, M_ = 8, SEG = 32, NSEG = 64, F_ = 1536, HE = H_ * E_;
constexpr int NS = 32, NZ = 80;
constexpr int MEMB = 12288, PB = 22528;
constexpr unsigned SENT = 0xFFFFFFFFu;
constexpr unsigned SPINCAP = 1u << 17;

constexpr size_t OFF_CTRL = 0;
constexpr int C_ABORT = 0, C_PROG = 128, C_FINAL = 256, C_W = 384, C_KV = 512, C_Q = C_KV + 2048, C_F1 = C_Q + 2048, C_F2 = C_F1 + 2048,
              C_P = C_F2 + 2048, C_QUEUE = C_P + 8192, CTRL_BYTES = 32768;
constexpr size_t OFF_MSGM = CTRL_BYTES;
constexpr size_t OFF_MSGP = OFF_MSGM + (size_t)(NSEG + 1) * MEMB;
constexpr size_t MSG_END = OFF_MSGP + (size_t)NSEG * PB;
constexpr size_t OFF_XN = (MSG_END + 4095) / 4096 * 4096;
constexpr size_t OFF_QX = OFF_XN + (size_t)S_ * D_ * 2;
constexpr size_t OFF_KX = OFF_QX + (size_t)S_ * HE * 2;
constexpr size_t OFF_G = OFF_KX + (size_t)S_ * HE * 2;
constexpr size_t OFF_WVO = OFF_G + (size_t)NSEG * NZ * 20480;
constexpr size_t OFF_HID = OFF_WVO + (size_t)NZ * 4 * 4 * 10 * 2048;
constexpr size_t OFF_Y = OFF_HID + (size_t)S_ * F_ * 2;
constexpr size_t OFF_P = OFF_Y + (size_t)S_ * D_ * 2;
constexpr size_t OFF_KM = OFF_P + (size_t)S_ * 1280 * 2;
constexpr size_t WS_END = OFF_KM + (size_t)NSEG * H_ * M_ * E_ * 2;
static_assert(WS_END + 16384 + 65536 <= (size_t)256 * 1024 * 1024, "workspace");

__device__ __forceinline__ float bf2f(u16 b) { return __uint_as_float((unsigned)b << 16); }
__device__ __forceinline__ unsigned pk2(float lo, float hi) {
  f32x2 v = {lo, hi};
  bf16x2 r = __builtin_convertvector(v, bf16x2);
  return __builtin_bit_cast(unsigned, r);
}
__device__ __forceinline__ u16 f2bf(float f) { return (u16)(pk2(f, 0.f) & 0xFFFFu); }

__host__ __device__ constexpr int bfrag_d(int bg, int j) {
  return 16 * ((bg >> 1) + 2 * (j >> 3)) + 8 * ((j & 3) >> 1) + 2 * (2 * (bg & 1) + ((j >> 2) & 1)) + (j & 1);
}

__global__ void __launch_bounds__(256) k_prep(const u16* __restrict__ x, unsigned char* __restrict__ ws) {
  const int b = blockIdx.x, tid = threadIdx.x, lane = tid & 63, w = tid >> 6;
  if (b < 512) {
    const int row = b * 4 + w;
    const u16* xr = x + (size_t)row * D_;
    float v[10];
    float s = 0.f;
#pragma unroll
    for (int i = 0; i < 5; ++i) {
      unsigned u = *(const unsigned*)(xr + i * 128 + lane * 2);
      v[2 * i] = bf2f(u & 0xFFFF); v[2 * i + 1] = bf2f(u >> 16);
      s += v[2 * i] + v[2 * i + 1];
    }
#pragma unroll
    for (int o = 32; o > 0; o >>= 1) s += __shfl_xor(s, o);
    const float mean = s * (1.f / D_);
    float q = 0.f;
#pragma unroll
    for (int i = 0; i < 10; ++i) { v[i] -= mean; q += v[i] * v[i]; }
#pragma unroll
    for (int o = 32; o > 0; o >>= 1) q += __shfl_xor(q, o);
    const float rstd = 1.f / sqrtf(q * (1.f / D_) + 1e-5f);
    unsigned char* xo = ws + OFF_XN + (size_t)(row >> 7) * 163840 + ((row >> 4) & 7) * 1024 + (row & 15) * 16;
#pragma unroll
    for (int i = 0; i < 5; ++i) {
      const int d = i * 128 + lane * 2;
      *(unsigned*)(xo + (d >> 5) * 8192 + ((d >> 3) & 3) * 256 + (d & 7) * 2) = pk2(v[2 * i] * rstd, v[2 * i + 1] * rstd);
    }
  } else {
    const int bb = b - 512;
    v4u sent = {SENT, SENT, SENT, SENT};
    v4u zero = {0u, 0u, 0u, 0u};
    for (size_t o = (size_t)bb * 4096 + tid * 16; o < MSG_END; o += (size_t)128 * 4096) *(v4u*)(ws + o) = (o < CTRL_BYTES) ? zero : sent;
  }
}

typedef int v4i __attribute__((ext_vector_type(4)));
constexpr int OOB = 0x40000000;
#define RK_ASM_GT \
    "s_mov_b32 s41, %[aoff]\n\t" \
    "s_mov_b32 s42, %[boff]\n\t" \
    "s_mov_b32 s44, 0\n\t" \
    "s_mov_b32 s43, 0\n\t" \
    "s_mov_b32 s48, %[bm]\n\t" \
    "s_mov_b32 s49, 0x40000000\n\t" \
    "s_mov_b32 s40, 0\n\t" \
    "s_barrier\n\t" \
    "s_add_u32 m0, s44, %[wq]\n\t" \
    "s_cmp_lt_u32 s41, %[aend]\n\t" \
    "s_cselect_b32 s45, s41, s49\n\t" \
    "buffer_load_dwordx4 %[asrc], %[srda], s45 offen lds\n\t" \
    "buffer_load_dwordx4 %[asrc], %[srda], s45 offen offset:1024 lds\n\t" \
    "buffer_load_dwordx4 %[asrc], %[srda], s45 offen offset:2048 lds\n\t" \
    "buffer_load_dwordx4 %[asrc], %[srda], s45 offen offset:3072 lds\n\t" \
    "s_add_u32 s41, s41, 16384\n\t" \
    "s_add_u32 s44, s44, 16384\n\t" \
    "s_cmp_eq_u32 s44, 49152\n\t" \
    "s_cselect_b32 s44, 0, s44\n\t" \
    "s_add_u32 m0, s48, 0\n\t" \
    "s_cmp_lt_u32 s42, %[bend]\n\t" \
    "s_cselect_b32 s46, s42, s49\n\t" \
    "buffer_load_dwordx4 %[bo0], %[srdb], s46 offen lds\n\t" \
    "buffer_load_dwordx4 %[bo1], %[srdb], s46 offen offset:1024 lds\n\t" \
    "buffer_load_dwordx4 %[bo2], %[srdb], s46 offen offset:2048 lds\n\t" \
    "buffer_load_dwordx4 %[bo3], %[srdb], s46 offen offset:3072 lds\n\t" \
    "s_add_u32 m0, s48, 4096\n\t" \
    "s_add_u32 s42, s42, %[bstep]\n\t" \
    "buffer_load_dwordx4 %[bo4], %[srdb], s46 offen lds\n\t" \
    "buffer_load_dwordx4 %[bo5], %[srdb], s46 offen offset:1024 lds\n\t" \
    "s_add_u32 m0, s44, %[wq]\n\t" \
    "s_cmp_lt_u32 s41, %[aend]\n\t" \
    "s_cselect_b32 s45, s41, s49\n\t" \
    "buffer_load_dwordx4 %[asrc], %[srda], s45 offen lds\n\t" \
    "buffer_load_dwordx4 %[asrc], %[srda], s45 offen offset:1024 lds\n\t" \
    "buffer_load_dwordx4 %[asrc], %[srda], s45 offen offset:2048 lds\n\t" \
    "buffer_load_dwordx4 %[asrc], %[srda], s45 offen offset:3072 lds\n\t" \
    "s_add_u32 s41, s41, 16384\n\t" \
    "s_add_u32 s44, s44, 16384\n\t" \
    "s_cmp_eq_u32 s44, 49152\n\t" \
    "s_cselect_b32 s44, 0, s44\n\t" \
    "s_add_u32 m0, s48, 6144\n\t" \
    "s_cmp_lt_u32 s42, %[bend]\n\t" \
    "s_cselect_b32 s46, s42, s49\n\t" \
    "buffer_load_dwordx4 %[bo0], %[srdb], s46 offen lds\n\t" \
    "buffer_load_dwordx4 %[bo1], %[srdb], s46 offen offset:1024 lds\n\t" \
    "buffer_load_dwordx4 %[bo2], %[srdb], s46 offen offset:2048 lds\n\t" \
    "buffer_load_dwordx4 %[bo3], %[srdb], s46 offen offset:3072 lds\n\t" \
    "s_add_u32 m0, s48, 10240\n\t" \
    "s_add_u32 s42, s42, %[bstep]\n\t" \
    "buffer_load_dwordx4 %[bo4], %[srdb], s46 offen lds\n\t" \
    "buffer_load_dwordx4 %[bo5], %[srdb], s46 offen offset:1024 lds\n\t" \
    "s_add_u32 m0, s48, 12288\n\t" \
    "s_cmp_lt_u32 s42, %[bend]\n\t" \
    "s_cselect_b32 s46, s42, s49\n\t" \
    "buffer_load_dwordx4 %[bo0], %[srdb], s46 offen lds\n\t" \
    "buffer_load_dwordx4 %[bo1], %[srdb], s46 offen offset:1024 lds\n\t" \
    "buffer_load_dwordx4 %[bo2], %[srdb], s46 offen offset:2048 lds\n\t" \
    "buffer_load_dwordx4 %[bo3], %[srdb], s46 offen offset:3072 lds\n\t" \
    "s_add_u32 m0, s48, 16384\n\t" \
    "s_add_u32 s42, s42, %[bstep]\n\t" \
    "buffer_load_dwordx4 %[bo4], %[srdb], s46 offen lds\n\t" \
    "buffer_load_dwordx4 %[bo5], %[srdb], s46 offen offset:1024 lds\n\t" \
    "s_add_u32 m0, s44, %[wq]\n\t" \
    "s_cmp_lt_u32 s41, %[aend]\n\t" \
    "s_cselect_b32 s45, s41, s49\n\t" \
    "buffer_load_dwordx4 %[asrc], %[srda], s45 offen lds\n\t" \
    "buffer_load_dwordx4 %[asrc], %[srda], s45 offen offset:1024 lds\n\t" \
    "buffer_load_dwordx4 %[asrc], %[srda], s45 offen offset:2048 lds\n\t" \
    "buffer_load_dwordx4 %[asrc], %[srda], s45 offen offset:3072 lds\n\t" \
    "s_add_u32 s41, s41, 16384\n\t" \
    "s_add_u32 s44, s44, 16384\n\t" \
    "s_cmp_eq_u32 s44, 49152\n\t" \
    "s_cselect_b32 s44, 0, s44\n\t" \
    "s_add_u32 m0, s48, 18432\n\t" \
    "s_cmp_lt_u32 s42, %[bend]\n\t" \
    "s_cselect_b32 s46, s42, s49\n\t" \
    "buffer_load_dwordx4 %[bo0], %[srdb], s46 offen lds\n\t" \
    "buffer_load_dwordx4 %[bo1], %[srdb], s46 offen offset:1024 lds\n\t" \
    "buffer_load_dwordx4 %[bo2], %[srdb], s46 offen offset:2048 lds\n\t" \
    "buffer_load_dwordx4 %[bo3], %[srdb], s46 offen offset:3072 lds\n\t" \
    "s_add_u32 m0, s48, 22528\n\t" \
    "s_add_u32 s42, s42, %[bstep]\n\t" \
    "buffer_load_dwordx4 %[bo4], %[srdb], s46 offen lds\n\t" \
    "buffer_load_dwordx4 %[bo5], %[srdb], s46 offen offset:1024 lds\n\t" \
    "v_mov_b64 v[24:25], 0\n\t" \
    "v_mov_b64 v[26:27], 0\n\t" \
    "v_mov_b64 v[28:29], 0\n\t" \
    "v_mov_b64 v[30:31], 0\n\t" \
    "v_mov_b64 v[32:33], 0\n\t" \
    "v_mov_b64 v[34:35], 0\n\t" \
    "v_mov_b64 v[36:37], 0\n\t" \
    "v_mov_b64 v[38:39], 0\n\t" \
    "v_mov_b64 v[40:41], 0\n\t" \
    "v_mov_b64 v[42:43], 0\n\t" \
    "v_mov_b64 v[44:45], 0\n\t" \
    "v_mov_b64 v[46:47], 0\n\t" \
    "v_mov_b64 v[48:49], 0\n\t" \
    "v_mov_b64 v[50:51], 0\n\t" \
    "v_mov_b64 v[52:53], 0\n\t" \
    "v_mov_b64 v[54:55], 0\n\t" \
    "v_mov_b64 v[56:57], 0\n\t" \
    "v_mov_b64 v[58:59], 0\n\t" \
    "v_mov_b64 v[60:61], 0\n\t" \
    "v_mov_b64 v[62:63], 0\n\t" \
    "v_mov_b64 v[64:65], 0\n\t" \
    "v_mov_b64 v[66:67], 0\n\t" \
    "v_mov_b64 v[68:69], 0\n\t" \
    "v_mov_b64 v[70:71], 0\n\t" \
    "v_mov_b64 v[72:73], 0\n\t" \
    "v_mov_b64 v[74:75], 0\n\t" \
    "v_mov_b64 v[76:77], 0\n\t" \
    "v_mov_b64 v[78:79], 0\n\t" \
    "v_mov_b64 v[80:81], 0\n\t" \
    "v_mov_b64 v[82:83], 0\n\t" \
    "v_mov_b64 v[84:85], 0\n\t" \
    "v_mov_b64 v[86:87], 0\n\t" \
    "v_mov_b64 v[88:89], 0\n\t" \
    "v_mov_b64 v[90:91], 0\n\t" \
    "v_mov_b64 v[92:93], 0\n\t" \
    "v_mov_b64 v[94:95], 0\n\t" \
    "v_mov_b64 v[96:97], 0\n\t" \
    "v_mov_b64 v[98:99], 0\n\t" \
    "v_mov_b64 v[100:101], 0\n\t" \
    "v_mov_b64 v[102:103], 0\n\t" \
    "v_mov_b64 v[104:105], 0\n\t" \
    "v_mov_b64 v[106:107], 0\n\t" \
    "v_mov_b64 v[108:109], 0\n\t" \
    "v_mov_b64 v[110:111], 0\n\t" \
    "v_mov_b64 v[112:113], 0\n\t" \
    "v_mov_b64 v[114:115], 0\n\t" \
    "v_mov_b64 v[116:117], 0\n\t" \
    "v_mov_b64 v[118:119], 0\n\t" \
    "v_mov_b64 v[120:121], 0\n\t" \
    "v_mov_b64 v[122:123], 0\n\t" \
    "v_mov_b64 v[124:125], 0\n\t" \
    "v_mov_b64 v[126:127], 0\n\t" \
    "v_mov_b64 v[128:129], 0\n\t" \
    "v_mov_b64 v[130:131], 0\n\t" \
    "v_mov_b64 v[132:133], 0\n\t" \
    "v_mov_b64 v[134:135], 0\n\t" \
    "v_mov_b64 v[136:137], 0\n\t" \
    "v_mov_b64 v[138:139], 0\n\t" \
    "v_mov_b64 v[140:141], 0\n\t" \
    "v_mov_b64 v[142:143], 0\n\t" \
    "v_mov_b64 v[144:145], 0\n\t" \
    "v_mov_b64 v[146:147], 0\n\t" \
    "v_mov_b64 v[148:149], 0\n\t" \
    "v_mov_b64 v[150:151], 0\n\t" \
    "v_mov_b64 v[152:153], 0\n\t" \
    "v_mov_b64 v[154:155], 0\n\t" \
    "v_mov_b64 v[156:157], 0\n\t" \
    "v_mov_b64 v[158:159], 0\n\t" \
    "v_mov_b64 v[160:161], 0\n\t" \
    "v_mov_b64 v[162:163], 0\n\t" \
    "v_mov_b64 v[164:165], 0\n\t" \
    "v_mov_b64 v[166:167], 0\n\t" \
    "v_mov_b64 v[168:169], 0\n\t" \
    "v_mov_b64 v[170:171], 0\n\t" \
    "v_mov_b64 v[172:173], 0\n\t" \
    "v_mov_b64 v[174:175], 0\n\t" \
    "v_mov_b64 v[176:177], 0\n\t" \
    "v_mov_b64 v[178:179], 0\n\t" \
    "v_mov_b64 v[180:181], 0\n\t" \
    "v_mov_b64 v[182:183], 0\n\t" \
    "v_mov_b64 v[184:185], 0\n\t" \
    "v_mov_b64 v[186:187], 0\n\t" \
    "v_mov_b64 v[188:189], 0\n\t" \
    "v_mov_b64 v[190:191], 0\n\t" \
    "v_mov_b64 v[192:193], 0\n\t" \
    "v_mov_b64 v[194:195], 0\n\t" \
    "v_mov_b64 v[196:197], 0\n\t" \
    "v_mov_b64 v[198:199], 0\n\t" \
    "v_mov_b64 v[200:201], 0\n\t" \
    "v_mov_b64 v[202:203], 0\n\t" \
    "v_mov_b64 v[204:205], 0\n\t" \
    "v_mov_b64 v[206:207], 0\n\t" \
    "v_mov_b64 v[208:209], 0\n\t" \
    "v_mov_b64 v[210:211], 0\n\t" \
    "v_mov_b64 v[212:213], 0\n\t" \
    "v_mov_b64 v[214:215], 0\n\t" \
    "v_mov_b32 v232, %[ard]\n\t" \
    "s_waitcnt vmcnt(26)\n\t" \
    "s_barrier\n\t" \
    "ds_read_b64_tr_b16 a[64:65], %[brd] offset:0\n\t" \
    "ds_read_b64_tr_b16 a[66:67], %[brd] offset:1536\n\t" \
    "ds_read_b128 a[0:3], v232 offset:0\n\t" \
    "ds_read_b128 a[4:7], v232 offset:1024\n\t" \
    "ds_read_b128 a[8:11], v232 offset:2048\n\t" \
    "ds_read_b128 a[12:15], v232 offset:3072\n\t" \
    "ds_read_b128 a[16:19], v232 offset:4096\n\t" \
    "ds_read_b128 a[20:23], v232 offset:5120\n\t" \
    "ds_read_b128 a[24:27], v232 offset:6144\n\t" \
    "ds_read_b128 a[28:31], v232 offset:7168\n\t" \
    "ds_read_b64_tr_b16 a[68:69], %[brd] offset:256\n\t" \
    "ds_read_b64_tr_b16 a[70:71], %[brd] offset:1792\n\t" \
    "ds_read_b64_tr_b16 a[72:73], %[brd] offset:512\n\t" \
    "ds_read_b64_tr_b16 a[74:75], %[brd] offset:2048\n\t" \
    "ds_read_b64_tr_b16 a[76:77], %[brd] offset:768\n\t" \
    "ds_read_b64_tr_b16 a[78:79], %[brd] offset:2304\n\t" \
    "ds_read_b64_tr_b16 a[80:81], %[brd] offset:1024\n\t" \
    "ds_read_b64_tr_b16 a[82:83], %[brd] offset:2560\n\t" \
    "ds_read_b64_tr_b16 a[84:85], %[brd] offset:1280\n\t" \
    "ds_read_b64_tr_b16 a[86:87], %[brd] offset:2816\n\t" \
    "tl_1_%=:\n\t" \
    "s_waitcnt vmcnt(16) lgkmcnt(0)\n\t" \
    "v_mfma_f32_16x16x32_bf16 v[24:27], a[64:67], a[0:3], v[24:27]\n\t" \
    "ds_read_b64_tr_b16 a[88:89], %[brd] offset:6144\n\t" \
    "v_mfma_f32_16x16x32_bf16 v[28:31], a[64:67], a[4:7], v[28:31]\n\t" \
    "ds_read_b64_tr_b16 a[90:91], %[brd] offset:7680\n\t" \
    "v_mfma_f32_16x16x32_bf16 v[32:35], a[64:67], a[8:11], v[32:35]\n\t" \
    "ds_read_b128 a[32:35], v232 offset:8192\n\t" \
    "v_mfma_f32_16x16x32_bf16 v[36:39], a[64:67], a[12:15], v[36:39]\n\t" \
    "ds_read_b128 a[36:39], v232 offset:9216\n\t" \
    "v_mfma_f32_16x16x32_bf16 v[40:43], a[64:67], a[16:19], v[40:43]\n\t" \
    "ds_read_b128 a[40:43], v232 offset:10240\n\t" \
    "v_mfma_f32_16x16x32_bf16 v[44:47], a[64:67], a[20:23], v[44:47]\n\t" \
    "ds_read_b128 a[44:47], v232 offset:11264\n\t" \
    "v_mfma_f32_16x16x32_bf16 v[48:51], a[64:67], a[24:27], v[48:51]\n\t" \
    "ds_read_b128 a[48:51], v232 offset:12288\n\t" \
    "v_mfma_f32_16x16x32_bf16 v[52:55], a[64:67], a[28:31], v[52:55]\n\t" \
    "ds_read_b128 a[52:55], v232 offset:13312\n\t" \
    "v_mfma_f32_16x16x32_bf16 v[56:59], a[68:71], a[0:3], v[56:59]\n\t" \
    "ds_read_b128 a[56:59], v232 offset:14336\n\t" \
    "v_mfma_f32_16x16x32_bf16 v[60:63], a[68:71], a[4:7], v[60:63]\n\t" \
    "ds_read_b128 a[60:63], v232 offset:15360\n\t" \
    "v_mfma_f32_16x16x32_bf16 v[64:67], a[68:71], a[8:11], v[64:67]\n\t" \
    "ds_read_b64_tr_b16 a[92:93], %[brd] offset:6400\n\t" \
    "v_mfma_f32_16x16x32_bf16 v[68:71], a[68:71], a[12:15], v[68:71]\n\t" \
    "ds_read_b64_tr_b16 a[94:95], %[brd] offset:7936\n\t" \
    "v_mfma_f32_16x16x32_bf16 v[72:75], a[68:71], a[16:19], v[72:75]\n\t" \
    "ds_read_b64_tr_b16 a[96:97], %[brd] offset:6656\n\t" \
    "v_mfma_f32_16x16x32_bf16 v[76:79], a[68:71], a[20:23], v[76:79]\n\t" \
    "ds_read_b64_tr_b16 a[98:99], %[brd] offset:8192\n\t" \
    "v_mfma_f32_16x16x32_bf16 v[80:83], a[68:71], a[24:27], v[80:83]\n\t" \
    "ds_read_b64_tr_b16 a[100:101], %[brd] offset:6912\n\t" \
    "v_mfma_f32_16x16x32_bf16 v[84:87], a[68:71], a[28:31], v[84:87]\n\t" \
    "ds_read_b64_tr_b16 a[102:103], %[brd] offset:8448\n\t" \
    "v_mfma_f32_16x16x32_bf16 v[88:91], a[72:75], a[0:3], v[88:91]\n\t" \
    "ds_read_b64_tr_b16 a[104:105], %[brd] offset:7168\n\t" \
    "v_mfma_f32_16x16x32_bf16 v[92:95], a[72:75], a[4:7], v[92:95]\n\t" \
    "ds_read_b64_tr_b16 a[106:107], %[brd] offset:8704\n\t" \
    "v_mfma_f32_16x16x32_bf16 v[96:99], a[72:75], a[8:11], v[96:99]\n\t" \
    "ds_read_b64_tr_b16 a[108:109], %[brd] offset:7424\n\t" \
    "v_mfma_f32_16x16x32_bf16 v[100:103], a[72:75], a[12:15], v[100:103]\n\t" \
    "ds_read_b64_tr_b16 a[110:111], %[brd] offset:8960\n\t" \
    "v_mfma_f32_16x16x32_bf16 v[104:107], a[72:75], a[16:19], v[104:107]\n\t" \
    "s_add_u32 m0, s48, 0\n\t" \
    "s_cmp_lt_u32 s42, %[bend]\n\t" \
    "s_cselect_b32 s46, s42, s49\n\t" \
    "buffer_load_dwordx4 %[bo0], %[srdb], s46 offen lds\n\t" \
    "v_mfma_f32_16x16x32_bf16 v[108:111], a[72:75], a[20:23], v[108:111]\n\t" \
    "buffer_load_dwordx4 %[bo1], %[srdb], s46 offen offset:1024 lds\n\t" \
    "v_mfma_f32_16x16x32_bf16 v[112:115], a[72:75], a[24:27], v[112:115]\n\t" \
    "buffer_load_dwordx4 %[bo2], %[srdb], s46 offen offset:2048 lds\n\t" \
    "v_mfma_f32_16x16x32_bf16 v[116:119], a[72:75], a[28:31], v[116:119]\n\t" \
    "buffer_load_dwordx4 %[bo3], %[srdb], s46 offen offset:3072 lds\n\t" \
    "v_mfma_f32_16x16x32_bf16 v[120:123], a[76:79], a[0:3], v[120:123]\n\t" \
    "s_add_u32 m0, s48, 4096\n\t" \
    "s_add_u32 s42, s42, %[bstep]\n\t" \
    "buffer_load_dwordx4 %[bo4], %[srdb], s46 offen lds\n\t" \
    "v_mfma_f32_16x16x32_bf16 v[124:127], a[76:79], a[4:7], v[124:127]\n\t" \
    "buffer_load_dwordx4 %[bo5], %[srdb], s46 offen offset:1024 lds\n\t" \
    "v_mfma_f32_16x16x32_bf16 v[128:131], a[76:79], a[8:11], v[128:131]\n\t" \
    "v_mfma_f32_16x16x32_bf16 v[132:135], a[76:79], a[12:15], v[132:135]\n\t" \
    "v_mfma_f32_16x16x32_bf16 v[136:139], a[76:79], a[16:19], v[136:139]\n\t" \
    "v_mfma_f32_16x16x32_bf16 v[140:143], a[76:79], a[20:23], v[140:143]\n\t" \
    "v_mfma_f32_16x16x32_bf16 v[144:147], a[76:79], a[24:27], v[144:147]\n\t" \
    "v_mfma_f32_16x16x32_bf16 v[148:151], a[76:79], a[28:31], v[148:151]\n\t" \
    "v_mfma_f32_16x16x32_bf16 v[152:155], a[80:83], a[0:3], v[152:155]\n\t" \
    "v_mfma_f32_16x16x32_bf16 v[156:159], a[80:83], a[4:7], v[156:159]\n\t" \
    "v_mfma_f32_16x16x32_bf16 v[160:163], a[80:83], a[8:11], v[160:163]\n\t" \
    "v_mfma_f32_16x16x32_bf16 v[164:167], a[80:83], a[12:15], v[164:167]\n\t" \
    "v_mfma_f32_16x16x32_bf16 v[168:171], a[80:83], a[16:19], v[168:171]\n\t" \
    "v_mfma_f32_16x16x32_bf16 v[172:175], a[80:83], a[20:23], v[172:175]\n\t" \
    "v_mfma_f32_16x16x32_bf16 v[176:179], a[80:83], a[24:27], v[176:179]\n\t" \
    "v_mfma_f32_16x16x32_bf16 v[180:183], a[80:83], a[28:31], v[180:183]\n\t" \
    "v_mfma_f32_16x16x32_bf16 v[184:187], a[84:87], a[0:3], v[184:187]\n\t" \
    "v_mfma_f32_16x16x32_bf16 v[188:191], a[84:87], a[4:7], v[188:191]\n\t" \
    "v_mfma_f32_16x16x32_bf16 v[192:195], a[84:87], a[8:11], v[192:195]\n\t" \
    "v_mfma_f32_16x16x32_bf16 v[196:199], a[84:87], a[12:15], v[196:199]\n\t" \
    "v_mfma_f32_16x16x32_bf16 v[200:203], a[84:87], a[16:19], v[200:203]\n\t" \
    "v_mfma_f32_16x16x32_bf16 v[204:207], a[84:87], a[20:23], v[204:207]\n\t" \
    "v_mfma_f32_16x16x32_bf16 v[208:211], a[84:87], a[24:27], v[208:211]\n\t" \
    "v_mfma_f32_16x16x32_bf16 v[212:215], a[84:87], a[28:31], v[212:215]\n\t" \
    "s_waitcnt vmcnt(16) lgkmcnt(0)\n\t" \
    "s_barrier\n\t" \
    "s_add_u32 s43, s43, 16384\n\t" \
    "s_cmp_eq_u32 s43, 49152\n\t" \
    "s_cselect_b32 s43, 0, s43\n\t" \
    "v_add_u32 v232, s43, %[ard]\n\t" \
    "v_mfma_f32_16x16x32_bf16 v[24:27], a[88:91], a[32:35], v[24:27]\n\t" \
    "ds_read_b64_tr_b16 a[64:65], %[brd] offset:12288\n\t" \
    "v_mfma_f32_16x16x32_bf16 v[28:31], a[88:91], a[36:39], v[28:31]\n\t" \
    "ds_read_b64_tr_b16 a[66:67], %[brd] offset:13824\n\t" \
    "v_mfma_f32_16x16x32_bf16 v[32:35], a[88:91], a[40:43], v[32:35]\n\t" \
    "ds_read_b128 a[0:3], v232 offset:0\n\t" \
    "v_mfma_f32_16x16x32_bf16 v[36:39], a[88:91], a[44:47], v[36:39]\n\t" \
    "ds_read_b128 a[4:7], v232 offset:1024\n\t" \
    "v_mfma_f32_16x16x32_bf16 v[40:43], a[88:91], a[48:51], v[40:43]\n\t" \
    "ds_read_b128 a[8:11], v232 offset:2048\n\t" \
    "v_mfma_f32_16x16x32_bf16 v[44:47], a[88:91], a[52:55], v[44:47]\n\t" \
    "ds_read_b128 a[12:15], v232 offset:3072\n\t" \
    "v_mfma_f32_16x16x32_bf16 v[48:51], a[88:91], a[56:59], v[48:51]\n\t" \
    "ds_read_b128 a[16:19], v232 offset:4096\n\t" \
    "v_mfma_f32_16x16x32_bf16 v[52:55], a[88:91], a[60:63], v[52:55]\n\t" \
    "ds_read_b128 a[20:23], v232 offset:5120\n\t" \
    "v_mfma_f32_16x16x32_bf16 v[56:59], a[92:95], a[32:35], v[56:59]\n\t" \
    "ds_read_b128 a[24:27], v232 offset:6144\n\t" \
    "v_mfma_f32_16x16x32_bf16 v[60:63], a[92:95], a[36:39], v[60:63]\n\t" \
    "ds_read_b128 a[28:31], v232 offset:7168\n\t" \
    "v_mfma_f32_16x16x32_bf16 v[64:67], a[92:95], a[40:43], v[64:67]\n\t" \
    "ds_read_b64_tr_b16 a[68:69], %[brd] offset:12544\n\t" \
    "v_mfma_f32_16x16x32_bf16 v[68:71], a[92:95], a[44:47], v[68:71]\n\t" \
    "ds_read_b64_tr_b16 a[70:71], %[brd] offset:14080\n\t" \
    "v_mfma_f32_16x16x32_bf16 v[72:75], a[92:95], a[48:51], v[72:75]\n\t" \
    "ds_read_b64_tr_b16 a[72:73], %[brd] offset:12800\n\t" \
    "v_mfma_f32_16x16x32_bf16 v[76:79], a[92:95], a[52:55], v[76:79]\n\t" \
    "ds_read_b64_tr_b16 a[74:75], %[brd] offset:14336\n\t" \
    "v_mfma_f32_16x16x32_bf16 v[80:83], a[92:95], a[56:59], v[80:83]\n\t" \
    "ds_read_b64_tr_b16 a[76:77], %[brd] offset:13056\n\t" \
    "v_mfma_f32_16x16x32_bf16 v[84:87], a[92:95], a[60:63], v[84:87]\n\t" \
    "ds_read_b64_tr_b16 a[78:79], %[brd] offset:14592\n\t" \
    "v_mfma_f32_16x16x32_bf16 v[88:91], a[96:99], a[32:35], v[88:91]\n\t" \
    "ds_read_b64_tr_b16 a[80:81], %[brd] offset:13312\n\t" \
    "v_mfma_f32_16x16x32_bf16 v[92:95], a[96:99], a[36:39], v[92:95]\n\t" \
    "ds_read_b64_tr_b16 a[82:83], %[brd] offset:14848\n\t" \
    "v_mfma_f32_16x16x32_bf16 v[96:99], a[96:99], a[40:43], v[96:99]\n\t" \
    "ds_read_b64_tr_b16 a[84:85], %[brd] offset:13568\n\t" \
    "v_mfma_f32_16x16x32_bf16 v[100:103], a[96:99], a[44:47], v[100:103]\n\t" \
    "ds_read_b64_tr_b16 a[86:87], %[brd] offset:15104\n\t" \
    "v_mfma_f32_16x16x32_bf16 v[104:107], a[96:99], a[48:51], v[104:107]\n\t" \
    "s_add_u32 m0, s44, %[wq]\n\t" \
    "s_cmp_lt_u32 s41, %[aend]\n\t" \
    "s_cselect_b32 s45, s41, s49\n\t" \
    "buffer_load_dwordx4 %[asrc], %[srda], s45 offen lds\n\t" \
    "v_mfma_f32_16x16x32_bf16 v[108:111], a[96:99], a[52:55], v[108:111]\n\t" \
    "buffer_load_dwordx4 %[asrc], %[srda], s45 offen offset:1024 lds\n\t" \
    "v_mfma_f32_16x16x32_bf16 v[112:115], a[96:99], a[56:59], v[112:115]\n\t" \
    "buffer_load_dwordx4 %[asrc], %[srda], s45 offen offset:2048 lds\n\t" \
    "v_mfma_f32_16x16x32_bf16 v[116:119], a[96:99], a[60:63], v[116:119]\n\t" \
    "buffer_load_dwordx4 %[asrc], %[srda], s45 offen offset:3072 lds\n\t" \
    "s_add_u32 s41, s41, 16384\n\t" \
    "s_add_u32 s44, s44, 16384\n\t" \
    "s_cmp_eq_u32 s44, 49152\n\t" \
    "s_cselect_b32 s44, 0, s44\n\t" \
    "v_mfma_f32_16x16x32_bf16 v[120:123], a[100:103], a[32:35], v[120:123]\n\t" \
    "s_add_u32 m0, s48, 6144\n\t" \
    "s_cmp_lt_u32 s42, %[bend]\n\t" \
    "s_cselect_b32 s46, s42, s49\n\t" \
    "buffer_load_dwordx4 %[bo0], %[srdb], s46 offen lds\n\t" \
    "v_mfma_f32_16x16x32_bf16 v[124:127], a[100:103], a[36:39], v[124:127]\n\t" \
    "buffer_load_dwordx4 %[bo1], %[srdb], s46 offen offset:1024 lds\n\t" \
    "v_mfma_f32_16x16x32_bf16 v[128:131], a[100:103], a[40:43], v[128:131]\n\t" \
    "buffer_load_dwordx4 %[bo2], %[srdb], s46 offen offset:2048 lds\n\t" \
    "v_mfma_f32_16x16x32_bf16 v[132:135], a[100:103], a[44:47], v[132:135]\n\t" \
    "buffer_load_dwordx4 %[bo3], %[srdb], s46 offen offset:3072 lds\n\t" \
    "v_mfma_f32_16x16x32_bf16 v[136:139], a[100:103], a[48:51], v[136:139]\n\t" \
    "s_add_u32 m0, s48, 10240\n\t" \
    "s_add_u32 s42, s42, %[bstep]\n\t" \
    "buffer_load_dwordx4 %[bo4], %[srdb], s46 offen lds\n\t" \
    "v_mfma_f32_16x16x32_bf16 v[140:143], a[100:103], a[52:55], v[140:143]\n\t" \
    "buffer_load_dwordx4 %[bo5], %[srdb], s46 offen offset:1024 lds\n\t" \
    "v_mfma_f32_16x16x32_bf16 v[144:147], a[100:103], a[56:59], v[144:147]\n\t" \
    "v_mfma_f32_16x16x32_bf16 v[148:151], a[100:103], a[60:63], v[148:151]\n\t" \
    "v_mfma_f32_16x16x32_bf16 v[152:155], a[104:107], a[32:35], v[152:155]\n\t" \
    "v_mfma_f32_16x16x32_bf16 v[156:159], a[104:107], a[36:39], v[156:159]\n\t" \
    "v_mfma_f32_16x16x32_bf16 v[160:163], a[104:107], a[40:43], v[160:163]\n\t" \
    "v_mfma_f32_16x16x32_bf16 v[164:167], a[104:107], a[44:47], v[164:167]\n\t" \
    "v_mfma_f32_16x16x32_bf16 v[168:171], a[104:107], a[48:51], v[168:171]\n\t" \
    "v_mfma_f32_16x16x32_bf16 v[172:175], a[104:107], a[52:55], v[172:175]\n\t" \
    "v_mfma_f32_16x16x32_bf16 v[176:179], a[104:107], a[56:59], v[176:179]\n\t" \
    "v_mfma_f32_16x16x32_bf16 v[180:183], a[104:107], a[60:63], v[180:183]\n\t" \
    "v_mfma_f32_16x16x32_bf16 v[184:187], a[108:111], a[32:35], v[184:187]\n\t" \
    "v_mfma_f32_16x16x32_bf16 v[188:191], a[108:111], a[36:39], v[188:191]\n\t" \
    "v_mfma_f32_16x16x32_bf16 v[192:195], a[108:111], a[40:43], v[192:195]\n\t" \
    "v_mfma_f32_16x16x32_bf16 v[196:199], a[108:111], a[44:47], v[196:199]\n\t" \
    "v_mfma_f32_16x16x32_bf16 v[200:203], a[108:111], a[48:51], v[200:203]\n\t" \
    "v_mfma_f32_16x16x32_bf16 v[204:207], a[108:111], a[52:55], v[204:207]\n\t" \
    "v_mfma_f32_16x16x32_bf16 v[208:211], a[108:111], a[56:59], v[208:211]\n\t" \
    "v_mfma_f32_16x16x32_bf16 v[212:215], a[108:111], a[60:63], v[212:215]\n\t" \
    "s_waitcnt vmcnt(16) lgkmcnt(0)\n\t" \
    "v_mfma_f32_16x16x32_bf16 v[24:27], a[64:67], a[0:3], v[24:27]\n\t" \
    "ds_read_b64_tr_b16 a[88:89], %[brd] offset:18432\n\t" \
    "v_mfma_f32_16x16x32_bf16 v[28:31], a[64:67], a[4:7], v[28:31]\n\t" \
    "ds_read_b64_tr_b16 a[90:91], %[brd] offset:19968\n\t" \
    "v_mfma_f32_16x16x32_bf16 v[32:35], a[64:67], a[8:11], v[32:35]\n\t" \
    "ds_read_b128 a[32:35], v232 offset:8192\n\t" \
    "v_mfma_f32_16x16x32_bf16 v[36:39], a[64:67], a[12:15], v[36:39]\n\t" \
    "ds_read_b128 a[36:39], v232 offset:9216\n\t" \
    "v_mfma_f32_16x16x32_bf16 v[40:43], a[64:67], a[16:19], v[40:43]\n\t" \
    "ds_read_b128 a[40:43], v232 offset:10240\n\t" \
    "v_mfma_f32_16x16x32_bf16 v[44:47], a[64:67], a[20:23], v[44:47]\n\t" \
    "ds_read_b128 a[44:47], v232 offset:11264\n\t" \
    "v_mfma_f32_16x16x32_bf16 v[48:51], a[64:67], a[24:27], v[48:51]\n\t" \
    "ds_read_b128 a[48:51], v232 offset:12288\n\t" \
    "v_mfma_f32_16x16x32_bf16 v[52:55], a[64:67], a[28:31], v[52:55]\n\t" \
    "ds_read_b128 a[52:55], v232 offset:13312\n\t" \
    "v_mfma_f32_16x16x32_bf16 v[56:59], a[68:71], a[0:3], v[56:59]\n\t" \
    "ds_read_b128 a[56:59], v232 offset:14336\n\t" \
    "v_mfma_f32_16x16x32_bf16 v[60:63], a[68:71], a[4:7], v[60:63]\n\t" \
    "ds_read_b128 a[60:63], v232 offset:15360\n\t" \
    "v_mfma_f32_16x16x32_bf16 v[64:67], a[68:71], a[8:11], v[64:67]\n\t" \
    "ds_read_b64_tr_b16 a[92:93], %[brd] offset:18688\n\t" \
    "v_mfma_f32_16x16x32_bf16 v[68:71], a[68:71], a[12:15], v[68:71]\n\t" \
    "ds_read_b64_tr_b16 a[94:95], %[brd] offset:20224\n\t" \
    "v_mfma_f32_16x16x32_bf16 v[72:75], a[68:71], a[16:19], v[72:75]\n\t" \
    "ds_read_b64_tr_b16 a[96:97], %[brd] offset:18944\n\t" \
    "v_mfma_f32_16x16x32_bf16 v[76:79], a[68:71], a[20:23], v[76:79]\n\t" \
    "ds_read_b64_tr_b16 a[98:99], %[brd] offset:20480\n\t" \
    "v_mfma_f32_16x16x32_bf16 v[80:83], a[68:71], a[24:27], v[80:83]\n\t" \
    "ds_read_b64_tr_b16 a[100:101], %[brd] offset:19200\n\t" \
    "v_mfma_f32_16x16x32_bf16 v[84:87], a[68:71], a[28:31], v[84:87]\n\t" \
    "ds_read_b64_tr_b16 a[102:103], %[brd] offset:20736\n\t" \
    "v_mfma_f32_16x16x32_bf16 v[88:91], a[72:75], a[0:3], v[88:91]\n\t" \
    "ds_read_b64_tr_b16 a[104:105], %[brd] offset:19456\n\t" \
    "v_mfma_f32_16x16x32_bf16 v[92:95], a[72:75], a[4:7], v[92:95]\n\t" \
    "ds_read_b64_tr_b16 a[106:107], %[brd] offset:20992\n\t" \
    "v_mfma_f32_16x16x32_bf16 v[96:99], a[72:75], a[8:11], v[96:99]\n\t" \
    "ds_read_b64_tr_b16 a[108:109], %[brd] offset:19712\n\t" \
    "v_mfma_f32_16x16x32_bf16 v[100:103], a[72:75], a[12:15], v[100:103]\n\t" \
    "ds_read_b64_tr_b16 a[110:111], %[brd] offset:21248\n\t" \
    "v_mfma_f32_16x16x32_bf16 v[104:107], a[72:75], a[16:19], v[104:107]\n\t" \
    "s_add_u32 m0, s48, 12288\n\t" \
    "s_cmp_lt_u32 s42, %[bend]\n\t" \
    "s_cselect_b32 s46, s42, s49\n\t" \
    "buffer_load_dwordx4 %[bo0], %[srdb], s46 offen lds\n\t" \
    "v_mfma_f32_16x16x32_bf16 v[108:111], a[72:75], a[20:23], v[108:111]\n\t" \
    "buffer_load_dwordx4 %[bo1], %[srdb], s46 offen offset:1024 lds\n\t" \
    "v_mfma_f32_16x16x32_bf16 v[112:115], a[72:75], a[24:27], v[112:115]\n\t" \
    "buffer_load_dwordx4 %[bo2], %[srdb], s46 offen offset:2048 lds\n\t" \
    "v_mfma_f32_16x16x32_bf16 v[116:119], a[72:75], a[28:31], v[116:119]\n\t" \
    "buffer_load_dwordx4 %[bo3], %[srdb], s46 offen offset:3072 lds\n\t" \
    "v_mfma_f32_16x16x32_bf16 v[120:123], a[76:79], a[0:3], v[120:123]\n\t" \
    "s_add_u32 m0, s48, 16384\n\t" \
    "s_add_u32 s42, s42, %[bstep]\n\t" \
    "buffer_load_dwordx4 %[bo4], %[srdb], s46 offen lds\n\t" \
    "v_mfma_f32_16x16x32_bf16 v[124:127], a[76:79], a[4:7], v[124:127]\n\t" \
    "buffer_load_dwordx4 %[bo5], %[srdb], s46 offen offset:1024 lds\n\t" \
    "v_mfma_f32_16x16x32_bf16 v[128:131], a[76:79], a[8:11], v[128:131]\n\t" \
    "v_mfma_f32_16x16x32_bf16 v[132:135], a[76:79], a[12:15], v[132:135]\n\t" \
    "v_mfma_f32_16x16x32_bf16 v[136:139], a[76:79], a[16:19], v[136:139]\n\t" \
    "v_mfma_f32_16x16x32_bf16 v[140:143], a[76:79], a[20:23], v[140:143]\n\t" \
    "v_mfma_f32_16x16x32_bf16 v[144:147], a[76:79], a[24:27], v[144:147]\n\t" \
    "v_mfma_f32_16x16x32_bf16 v[148:151], a[76:79], a[28:31], v[148:151]\n\t" \
    "v_mfma_f32_16x16x32_bf16 v[152:155], a[80:83], a[0:3], v[152:155]\n\t" \
    "v_mfma_f32_16x16x32_bf16 v[156:159], a[80:83], a[4:7], v[156:159]\n\t" \
    "v_mfma_f32_16x16x32_bf16 v[160:163], a[80:83], a[8:11], v[160:163]\n\t" \
    "v_mfma_f32_16x16x32_bf16 v[164:167], a[80:83], a[12:15], v[164:167]\n\t" \
    "v_mfma_f32_16x16x32_bf16 v[168:171], a[80:83], a[16:19], v[168:171]\n\t" \
    "v_mfma_f32_16x16x32_bf16 v[172:175], a[80:83], a[20:23], v[172:175]\n\t" \
    "v_mfma_f32_16x16x32_bf16 v[176:179], a[80:83], a[24:27], v[176:179]\n\t" \
    "v_mfma_f32_16x16x32_bf16 v[180:183], a[80:83], a[28:31], v[180:183]\n\t" \
    "v_mfma_f32_16x16x32_bf16 v[184:187], a[84:87], a[0:3], v[184:187]\n\t" \
    "v_mfma_f32_16x16x32_bf16 v[188:191], a[84:87], a[4:7], v[188:191]\n\t" \
    "v_mfma_f32_16x16x32_bf16 v[192:195], a[84:87], a[8:11], v[192:195]\n\t" \
    "v_mfma_f32_16x16x32_bf16 v[196:199], a[84:87], a[12:15], v[196:199]\n\t" \
    "v_mfma_f32_16x16x32_bf16 v[200:203], a[84:87], a[16:19], v[200:203]\n\t" \
    "v_mfma_f32_16x16x32_bf16 v[204:207], a[84:87], a[20:23], v[204:207]\n\t" \
    "v_mfma_f32_16x16x32_bf16 v[208:211], a[84:87], a[24:27], v[208:211]\n\t" \
    "v_mfma_f32_16x16x32_bf16 v[212:215], a[84:87], a[28:31], v[212:215]\n\t" \
    "s_waitcnt vmcnt(16) lgkmcnt(0)\n\t" \
    "s_barrier\n\t" \
    "s_add_u32 s43, s43, 16384\n\t" \
    "s_cmp_eq_u32 s43, 49152\n\t" \
    "s_cselect_b32 s43, 0, s43\n\t" \
    "v_add_u32 v232, s43, %[ard]\n\t" \
    "v_mfma_f32_16x16x32_bf16 v[24:27], a[88:91], a[32:35], v[24:27]\n\t" \
    "ds_read_b64_tr_b16 a[64:65], %[brd] offset:0\n\t" \
    "v_mfma_f32_16x16x32_bf16 v[28:31], a[88:91], a[36:39], v[28:31]\n\t" \
    "ds_read_b64_tr_b16 a[66:67], %[brd] offset:1536\n\t" \
    "v_mfma_f32_16x16x32_bf16 v[32:35], a[88:91], a[40:43], v[32:35]\n\t" \
    "ds_read_b128 a[0:3], v232 offset:0\n\t" \
    "v_mfma_f32_16x16x32_bf16 v[36:39], a[88:91], a[44:47], v[36:39]\n\t" \
    "ds_read_b128 a[4:7], v232 offset:1024\n\t" \
    "v_mfma_f32_16x16x32_bf16 v[40:43], a[88:91], a[48:51], v[40:43]\n\t" \
    "ds_read_b128 a[8:11], v232 offset:2048\n\t" \
    "v_mfma_f32_16x16x32_bf16 v[44:47], a[88:91], a[52:55], v[44:47]\n\t" \
    "ds_read_b128 a[12:15], v232 offset:3072\n\t" \
    "v_mfma_f32_16x16x32_bf16 v[48:51], a[88:91], a[56:59], v[48:51]\n\t" \
    "ds_read_b128 a[16:19], v232 offset:4096\n\t" \
    "v_mfma_f32_16x16x32_bf16 v[52:55], a[88:91], a[60:63], v[52:55]\n\t" \
    "ds_read_b128 a[20:23], v232 offset:5120\n\t" \
    "v_mfma_f32_16x16x32_bf16 v[56:59], a[92:95], a[32:35], v[56:59]\n\t" \
    "ds_read_b128 a[24:27], v232 offset:6144\n\t" \
    "v_mfma_f32_16x16x32_bf16 v[60:63], a[92:95], a[36:39], v[60:63]\n\t" \
    "ds_read_b128 a[28:31], v232 offset:7168\n\t" \
    "v_mfma_f32_16x16x32_bf16 v[64:67], a[92:95], a[40:43], v[64:67]\n\t" \
    "ds_read_b64_tr_b16 a[68:69], %[brd] offset:256\n\t" \
    "v_mfma_f32_16x16x32_bf16 v[68:71], a[92:95], a[44:47], v[68:71]\n\t" \
    "ds_read_b64_tr_b16 a[70:71], %[brd] offset:1792\n\t" \
    "v_mfma_f32_16x16x32_bf16 v[72:75], a[92:95], a[48:51], v[72:75]\n\t" \
    "ds_read_b64_tr_b16 a[72:73], %[brd] offset:512\n\t" \
    "v_mfma_f32_16x16x32_bf16 v[76:79], a[92:95], a[52:55], v[76:79]\n\t" \
    "ds_read_b64_tr_b16 a[74:75], %[brd] offset:2048\n\t" \
    "v_mfma_f32_16x16x32_bf16 v[80:83], a[92:95], a[56:59], v[80:83]\n\t" \
    "ds_read_b64_tr_b16 a[76:77], %[brd] offset:768\n\t" \
    "v_mfma_f32_16x16x32_bf16 v[84:87], a[92:95], a[60:63], v[84:87]\n\t" \
    "ds_read_b64_tr_b16 a[78:79], %[brd] offset:2304\n\t" \
    "v_mfma_f32_16x16x32_bf16 v[88:91], a[96:99], a[32:35], v[88:91]\n\t" \
    "ds_read_b64_tr_b16 a[80:81], %[brd] offset:1024\n\t" \
    "v_mfma_f32_16x16x32_bf16 v[92:95], a[96:99], a[36:39], v[92:95]\n\t" \
    "ds_read_b64_tr_b16 a[82:83], %[brd] offset:2560\n\t" \
    "v_mfma_f32_16x16x32_bf16 v[96:99], a[96:99], a[40:43], v[96:99]\n\t" \
    "ds_read_b64_tr_b16 a[84:85], %[brd] offset:1280\n\t" \
    "v_mfma_f32_16x16x32_bf16 v[100:103], a[96:99], a[44:47], v[100:103]\n\t" \
    "ds_read_b64_tr_b16 a[86:87], %[brd] offset:2816\n\t" \
    "v_mfma_f32_16x16x32_bf16 v[104:107], a[96:99], a[48:51], v[104:107]\n\t" \
    "s_add_u32 m0, s44, %[wq]\n\t" \
    "s_cmp_lt_u32 s41, %[aend]\n\t" \
    "s_cselect_b32 s45, s41, s49\n\t" \
    "buffer_load_dwordx4 %[asrc], %[srda], s45 offen lds\n\t" \
    "v_mfma_f32_16x16x32_bf16 v[108:111], a[96:99], a[52:55], v[108:111]\n\t" \
    "buffer_load_dwordx4 %[asrc], %[srda], s45 offen offset:1024 lds\n\t" \
    "v_mfma_f32_16x16x32_bf16 v[112:115], a[96:99], a[56:59], v[112:115]\n\t" \
    "buffer_load_dwordx4 %[asrc], %[srda], s45 offen offset:2048 lds\n\t" \
    "v_mfma_f32_16x16x32_bf16 v[116:119], a[96:99], a[60:63], v[116:119]\n\t" \
    "buffer_load_dwordx4 %[asrc], %[srda], s45 offen offset:3072 lds\n\t" \
    "s_add_u32 s41, s41, 16384\n\t" \
    "s_add_u32 s44, s44, 16384\n\t" \
    "s_cmp_eq_u32 s44, 49152\n\t" \
    "s_cselect_b32 s44, 0, s44\n\t" \
    "v_mfma_f32_16x16x32_bf16 v[120:123], a[100:103], a[32:35], v[120:123]\n\t" \
    "s_add_u32 m0, s48, 18432\n\t" \
    "s_cmp_lt_u32 s42, %[bend]\n\t" \
    "s_cselect_b32 s46, s42, s49\n\t" \
    "buffer_load_dwordx4 %[bo0], %[srdb], s46 offen lds\n\t" \
    "v_mfma_f32_16x16x32_bf16 v[124:127], a[100:103], a[36:39], v[124:127]\n\t" \
    "buffer_load_dwordx4 %[bo1], %[srdb], s46 offen offset:1024 lds\n\t" \
    "v_mfma_f32_16x16x32_bf16 v[128:131], a[100:103], a[40:43], v[128:131]\n\t" \
    "buffer_load_dwordx4 %[bo2], %[srdb], s46 offen offset:2048 lds\n\t" \
    "v_mfma_f32_16x16x32_bf16 v[132:135], a[100:103], a[44:47], v[132:135]\n\t" \
    "buffer_load_dwordx4 %[bo3], %[srdb], s46 offen offset:3072 lds\n\t" \
    "v_mfma_f32_16x16x32_bf16 v[136:139], a[100:103], a[48:51], v[136:139]\n\t" \
    "s_add_u32 m0, s48, 22528\n\t" \
    "s_add_u32 s42, s42, %[bstep]\n\t" \
    "buffer_load_dwordx4 %[bo4], %[srdb], s46 offen lds\n\t" \
    "v_mfma_f32_16x16x32_bf16 v[140:143], a[100:103], a[52:55], v[140:143]\n\t" \
    "buffer_load_dwordx4 %[bo5], %[srdb], s46 offen offset:1024 lds\n\t" \
    "v_mfma_f32_16x16x32_bf16 v[144:147], a[100:103], a[56:59], v[144:147]\n\t" \
    "v_mfma_f32_16x16x32_bf16 v[148:151], a[100:103], a[60:63], v[148:151]\n\t" \
    "v_mfma_f32_16x16x32_bf16 v[152:155], a[104:107], a[32:35], v[152:155]\n\t" \
    "v_mfma_f32_16x16x32_bf16 v[156:159], a[104:107], a[36:39], v[156:159]\n\t" \
    "v_mfma_f32_16x16x32_bf16 v[160:163], a[104:107], a[40:43], v[160:163]\n\t" \
    "v_mfma_f32_16x16x32_bf16 v[164:167], a[104:107], a[44:47], v[164:167]\n\t" \
    "v_mfma_f32_16x16x32_bf16 v[168:171], a[104:107], a[48:51], v[168:171]\n\t" \
    "v_mfma_f32_16x16x32_bf16 v[172:175], a[104:107], a[52:55], v[172:175]\n\t" \
    "v_mfma_f32_16x16x32_bf16 v[176:179], a[104:107], a[56:59], v[176:179]\n\t" \
    "v_mfma_f32_16x16x32_bf16 v[180:183], a[104:107], a[60:63], v[180:183]\n\t" \
    "v_mfma_f32_16x16x32_bf16 v[184:187], a[108:111], a[32:35], v[184:187]\n\t" \
    "v_mfma_f32_16x16x32_bf16 v[188:191], a[108:111], a[36:39], v[188:191]\n\t" \
    "v_mfma_f32_16x16x32_bf16 v[192:195], a[108:111], a[40:43], v[192:195]\n\t" \
    "v_mfma_f32_16x16x32_bf16 v[196:199], a[108:111], a[44:47], v[196:199]\n\t" \
    "v_mfma_f32_16x16x32_bf16 v[200:203], a[108:111], a[48:51], v[200:203]\n\t" \
    "v_mfma_f32_16x16x32_bf16 v[204:207], a[108:111], a[52:55], v[204:207]\n\t" \
    "v_mfma_f32_16x16x32_bf16 v[208:211], a[108:111], a[56:59], v[208:211]\n\t" \
    "v_mfma_f32_16x16x32_bf16 v[212:215], a[108:111], a[60:63], v[212:215]\n\t" \
    "s_add_u32 s40, s40, 4\n\t" \
    "s_cmp_lt_u32 s40, %[nk]\n\t" \
    "s_cbranch_scc1 tl_1_%=\n\t" \
    "s_waitcnt vmcnt(0) lgkmcnt(0)\n\t" \
    "s_barrier\n\t" \
    "s_cmp_lg_u32 %[tolds], 0\n\t" \
    "s_cbranch_scc1 el_2_%=\n\t" \
    "v_cvt_pk_bf16_f32 v236, v24, v25\n\t" \
    "v_cvt_pk_bf16_f32 v237, v26, v27\n\t" \
    "v_cvt_pk_bf16_f32 v238, v56, v57\n\t" \
    "v_cvt_pk_bf16_f32 v239, v58, v59\n\t" \
    "s_mul_i32 s45, %[os1], 0\n\t" \
    "s_add_u32 s45, s45, %[ooff]\n\t" \
    "v_permlane32_swap_b32 v236, v238\n\t" \
    "v_permlane32_swap_b32 v237, v239\n\t" \
    "s_nop 1\n\t" \
    "v_permlane16_swap_b32 v236, v238\n\t" \
    "v_permlane16_swap_b32 v237, v239\n\t" \
    "buffer_store_dwordx4 v[236:239], %[ov], %[srda], s45 offen sc1\n\t" \
    "v_cvt_pk_bf16_f32 v240, v88, v89\n\t" \
    "v_cvt_pk_bf16_f32 v241, v90, v91\n\t" \
    "v_cvt_pk_bf16_f32 v242, v120, v121\n\t" \
    "v_cvt_pk_bf16_f32 v243, v122, v123\n\t" \
    "s_add_u32 s45, s45, %[os3]\n\t" \
    "s_nop 0\n\t" \
    "v_permlane32_swap_b32 v240, v242\n\t" \
    "v_permlane32_swap_b32 v241, v243\n\t" \
    "s_nop 1\n\t" \
    "v_permlane16_swap_b32 v240, v242\n\t" \
    "v_permlane16_swap_b32 v241, v243\n\t" \
    "buffer_store_dwordx4 v[240:243], %[ov], %[srda], s45 offen sc1\n\t" \
    "v_cvt_pk_bf16_f32 v244, v152, v153\n\t" \
    "v_cvt_pk_bf16_f32 v245, v154, v155\n\t" \
    "v_cvt_pk_bf16_f32 v246, v184, v185\n\t" \
    "v_cvt_pk_bf16_f32 v247, v186, v187\n\t" \
    "s_add_u32 s45, s45, %[os3]\n\t" \
    "s_nop 0\n\t" \
    "v_permlane32_swap_b32 v244, v246\n\t" \
    "v_permlane32_swap_b32 v245, v247\n\t" \
    "s_nop 1\n\t" \
    "v_permlane16_swap_b32 v244, v246\n\t" \
    "v_permlane16_swap_b32 v245, v247\n\t" \
    "buffer_store_dwordx4 v[244:247], %[ov], %[srda], s45 offen sc1\n\t" \
    "v_cvt_pk_bf16_f32 v248, v28, v29\n\t" \
    "v_cvt_pk_bf16_f32 v249, v30, v31\n\t" \
    "v_cvt_pk_bf16_f32 v250, v60, v61\n\t" \
    "v_cvt_pk_bf16_f32 v251, v62, v63\n\t" \
    "s_mul_i32 s45, %[os1], 0\n\t" \
    "s_add_u32 s45, s45, %[ooff]\n\t" \
    "s_add_u32 s45, s45, %[os2]\n\t" \
    "v_permlane32_swap_b32 v248, v250\n\t" \
    "v_permlane32_swap_b32 v249, v251\n\t" \
    "s_nop 1\n\t" \
    "v_permlane16_swap_b32 v248, v250\n\t" \
    "v_permlane16_swap_b32 v249, v251\n\t" \
    "buffer_store_dwordx4 v[248:251], %[ov], %[srda], s45 offen sc1\n\t" \
    "v_cvt_pk_bf16_f32 v236, v92, v93\n\t" \
    "v_cvt_pk_bf16_f32 v237, v94, v95\n\t" \
    "v_cvt_pk_bf16_f32 v238, v124, v125\n\t" \
    "v_cvt_pk_bf16_f32 v239, v126, v127\n\t" \
    "s_add_u32 s45, s45, %[os3]\n\t" \
    "s_nop 0\n\t" \
    "v_permlane32_swap_b32 v236, v238\n\t" \
    "v_permlane32_swap_b32 v237, v239\n\t" \
    "s_nop 1\n\t" \
    "v_permlane16_swap_b32 v236, v238\n\t" \
    "v_permlane16_swap_b32 v237, v239\n\t" \
    "buffer_store_dwordx4 v[236:239], %[ov], %[srda], s45 offen sc1\n\t" \
    "v_cvt_pk_bf16_f32 v240, v156, v157\n\t" \
    "v_cvt_pk_bf16_f32 v241, v158, v159\n\t" \
    "v_cvt_pk_bf16_f32 v242, v188, v189\n\t" \
    "v_cvt_pk_bf16_f32 v243, v190, v191\n\t" \
    "s_add_u32 s45, s45, %[os3]\n\t" \
    "s_nop 0\n\t" \
    "v_permlane32_swap_b32 v240, v242\n\t" \
    "v_permlane32_swap_b32 v241, v243\n\t" \
    "s_nop 1\n\t" \
    "v_permlane16_swap_b32 v240, v242\n\t" \
    "v_permlane16_swap_b32 v241, v243\n\t" \
    "buffer_store_dwordx4 v[240:243], %[ov], %[srda], s45 offen sc1\n\t" \
    "v_cvt_pk_bf16_f32 v244, v32, v33\n\t" \
    "v_cvt_pk_bf16_f32 v245, v34, v35\n\t" \
    "v_cvt_pk_bf16_f32 v246, v64, v65\n\t" \
    "v_cvt_pk_bf16_f32 v247, v66, v67\n\t" \
    "s_mul_i32 s45, %[os1], 1\n\t" \
    "s_add_u32 s45, s45, %[ooff]\n\t" \
    "v_permlane32_swap_b32 v244, v246\n\t" \
    "v_permlane32_swap_b32 v245, v247\n\t" \
    "s_nop 1\n\t" \
    "v_permlane16_swap_b32 v244, v246\n\t" \
    "v_permlane16_swap_b32 v245, v247\n\t" \
    "buffer_store_dwordx4 v[244:247], %[ov], %[srda], s45 offen sc1\n\t" \
    "v_cvt_pk_bf16_f32 v248, v96, v97\n\t" \
    "v_cvt_pk_bf16_f32 v249, v98, v99\n\t" \
    "v_cvt_pk_bf16_f32 v250, v128, v129\n\t" \
    "v_cvt_pk_bf16_f32 v251, v130, v131\n\t" \
    "s_add_u32 s45, s45, %[os3]\n\t" \
    "s_nop 0\n\t" \
    "v_permlane32_swap_b32 v248, v250\n\t" \
    "v_permlane32_swap_b32 v249, v251\n\t" \
    "s_nop 1\n\t" \
    "v_permlane16_swap_b32 v248, v250\n\t" \
    "v_permlane16_swap_b32 v249, v251\n\t" \
    "buffer_store_dwordx4 v[248:251], %[ov], %[srda], s45 offen sc1\n\t" \
    "v_cvt_pk_bf16_f32 v236, v160, v161\n\t" \
    "v_cvt_pk_bf16_f32 v237, v162, v163\n\t" \
    "v_cvt_pk_bf16_f32 v238, v192, v193\n\t" \
    "v_cvt_pk_bf16_f32 v239, v194, v195\n\t" \
    "s_add_u32 s45, s45, %[os3]\n\t" \
    "s_nop 0\n\t" \
    "v_permlane32_swap_b32 v236, v238\n\t" \
    "v_permlane32_swap_b32 v237, v239\n\t" \
    "s_nop 1\n\t" \
    "v_permlane16_swap_b32 v236, v238\n\t" \
    "v_permlane16_swap_b32 v237, v239\n\t" \
    "buffer_store_dwordx4 v[236:239], %[ov], %[srda], s45 offen sc1\n\t" \
    "v_cvt_pk_bf16_f32 v240, v36, v37\n\t" \
    "v_cvt_pk_bf16_f32 v241, v38, v39\n\t" \
    "v_cvt_pk_bf16_f32 v242, v68, v69\n\t" \
    "v_cvt_pk_bf16_f32 v243, v70, v71\n\t" \
    "s_mul_i32 s45, %[os1], 1\n\t" \
    "s_add_u32 s45, s45, %[ooff]\n\t" \
    "s_add_u32 s45, s45, %[os2]\n\t" \
    "v_permlane32_swap_b32 v240, v242\n\t" \
    "v_permlane32_swap_b32 v241, v243\n\t" \
    "s_nop 1\n\t" \
    "v_permlane16_swap_b32 v240, v242\n\t" \
    "v_permlane16_swap_b32 v241, v243\n\t" \
    "buffer_store_dwordx4 v[240:243], %[ov], %[srda], s45 offen sc1\n\t" \
    "v_cvt_pk_bf16_f32 v244, v100, v101\n\t" \
    "v_cvt_pk_bf16_f32 v245, v102, v103\n\t" \
    "v_cvt_pk_bf16_f32 v246, v132, v133\n\t" \
    "v_cvt_pk_bf16_f32 v247, v134, v135\n\t" \
    "s_add_u32 s45, s45, %[os3]\n\t" \
    "s_nop 0\n\t" \
    "v_permlane32_swap_b32 v244, v246\n\t" \
    "v_permlane32_swap_b32 v245, v247\n\t" \
    "s_nop 1\n\t" \
    "v_permlane16_swap_b32 v244, v246\n\t" \
    "v_permlane16_swap_b32 v245, v247\n\t" \
    "buffer_store_dwordx4 v[244:247], %[ov], %[srda], s45 offen sc1\n\t" \
    "v_cvt_pk_bf16_f32 v248, v164, v165\n\t" \
    "v_cvt_pk_bf16_f32 v249, v166, v167\n\t" \
    "v_cvt_pk_bf16_f32 v250, v196, v197\n\t" \
    "v_cvt_pk_bf16_f32 v251, v198, v199\n\t" \
    "s_add_u32 s45, s45, %[os3]\n\t" \
    "s_nop 0\n\t" \
    "v_permlane32_swap_b32 v248, v250\n\t" \
    "v_permlane32_swap_b32 v249, v251\n\t" \
    "s_nop 1\n\t" \
    "v_permlane16_swap_b32 v248, v250\n\t" \
    "v_permlane16_swap_b32 v249, v251\n\t" \
    "buffer_store_dwordx4 v[248:251], %[ov], %[srda], s45 offen sc1\n\t" \
    "v_cvt_pk_bf16_f32 v236, v40, v41\n\t" \
    "v_cvt_pk_bf16_f32 v237, v42, v43\n\t" \
    "v_cvt_pk_bf16_f32 v238, v72, v73\n\t" \
    "v_cvt_pk_bf16_f32 v239, v74, v75\n\t" \
    "s_mul_i32 s45, %[os1], 2\n\t" \
    "s_add_u32 s45, s45, %[ooff]\n\t" \
    "v_permlane32_swap_b32 v236, v238\n\t" \
    "v_permlane32_swap_b32 v237, v239\n\t" \
    "s_nop 1\n\t" \
    "v_permlane16_swap_b32 v236, v238\n\t" \
    "v_permlane16_swap_b32 v237, v239\n\t" \
    "buffer_store_dwordx4 v[236:239], %[ov], %[srda], s45 offen sc1\n\t" \
    "v_cvt_pk_bf16_f32 v240, v104, v105\n\t" \
    "v_cvt_pk_bf16_f32 v241, v106, v107\n\t" \
    "v_cvt_pk_bf16_f32 v242, v136, v137\n\t" \
    "v_cvt_pk_bf16_f32 v243, v138, v139\n\t" \
    "s_add_u32 s45, s45, %[os3]\n\t" \
    "s_nop 0\n\t" \
    "v_permlane32_swap_b32 v240, v242\n\t" \
    "v_permlane32_swap_b32 v241, v243\n\t" \
    "s_nop 1\n\t" \
    "v_permlane16_swap_b32 v240, v242\n\t" \
    "v_permlane16_swap_b32 v241, v243\n\t" \
    "buffer_store_dwordx4 v[240:243], %[ov], %[srda], s45 offen sc1\n\t" \
    "v_cvt_pk_bf16_f32 v244, v168, v169\n\t" \
    "v_cvt_pk_bf16_f32 v245, v170, v171\n\t" \
    "v_cvt_pk_bf16_f32 v246, v200, v201\n\t" \
    "v_cvt_pk_bf16_f32 v247, v202, v203\n\t" \
    "s_add_u32 s45, s45, %[os3]\n\t" \
    "s_nop 0\n\t" \
    "v_permlane32_swap_b32 v244, v246\n\t" \
    "v_permlane32_swap_b32 v245, v247\n\t" \
    "s_nop 1\n\t" \
    "v_permlane16_swap_b32 v244, v246\n\t" \
    "v_permlane16_swap_b32 v245, v247\n\t" \
    "buffer_store_dwordx4 v[244:247], %[ov], %[srda], s45 offen sc1\n\t" \
    "v_cvt_pk_bf16_f32 v248, v44, v45\n\t" \
    "v_cvt_pk_bf16_f32 v249, v46, v47\n\t" \
    "v_cvt_pk_bf16_f32 v250, v76, v77\n\t" \
    "v_cvt_pk_bf16_f32 v251, v78, v79\n\t" \
    "s_mul_i32 s45, %[os1], 2\n\t" \
    "s_add_u32 s45, s45, %[ooff]\n\t" \
    "s_add_u32 s45, s45, %[os2]\n\t" \
    "v_permlane32_swap_b32 v248, v250\n\t" \
    "v_permlane32_swap_b32 v249, v251\n\t" \
    "s_nop 1\n\t" \
    "v_permlane16_swap_b32 v248, v250\n\t" \
    "v_permlane16_swap_b32 v249, v251\n\t" \
    "buffer_store_dwordx4 v[248:251], %[ov], %[srda], s45 offen sc1\n\t" \
    "v_cvt_pk_bf16_f32 v236, v108, v109\n\t" \
    "v_cvt_pk_bf16_f32 v237, v110, v111\n\t" \
    "v_cvt_pk_bf16_f32 v238, v140, v141\n\t" \
    "v_cvt_pk_bf16_f32 v239, v142, v143\n\t" \
    "s_add_u32 s45, s45, %[os3]\n\t" \
    "s_nop 0\n\t" \
    "v_permlane32_swap_b32 v236, v238\n\t" \
    "v_permlane32_swap_b32 v237, v239\n\t" \
    "s_nop 1\n\t" \
    "v_permlane16_swap_b32 v236, v238\n\t" \
    "v_permlane16_swap_b32 v237, v239\n\t" \
    "buffer_store_dwordx4 v[236:239], %[ov], %[srda], s45 offen sc1\n\t" \
    "v_cvt_pk_bf16_f32 v240, v172, v173\n\t" \
    "v_cvt_pk_bf16_f32 v241, v174, v175\n\t" \
    "v_cvt_pk_bf16_f32 v242, v204, v205\n\t" \
    "v_cvt_pk_bf16_f32 v243, v206, v207\n\t" \
    "s_add_u32 s45, s45, %[os3]\n\t" \
    "s_nop 0\n\t" \
    "v_permlane32_swap_b32 v240, v242\n\t" \
    "v_permlane32_swap_b32 v241, v243\n\t" \
    "s_nop 1\n\t" \
    "v_permlane16_swap_b32 v240, v242\n\t" \
    "v_permlane16_swap_b32 v241, v243\n\t" \
    "buffer_store_dwordx4 v[240:243], %[ov], %[srda], s45 offen sc1\n\t" \
    "v_cvt_pk_bf16_f32 v244, v48, v49\n\t" \
    "v_cvt_pk_bf16_f32 v245, v50, v51\n\t" \
    "v_cvt_pk_bf16_f32 v246, v80, v81\n\t" \
    "v_cvt_pk_bf16_f32 v247, v82, v83\n\t" \
    "s_mul_i32 s45, %[os1], 3\n\t" \
    "s_add_u32 s45, s45, %[ooff]\n\t" \
    "v_permlane32_swap_b32 v244, v246\n\t" \
    "v_permlane32_swap_b32 v245, v247\n\t" \
    "s_nop 1\n\t" \
    "v_permlane16_swap_b32 v244, v246\n\t" \
    "v_permlane16_swap_b32 v245, v247\n\t" \
    "buffer_store_dwordx4 v[244:247], %[ov], %[srda], s45 offen sc1\n\t" \
    "v_cvt_pk_bf16_f32 v248, v112, v113\n\t" \
    "v_cvt_pk_bf16_f32 v249, v114, v115\n\t" \
    "v_cvt_pk_bf16_f32 v250, v144, v145\n\t" \
    "v_cvt_pk_bf16_f32 v251, v146, v147\n\t" \
    "s_add_u32 s45, s45, %[os3]\n\t" \
    "s_nop 0\n\t" \
    "v_permlane32_swap_b32 v248, v250\n\t" \
    "v_permlane32_swap_b32 v249, v251\n\t" \
    "s_nop 1\n\t" \
    "v_permlane16_swap_b32 v248, v250\n\t" \
    "v_permlane16_swap_b32 v249, v251\n\t" \
    "buffer_store_dwordx4 v[248:251], %[ov], %[srda], s45 offen sc1\n\t" \
    "v_cvt_pk_bf16_f32 v236, v176, v177\n\t" \
    "v_cvt_pk_bf16_f32 v237, v178, v179\n\t" \
    "v_cvt_pk_bf16_f32 v238, v208, v209\n\t" \
    "v_cvt_pk_bf16_f32 v239, v210, v211\n\t" \
    "s_add_u32 s45, s45, %[os3]\n\t" \
    "s_nop 0\n\t" \
    "v_permlane32_swap_b32 v236, v238\n\t" \
    "v_permlane32_swap_b32 v237, v239\n\t" \
    "s_nop 1\n\t" \
    "v_permlane16_swap_b32 v236, v238\n\t" \
    "v_permlane16_swap_b32 v237, v239\n\t" \
    "buffer_store_dwordx4 v[236:239], %[ov], %[srda], s45 offen sc1\n\t" \
    "v_cvt_pk_bf16_f32 v240, v52, v53\n\t" \
    "v_cvt_pk_bf16_f32 v241, v54, v55\n\t" \
    "v_cvt_pk_bf16_f32 v242, v84, v85\n\t" \
    "v_cvt_pk_bf16_f32 v243, v86, v87\n\t" \
    "s_mul_i32 s45, %[os1], 3\n\t" \
    "s_add_u32 s45, s45, %[ooff]\n\t" \
    "s_add_u32 s45, s45, %[os2]\n\t" \
    "v_permlane32_swap_b32 v240, v242\n\t" \
    "v_permlane32_swap_b32 v241, v243\n\t" \
    "s_nop 1\n\t" \
    "v_permlane16_swap_b32 v240, v242\n\t" \
    "v_permlane16_swap_b32 v241, v243\n\t" \
    "buffer_store_dwordx4 v[240:243], %[ov], %[srda], s45 offen sc1\n\t" \
    "v_cvt_pk_bf16_f32 v244, v116, v117\n\t" \
    "v_cvt_pk_bf16_f32 v245, v118, v119\n\t" \
    "v_cvt_pk_bf16_f32 v246, v148, v149\n\t" \
    "v_cvt_pk_bf16_f32 v247, v150, v151\n\t" \
    "s_add_u32 s45, s45, %[os3]\n\t" \
    "s_nop 0\n\t" \
    "v_permlane32_swap_b32 v244, v246\n\t" \
    "v_permlane32_swap_b32 v245, v247\n\t" \
    "s_nop 1\n\t" \
    "v_permlane16_swap_b32 v244, v246\n\t" \
    "v_permlane16_swap_b32 v245, v247\n\t" \
    "buffer_store_dwordx4 v[244:247], %[ov], %[srda], s45 offen sc1\n\t" \
    "v_cvt_pk_bf16_f32 v248, v180, v181\n\t" \
    "v_cvt_pk_bf16_f32 v249, v182, v183\n\t" \
    "v_cvt_pk_bf16_f32 v250, v212, v213\n\t" \
    "v_cvt_pk_bf16_f32 v251, v214, v215\n\t" \
    "s_add_u32 s45, s45, %[os3]\n\t" \
    "s_nop 0\n\t" \
    "v_permlane32_swap_b32 v248, v250\n\t" \
    "v_permlane32_swap_b32 v249, v251\n\t" \
    "s_nop 1\n\t" \
    "v_permlane16_swap_b32 v248, v250\n\t" \
    "v_permlane16_swap_b32 v249, v251\n\t" \
    "buffer_store_dwordx4 v[248:251], %[ov], %[srda], s45 offen sc1\n\t" \
    "s_branch ed_3_%=\n\t" \
    "el_2_%=:\n\t" \
    "v_cvt_pk_bf16_f32 v236, v24, v25\n\t" \
    "v_cvt_pk_bf16_f32 v237, v26, v27\n\t" \
    "v_cvt_pk_bf16_f32 v238, v56, v57\n\t" \
    "v_cvt_pk_bf16_f32 v239, v58, v59\n\t" \
    "s_nop 1\n\t" \
    "v_permlane32_swap_b32 v236, v238\n\t" \
    "v_permlane32_swap_b32 v237, v239\n\t" \
    "s_nop 1\n\t" \
    "v_permlane16_swap_b32 v236, v238\n\t" \
    "v_permlane16_swap_b32 v237, v239\n\t" \
    "ds_write_b128 %[ov], v[236:239] offset:0\n\t" \
    "v_cvt_pk_bf16_f32 v240, v88, v89\n\t" \
    "v_cvt_pk_bf16_f32 v241, v90, v91\n\t" \
    "v_cvt_pk_bf16_f32 v242, v120, v121\n\t" \
    "v_cvt_pk_bf16_f32 v243, v122, v123\n\t" \
    "s_nop 1\n\t" \
    "v_permlane32_swap_b32 v240, v242\n\t" \
    "v_permlane32_swap_b32 v241, v243\n\t" \
    "s_nop 1\n\t" \
    "v_permlane16_swap_b32 v240, v242\n\t" \
    "v_permlane16_swap_b32 v241, v243\n\t" \
    "ds_write_b128 %[ov], v[240:243] offset:8192\n\t" \
    "v_cvt_pk_bf16_f32 v244, v152, v153\n\t" \
    "v_cvt_pk_bf16_f32 v245, v154, v155\n\t" \
    "v_cvt_pk_bf16_f32 v246, v184, v185\n\t" \
    "v_cvt_pk_bf16_f32 v247, v186, v187\n\t" \
    "s_nop 1\n\t" \
    "v_permlane32_swap_b32 v244, v246\n\t" \
    "v_permlane32_swap_b32 v245, v247\n\t" \
    "s_nop 1\n\t" \
    "v_permlane16_swap_b32 v244, v246\n\t" \
    "v_permlane16_swap_b32 v245, v247\n\t" \
    "ds_write_b128 %[ov], v[244:247] offset:16384\n\t" \
    "v_cvt_pk_bf16_f32 v248, v28, v29\n\t" \
    "v_cvt_pk_bf16_f32 v249, v30, v31\n\t" \
    "v_cvt_pk_bf16_f32 v250, v60, v61\n\t" \
    "v_cvt_pk_bf16_f32 v251, v62, v63\n\t" \
    "s_nop 1\n\t" \
    "v_permlane32_swap_b32 v248, v250\n\t" \
    "v_permlane32_swap_b32 v249, v251\n\t" \
    "s_nop 1\n\t" \
    "v_permlane16_swap_b32 v248, v250\n\t" \
    "v_permlane16_swap_b32 v249, v251\n\t" \
    "ds_write_b128 %[ov], v[248:251] offset:128\n\t" \
    "v_cvt_pk_bf16_f32 v236, v92, v93\n\t" \
    "v_cvt_pk_bf16_f32 v237, v94, v95\n\t" \
    "v_cvt_pk_bf16_f32 v238, v124, v125\n\t" \
    "v_cvt_pk_bf16_f32 v239, v126, v127\n\t" \
    "s_nop 1\n\t" \
    "v_permlane32_swap_b32 v236, v238\n\t" \
    "v_permlane32_swap_b32 v237, v239\n\t" \
    "s_nop 1\n\t" \
    "v_permlane16_swap_b32 v236, v238\n\t" \
    "v_permlane16_swap_b32 v237, v239\n\t" \
    "ds_write_b128 %[ov], v[236:239] offset:8320\n\t" \
    "v_cvt_pk_bf16_f32 v240, v156, v157\n\t" \
    "v_cvt_pk_bf16_f32 v241, v158, v159\n\t" \
    "v_cvt_pk_bf16_f32 v242, v188, v189\n\t" \
    "v_cvt_pk_bf16_f32 v243, v190, v191\n\t" \
    "s_nop 1\n\t" \
    "v_permlane32_swap_b32 v240, v242\n\t" \
    "v_permlane32_swap_b32 v241, v243\n\t" \
    "s_nop 1\n\t" \
    "v_permlane16_swap_b32 v240, v242\n\t" \
    "v_permlane16_swap_b32 v241, v243\n\t" \
    "ds_write_b128 %[ov], v[240:243] offset:16512\n\t" \
    "v_cvt_pk_bf16_f32 v244, v32, v33\n\t" \
    "v_cvt_pk_bf16_f32 v245, v34, v35\n\t" \
    "v_cvt_pk_bf16_f32 v246, v64, v65\n\t" \
    "v_cvt_pk_bf16_f32 v247, v66, v67\n\t" \
    "s_nop 1\n\t" \
    "v_permlane32_swap_b32 v244, v246\n\t" \
    "v_permlane32_swap_b32 v245, v247\n\t" \
    "s_nop 1\n\t" \
    "v_permlane16_swap_b32 v244, v246\n\t" \
    "v_permlane16_swap_b32 v245, v247\n\t" \
    "ds_write_b128 %[ov], v[244:247] offset:2048\n\t" \
    "v_cvt_pk_bf16_f32 v248, v96, v97\n\t" \
    "v_cvt_pk_bf16_f32 v249, v98, v99\n\t" \
    "v_cvt_pk_bf16_f32 v250, v128, v129\n\t" \
    "v_cvt_pk_bf16_f32 v251, v130, v131\n\t" \
    "s_nop 1\n\t" \
    "v_permlane32_swap_b32 v248, v250\n\t" \
    "v_permlane32_swap_b32 v249, v251\n\t" \
    "s_nop 1\n\t" \
    "v_permlane16_swap_b32 v248, v250\n\t" \
    "v_permlane16_swap_b32 v249, v251\n\t" \
    "ds_write_b128 %[ov], v[248:251] offset:10240\n\t" \
    "v_cvt_pk_bf16_f32 v236, v160, v161\n\t" \
    "v_cvt_pk_bf16_f32 v237, v162, v163\n\t" \
    "v_cvt_pk_bf16_f32 v238, v192, v193\n\t" \
    "v_cvt_pk_bf16_f32 v239, v194, v195\n\t" \
    "s_nop 1\n\t" \
    "v_permlane32_swap_b32 v236, v238\n\t" \
    "v_permlane32_swap_b32 v237, v239\n\t" \
    "s_nop 1\n\t" \
    "v_permlane16_swap_b32 v236, v238\n\t" \
    "v_permlane16_swap_b32 v237, v239\n\t" \
    "ds_write_b128 %[ov], v[236:239] offset:18432\n\t" \
    "v_cvt_pk_bf16_f32 v240, v36, v37\n\t" \
    "v_cvt_pk_bf16_f32 v241, v38, v39\n\t" \
    "v_cvt_pk_bf16_f32 v242, v68, v69\n\t" \
    "v_cvt_pk_bf16_f32 v243, v70, v71\n\t" \
    "s_nop 1\n\t" \
    "v_permlane32_swap_b32 v240, v242\n\t" \
    "v_permlane32_swap_b32 v241, v243\n\t" \
    "s_nop 1\n\t" \
    "v_permlane16_swap_b32 v240, v242\n\t" \
    "v_permlane16_swap_b32 v241, v243\n\t" \
    "ds_write_b128 %[ov], v[240:243] offset:2176\n\t" \
    "v_cvt_pk_bf16_f32 v244, v100, v101\n\t" \
    "v_cvt_pk_bf16_f32 v245, v102, v103\n\t" \
    "v_cvt_pk_bf16_f32 v246, v132, v133\n\t" \
    "v_cvt_pk_bf16_f32 v247, v134, v135\n\t" \
    "s_nop 1\n\t" \
    "v_permlane32_swap_b32 v244, v246\n\t" \
    "v_permlane32_swap_b32 v245, v247\n\t" \
    "s_nop 1\n\t" \
    "v_permlane16_swap_b32 v244, v246\n\t" \
    "v_permlane16_swap_b32 v245, v247\n\t" \
    "ds_write_b128 %[ov], v[244:247] offset:10368\n\t" \
    "v_cvt_pk_bf16_f32 v248, v164, v165\n\t" \
    "v_cvt_pk_bf16_f32 v249, v166, v167\n\t" \
    "v_cvt_pk_bf16_f32 v250, v196, v197\n\t" \
    "v_cvt_pk_bf16_f32 v251, v198, v199\n\t" \
    "s_nop 1\n\t" \
    "v_permlane32_swap_b32 v248, v250\n\t" \
    "v_permlane32_swap_b32 v249, v251\n\t" \
    "s_nop 1\n\t" \
    "v_permlane16_swap_b32 v248, v250\n\t" \
    "v_permlane16_swap_b32 v249, v251\n\t" \
    "ds_write_b128 %[ov], v[248:251] offset:18560\n\t" \
    "v_cvt_pk_bf16_f32 v236, v40, v41\n\t" \
    "v_cvt_pk_bf16_f32 v237, v42, v43\n\t" \
    "v_cvt_pk_bf16_f32 v238, v72, v73\n\t" \
    "v_cvt_pk_bf16_f32 v239, v74, v75\n\t" \
    "s_nop 1\n\t" \
    "v_permlane32_swap_b32 v236, v238\n\t" \
    "v_permlane32_swap_b32 v237, v239\n\t" \
    "s_nop 1\n\t" \
    "v_permlane16_swap_b32 v236, v238\n\t" \
    "v_permlane16_swap_b32 v237, v239\n\t" \
    "ds_write_b128 %[ov], v[236:239] offset:4096\n\t" \
    "v_cvt_pk_bf16_f32 v240, v104, v105\n\t" \
    "v_cvt_pk_bf16_f32 v241, v106, v107\n\t" \
    "v_cvt_pk_bf16_f32 v242, v136, v137\n\t" \
    "v_cvt_pk_bf16_f32 v243, v138, v139\n\t" \
    "s_nop 1\n\t" \
    "v_permlane32_swap_b32 v240, v242\n\t" \
    "v_permlane32_swap_b32 v241, v243\n\t" \
    "s_nop 1\n\t" \
    "v_permlane16_swap_b32 v240, v242\n\t" \
    "v_permlane16_swap_b32 v241, v243\n\t" \
    "ds_write_b128 %[ov], v[240:243] offset:12288\n\t" \
    "v_cvt_pk_bf16_f32 v244, v168, v169\n\t" \
    "v_cvt_pk_bf16_f32 v245, v170, v171\n\t" \
    "v_cvt_pk_bf16_f32 v246, v200, v201\n\t" \
    "v_cvt_pk_bf16_f32 v247, v202, v203\n\t" \
    "s_nop 1\n\t" \
    "v_permlane32_swap_b32 v244, v246\n\t" \
    "v_permlane32_swap_b32 v245, v247\n\t" \
    "s_nop 1\n\t" \
    "v_permlane16_swap_b32 v244, v246\n\t" \
    "v_permlane16_swap_b32 v245, v247\n\t" \
    "ds_write_b128 %[ov], v[244:247] offset:20480\n\t" \
    "v_cvt_pk_bf16_f32 v248, v44, v45\n\t" \
    "v_cvt_pk_bf16_f32 v249, v46, v47\n\t" \
    "v_cvt_pk_bf16_f32 v250, v76, v77\n\t" \
    "v_cvt_pk_bf16_f32 v251, v78, v79\n\t" \
    "s_nop 1\n\t" \
    "v_permlane32_swap_b32 v248, v250\n\t" \
    "v_permlane32_swap_b32 v249, v251\n\t" \
    "s_nop 1\n\t" \
    "v_permlane16_swap_b32 v248, v250\n\t" \
    "v_permlane16_swap_b32 v249, v251\n\t" \
    "ds_write_b128 %[ov], v[248:251] offset:4224\n\t" \
    "v_cvt_pk_bf16_f32 v236, v108, v109\n\t" \
    "v_cvt_pk_bf16_f32 v237, v110, v111\n\t" \
    "v_cvt_pk_bf16_f32 v238, v140, v141\n\t" \
    "v_cvt_pk_bf16_f32 v239, v142, v143\n\t" \
    "s_nop 1\n\t" \
    "v_permlane32_swap_b32 v236, v238\n\t" \
    "v_permlane32_swap_b32 v237, v239\n\t" \
    "s_nop 1\n\t" \
    "v_permlane16_swap_b32 v236, v238\n\t" \
    "v_permlane16_swap_b32 v237, v239\n\t" \
    "ds_write_b128 %[ov], v[236:239] offset:12416\n\t" \
    "v_cvt_pk_bf16_f32 v240, v172, v173\n\t" \
    "v_cvt_pk_bf16_f32 v241, v174, v175\n\t" \
    "v_cvt_pk_bf16_f32 v242, v204, v205\n\t" \
    "v_cvt_pk_bf16_f32 v243, v206, v207\n\t" \
    "s_nop 1\n\t" \
    "v_permlane32_swap_b32 v240, v242\n\t" \
    "v_permlane32_swap_b32 v241, v243\n\t" \
    "s_nop 1\n\t" \
    "v_permlane16_swap_b32 v240, v242\n\t" \
    "v_permlane16_swap_b32 v241, v243\n\t" \
    "ds_write_b128 %[ov], v[240:243] offset:20608\n\t" \
    "v_cvt_pk_bf16_f32 v244, v48, v49\n\t" \
    "v_cvt_pk_bf16_f32 v245, v50, v51\n\t" \
    "v_cvt_pk_bf16_f32 v246, v80, v81\n\t" \
    "v_cvt_pk_bf16_f32 v247, v82, v83\n\t" \
    "s_nop 1\n\t" \
    "v_permlane32_swap_b32 v244, v246\n\t" \
    "v_permlane32_swap_b32 v245, v247\n\t" \
    "s_nop 1\n\t" \
    "v_permlane16_swap_b32 v244, v246\n\t" \
    "v_permlane16_swap_b32 v245, v247\n\t" \
    "ds_write_b128 %[ov], v[244:247] offset:6144\n\t" \
    "v_cvt_pk_bf16_f32 v248, v112, v113\n\t" \
    "v_cvt_pk_bf16_f32 v249, v114, v115\n\t" \
    "v_cvt_pk_bf16_f32 v250, v144, v145\n\t" \
    "v_cvt_pk_bf16_f32 v251, v146, v147\n\t" \
    "s_nop 1\n\t" \
    "v_permlane32_swap_b32 v248, v250\n\t" \
    "v_permlane32_swap_b32 v249, v251\n\t" \
    "s_nop 1\n\t" \
    "v_permlane16_swap_b32 v248, v250\n\t" \
    "v_permlane16_swap_b32 v249, v251\n\t" \
    "ds_write_b128 %[ov], v[248:251] offset:14336\n\t" \
    "v_cvt_pk_bf16_f32 v236, v176, v177\n\t" \
    "v_cvt_pk_bf16_f32 v237, v178, v179\n\t" \
    "v_cvt_pk_bf16_f32 v238, v208, v209\n\t" \
    "v_cvt_pk_bf16_f32 v239, v210, v211\n\t" \
    "s_nop 1\n\t" \
    "v_permlane32_swap_b32 v236, v238\n\t" \
    "v_permlane32_swap_b32 v237, v239\n\t" \
    "s_nop 1\n\t" \
    "v_permlane16_swap_b32 v236, v238\n\t" \
    "v_permlane16_swap_b32 v237, v239\n\t" \
    "ds_write_b128 %[ov], v[236:239] offset:22528\n\t" \
    "v_cvt_pk_bf16_f32 v240, v52, v53\n\t" \
    "v_cvt_pk_bf16_f32 v241, v54, v55\n\t" \
    "v_cvt_pk_bf16_f32 v242, v84, v85\n\t" \
    "v_cvt_pk_bf16_f32 v243, v86, v87\n\t" \
    "s_nop 1\n\t" \
    "v_permlane32_swap_b32 v240, v242\n\t" \
    "v_permlane32_swap_b32 v241, v243\n\t" \
    "s_nop 1\n\t" \
    "v_permlane16_swap_b32 v240, v242\n\t" \
    "v_permlane16_swap_b32 v241, v243\n\t" \
    "ds_write_b128 %[ov], v[240:243] offset:6272\n\t" \
    "v_cvt_pk_bf16_f32 v244, v116, v117\n\t" \
    "v_cvt_pk_bf16_f32 v245, v118, v119\n\t" \
    "v_cvt_pk_bf16_f32 v246, v148, v149\n\t" \
    "v_cvt_pk_bf16_f32 v247, v150, v151\n\t" \
    "s_nop 1\n\t" \
    "v_permlane32_swap_b32 v244, v246\n\t" \
    "v_permlane32_swap_b32 v245, v247\n\t" \
    "s_nop 1\n\t" \
    "v_permlane16_swap_b32 v244, v246\n\t" \
    "v_permlane16_swap_b32 v245, v247\n\t" \
    "ds_write_b128 %[ov], v[244:247] offset:14464\n\t" \
    "v_cvt_pk_bf16_f32 v248, v180, v181\n\t" \
    "v_cvt_pk_bf16_f32 v249, v182, v183\n\t" \
    "v_cvt_pk_bf16_f32 v250, v212, v213\n\t" \
    "v_cvt_pk_bf16_f32 v251, v214, v215\n\t" \
    "s_nop 1\n\t" \
    "v_permlane32_swap_b32 v248, v250\n\t" \
    "v_permlane32_swap_b32 v249, v251\n\t" \
    "s_nop 1\n\t" \
    "v_permlane16_swap_b32 v248, v250\n\t" \
    "v_permlane16_swap_b32 v249, v251\n\t" \
    "ds_write_b128 %[ov], v[248:251] offset:22656\n\t" \
    "ed_3_%=:\n\t" \
    "s_waitcnt lgkmcnt(0)\n\t"
#define RK_ASM_GU0 \
    "s_nop 4\n\t" \
    "s_mov_b32 s48, %[bm]\n\t" \
    "s_add_u32 m0, s48, 0\n\t" \
    "s_add_u32 s46, %[boff], 0\n\t" \
    "buffer_load_dwordx4 %[bo0], %[srdb], s46 offen lds\n\t" \
    "buffer_load_dwordx4 %[bo1], %[srdb], s46 offen offset:1024 lds\n\t" \
    "buffer_load_dwordx4 %[bo2], %[srdb], s46 offen offset:2048 lds\n\t" \
    "buffer_load_dwordx4 %[bo3], %[srdb], s46 offen offset:3072 lds\n\t" \
    "s_add_u32 m0, s48, 4096\n\t" \
    "s_nop 0\n\t" \
    "buffer_load_dwordx4 %[bo4], %[srdb], s46 offen lds\n\t" \
    "s_add_u32 m0, s48, 5120\n\t" \
    "s_add_u32 s46, %[boff], 40960\n\t" \
    "buffer_load_dwordx4 %[bo0], %[srdb], s46 offen lds\n\t" \
    "buffer_load_dwordx4 %[bo1], %[srdb], s46 offen offset:1024 lds\n\t" \
    "buffer_load_dwordx4 %[bo2], %[srdb], s46 offen offset:2048 lds\n\t" \
    "buffer_load_dwordx4 %[bo3], %[srdb], s46 offen offset:3072 lds\n\t" \
    "s_add_u32 m0, s48, 9216\n\t" \
    "s_nop 0\n\t" \
    "buffer_load_dwordx4 %[bo4], %[srdb], s46 offen lds\n\t" \
    "s_add_u32 m0, s48, 10240\n\t" \
    "s_add_u32 s46, %[boff], 81920\n\t" \
    "buffer_load_dwordx4 %[bo0], %[srdb], s46 offen lds\n\t" \
    "buffer_load_dwordx4 %[bo1], %[srdb], s46 offen offset:1024 lds\n\t" \
    "buffer_load_dwordx4 %[bo2], %[srdb], s46 offen offset:2048 lds\n\t" \
    "buffer_load_dwordx4 %[bo3], %[srdb], s46 offen offset:3072 lds\n\t" \
    "s_add_u32 m0, s48, 14336\n\t" \
    "s_nop 0\n\t" \
    "buffer_load_dwordx4 %[bo4], %[srdb], s46 offen lds\n\t" \
    "s_add_u32 m0, s48, 15360\n\t" \
    "s_add_u32 s46, %[boff], 122880\n\t" \
    "buffer_load_dwordx4 %[bo0], %[srdb], s46 offen lds\n\t" \
    "buffer_load_dwordx4 %[bo1], %[srdb], s46 offen offset:1024 lds\n\t" \
    "buffer_load_dwordx4 %[bo2], %[srdb], s46 offen offset:2048 lds\n\t" \
    "buffer_load_dwordx4 %[bo3], %[srdb], s46 offen offset:3072 lds\n\t" \
    "s_add_u32 m0, s48, 19456\n\t" \
    "s_nop 0\n\t" \
    "buffer_load_dwordx4 %[bo4], %[srdb], s46 offen lds\n\t" \
    "s_waitcnt vmcnt(15) lgkmcnt(0)\n\t" \
    "s_barrier\n\t" \
    "ds_read_b64_tr_b16 a[64:65], %[brd] offset:0\n\t" \
    "ds_read_b64_tr_b16 a[66:67], %[brd] offset:1280\n\t" \
    "ds_read_b128 a[0:3], %[ard] offset:0\n\t" \
    "ds_read_b128 a[4:7], %[ard] offset:1024\n\t" \
    "ds_read_b128 a[8:11], %[ard] offset:2048\n\t" \
    "ds_read_b128 a[12:15], %[ard] offset:3072\n\t" \
    "ds_read_b128 a[16:19], %[ard] offset:4096\n\t" \
    "ds_read_b128 a[20:23], %[ard] offset:5120\n\t" \
    "ds_read_b128 a[24:27], %[ard] offset:6144\n\t" \
    "ds_read_b128 a[28:31], %[ard] offset:7168\n\t" \
    "ds_read_b64_tr_b16 a[68:69], %[brd] offset:256\n\t" \
    "ds_read_b64_tr_b16 a[70:71], %[brd] offset:1536\n\t" \
    "ds_read_b64_tr_b16 a[72:73], %[brd] offset:512\n\t" \
    "ds_read_b64_tr_b16 a[74:75], %[brd] offset:1792\n\t" \
    "ds_read_b64_tr_b16 a[76:77], %[brd] offset:768\n\t" \
    "ds_read_b64_tr_b16 a[78:79], %[brd] offset:2048\n\t" \
    "ds_read_b64_tr_b16 a[80:81], %[brd] offset:1024\n\t" \
    "ds_read_b64_tr_b16 a[82:83], %[brd] offset:2304\n\t" \
    "s_waitcnt lgkmcnt(0) vmcnt(10)\n\t" \
    "v_mfma_f32_16x16x32_bf16 v[24:27], a[0:3], a[64:67], 0\n\t" \
    "ds_read_b64_tr_b16 a[88:89], %[brd] offset:5120\n\t" \
    "v_mfma_f32_16x16x32_bf16 v[28:31], a[4:7], a[64:67], 0\n\t" \
    "ds_read_b64_tr_b16 a[90:91], %[brd] offset:6400\n\t" \
    "v_mfma_f32_16x16x32_bf16 v[32:35], a[8:11], a[64:67], 0\n\t" \
    "ds_read_b128 a[32:35], %[ard] offset:8192\n\t" \
    "v_mfma_f32_16x16x32_bf16 v[36:39], a[12:15], a[64:67], 0\n\t" \
    "ds_read_b128 a[36:39], %[ard] offset:9216\n\t" \
    "v_mfma_f32_16x16x32_bf16 v[40:43], a[16:19], a[64:67], 0\n\t" \
    "ds_read_b128 a[40:43], %[ard] offset:10240\n\t" \
    "v_mfma_f32_16x16x32_bf16 v[44:47], a[20:23], a[64:67], 0\n\t" \
    "ds_read_b128 a[44:47], %[ard] offset:11264\n\t" \
    "v_mfma_f32_16x16x32_bf16 v[48:51], a[24:27], a[64:67], 0\n\t" \
    "ds_read_b128 a[48:51], %[ard] offset:12288\n\t" \
    "v_mfma_f32_16x16x32_bf16 v[52:55], a[28:31], a[64:67], 0\n\t" \
    "ds_read_b128 a[52:55], %[ard] offset:13312\n\t" \
    "v_mfma_f32_16x16x32_bf16 v[56:59], a[0:3], a[68:71], 0\n\t" \
    "ds_read_b128 a[56:59], %[ard] offset:14336\n\t" \
    "v_mfma_f32_16x16x32_bf16 v[60:63], a[4:7], a[68:71], 0\n\t" \
    "ds_read_b128 a[60:63], %[ard] offset:15360\n\t" \
    "v_mfma_f32_16x16x32_bf16 v[64:67], a[8:11], a[68:71], 0\n\t" \
    "ds_read_b64_tr_b16 a[92:93], %[brd] offset:5376\n\t" \
    "v_mfma_f32_16x16x32_bf16 v[68:71], a[12:15], a[68:71], 0\n\t" \
    "ds_read_b64_tr_b16 a[94:95], %[brd] offset:6656\n\t" \
    "v_mfma_f32_16x16x32_bf16 v[72:75], a[16:19], a[68:71], 0\n\t" \
    "ds_read_b64_tr_b16 a[96:97], %[brd] offset:5632\n\t" \
    "v_mfma_f32_16x16x32_bf16 v[76:79], a[20:23], a[68:71], 0\n\t" \
    "ds_read_b64_tr_b16 a[98:99], %[brd] offset:6912\n\t" \
    "v_mfma_f32_16x16x32_bf16 v[80:83], a[24:27], a[68:71], 0\n\t" \
    "ds_read_b64_tr_b16 a[100:101], %[brd] offset:5888\n\t" \
    "v_mfma_f32_16x16x32_bf16 v[84:87], a[28:31], a[68:71], 0\n\t" \
    "ds_read_b64_tr_b16 a[102:103], %[brd] offset:7168\n\t" \
    "v_mfma_f32_16x16x32_bf16 v[88:91], a[0:3], a[72:75], 0\n\t" \
    "ds_read_b64_tr_b16 a[104:105], %[brd] offset:6144\n\t" \
    "v_mfma_f32_16x16x32_bf16 v[92:95], a[4:7], a[72:75], 0\n\t" \
    "ds_read_b64_tr_b16 a[106:107], %[brd] offset:7424\n\t" \
    "v_mfma_f32_16x16x32_bf16 v[96:99], a[8:11], a[72:75], 0\n\t" \
    "s_add_u32 m0, s48, 0\n\t" \
    "s_add_u32 s46, %[boff], 163840\n\t" \
    "buffer_load_dwordx4 %[bo0], %[srdb], s46 offen lds\n\t" \
    "v_mfma_f32_16x16x32_bf16 v[100:103], a[12:15], a[72:75], 0\n\t" \
    "buffer_load_dwordx4 %[bo1], %[srdb], s46 offen offset:1024 lds\n\t" \
    "v_mfma_f32_16x16x32_bf16 v[104:107], a[16:19], a[72:75], 0\n\t" \
    "buffer_load_dwordx4 %[bo2], %[srdb], s46 offen offset:2048 lds\n\t" \
    "v_mfma_f32_16x16x32_bf16 v[108:111], a[20:23], a[72:75], 0\n\t" \
    "buffer_load_dwordx4 %[bo3], %[srdb], s46 offen offset:3072 lds\n\t" \
    "v_mfma_f32_16x16x32_bf16 v[112:115], a[24:27], a[72:75], 0\n\t" \
    "s_add_u32 m0, s48, 4096\n\t" \
    "s_nop 0\n\t" \
    "buffer_load_dwordx4 %[bo4], %[srdb], s46 offen lds\n\t" \
    "v_mfma_f32_16x16x32_bf16 v[116:119], a[28:31], a[72:75], 0\n\t" \
    "v_mfma_f32_16x16x32_bf16 v[120:123], a[0:3], a[76:79], 0\n\t" \
    "v_mfma_f32_16x16x32_bf16 v[124:127], a[4:7], a[76:79], 0\n\t" \
    "v_mfma_f32_16x16x32_bf16 v[128:131], a[8:11], a[76:79], 0\n\t" \
    "v_mfma_f32_16x16x32_bf16 v[132:135], a[12:15], a[76:79], 0\n\t" \
    "v_mfma_f32_16x16x32_bf16 v[136:139], a[16:19], a[76:79], 0\n\t" \
    "v_mfma_f32_16x16x32_bf16 v[140:143], a[20:23], a[76:79], 0\n\t" \
    "v_mfma_f32_16x16x32_bf16 v[144:147], a[24:27], a[76:79], 0\n\t" \
    "v_mfma_f32_16x16x32_bf16 v[148:151], a[28:31], a[76:79], 0\n\t" \
    "v_mfma_f32_16x16x32_bf16 v[152:155], a[0:3], a[80:83], 0\n\t" \
    "v_mfma_f32_16x16x32_bf16 v[156:159], a[4:7], a[80:83], 0\n\t" \
    "v_mfma_f32_16x16x32_bf16 v[160:163], a[8:11], a[80:83], 0\n\t" \
    "v_mfma_f32_16x16x32_bf16 v[164:167], a[12:15], a[80:83], 0\n\t" \
    "v_mfma_f32_16x16x32_bf16 v[168:171], a[16:19], a[80:83], 0\n\t" \
    "v_mfma_f32_16x16x32_bf16 v[172:175], a[20:23], a[80:83], 0\n\t" \
    "v_mfma_f32_16x16x32_bf16 v[176:179], a[24:27], a[80:83], 0\n\t" \
    "v_mfma_f32_16x16x32_bf16 v[180:183], a[28:31], a[80:83], 0\n\t" \
    "s_waitcnt lgkmcnt(0) vmcnt(10)\n\t" \
    "v_mfma_f32_16x16x32_bf16 v[24:27], a[32:35], a[88:91], v[24:27]\n\t" \
    "ds_read_b64_tr_b16 a[64:65], %[brd] offset:10240\n\t" \
    "v_mfma_f32_16x16x32_bf16 v[28:31], a[36:39], a[88:91], v[28:31]\n\t" \
    "ds_read_b64_tr_b16 a[66:67], %[brd] offset:11520\n\t" \
    "v_mfma_f32_16x16x32_bf16 v[32:35], a[40:43], a[88:91], v[32:35]\n\t" \
    "ds_read_b128 a[0:3], %[ard] offset:16384\n\t" \
    "v_mfma_f32_16x16x32_bf16 v[36:39], a[44:47], a[88:91], v[36:39]\n\t" \
    "ds_read_b128 a[4:7], %[ard] offset:17408\n\t" \
    "v_mfma_f32_16x16x32_bf16 v[40:43], a[48:51], a[88:91], v[40:43]\n\t" \
    "ds_read_b128 a[8:11], %[ard] offset:18432\n\t" \
    "v_mfma_f32_16x16x32_bf16 v[44:47], a[52:55], a[88:91], v[44:47]\n\t" \
    "ds_read_b128 a[12:15], %[ard] offset:19456\n\t" \
    "v_mfma_f32_16x16x32_bf16 v[48:51], a[56:59], a[88:91], v[48:51]\n\t" \
    "ds_read_b128 a[16:19], %[ard] offset:20480\n\t" \
    "v_mfma_f32_16x16x32_bf16 v[52:55], a[60:63], a[88:91], v[52:55]\n\t" \
    "ds_read_b128 a[20:23], %[ard] offset:21504\n\t" \
    "v_mfma_f32_16x16x32_bf16 v[56:59], a[32:35], a[92:95], v[56:59]\n\t" \
    "ds_read_b128 a[24:27], %[ard] offset:22528\n\t" \
    "v_mfma_f32_16x16x32_bf16 v[60:63], a[36:39], a[92:95], v[60:63]\n\t" \
    "ds_read_b128 a[28:31], %[ard] offset:23552\n\t" \
    "v_mfma_f32_16x16x32_bf16 v[64:67], a[40:43], a[92:95], v[64:67]\n\t" \
    "ds_read_b64_tr_b16 a[68:69], %[brd] offset:10496\n\t" \
    "v_mfma_f32_16x16x32_bf16 v[68:71], a[44:47], a[92:95], v[68:71]\n\t" \
    "ds_read_b64_tr_b16 a[70:71], %[brd] offset:11776\n\t" \
    "v_mfma_f32_16x16x32_bf16 v[72:75], a[48:51], a[92:95], v[72:75]\n\t" \
    "ds_read_b64_tr_b16 a[72:73], %[brd] offset:10752\n\t" \
    "v_mfma_f32_16x16x32_bf16 v[76:79], a[52:55], a[92:95], v[76:79]\n\t" \
    "ds_read_b64_tr_b16 a[74:75], %[brd] offset:12032\n\t" \
    "v_mfma_f32_16x16x32_bf16 v[80:83], a[56:59], a[92:95], v[80:83]\n\t" \
    "ds_read_b64_tr_b16 a[76:77], %[brd] offset:11008\n\t" \
    "v_mfma_f32_16x16x32_bf16 v[84:87], a[60:63], a[92:95], v[84:87]\n\t" \
    "ds_read_b64_tr_b16 a[78:79], %[brd] offset:12288\n\t" \
    "v_mfma_f32_16x16x32_bf16 v[88:91], a[32:35], a[96:99], v[88:91]\n\t" \
    "ds_read_b64_tr_b16 a[80:81], %[brd] offset:11264\n\t" \
    "v_mfma_f32_16x16x32_bf16 v[92:95], a[36:39], a[96:99], v[92:95]\n\t" \
    "ds_read_b64_tr_b16 a[82:83], %[brd] offset:12544\n\t" \
    "v_mfma_f32_16x16x32_bf16 v[96:99], a[40:43], a[96:99], v[96:99]\n\t" \
    "s_add_u32 m0, s48, 5120\n\t" \
    "s_add_u32 s46, %[boff], 204800\n\t" \
    "buffer_load_dwordx4 %[bo0], %[srdb], s46 offen lds\n\t" \
    "v_mfma_f32_16x16x32_bf16 v[100:103], a[44:47], a[96:99], v[100:103]\n\t" \
    "buffer_load_dwordx4 %[bo1], %[srdb], s46 offen offset:1024 lds\n\t" \
    "v_mfma_f32_16x16x32_bf16 v[104:107], a[48:51], a[96:99], v[104:107]\n\t" \
    "buffer_load_dwordx4 %[bo2], %[srdb], s46 offen offset:2048 lds\n\t" \
    "v_mfma_f32_16x16x32_bf16 v[108:111], a[52:55], a[96:99], v[108:111]\n\t" \
    "buffer_load_dwordx4 %[bo3], %[srdb], s46 offen offset:3072 lds\n\t" \
    "v_mfma_f32_16x16x32_bf16 v[112:115], a[56:59], a[96:99], v[112:115]\n\t" \
    "s_add_u32 m0, s48, 9216\n\t" \
    "s_nop 0\n\t" \
    "buffer_load_dwordx4 %[bo4], %[srdb], s46 offen lds\n\t" \
    "v_mfma_f32_16x16x32_bf16 v[116:119], a[60:63], a[96:99], v[116:119]\n\t" \
    "v_mfma_f32_16x16x32_bf16 v[120:123], a[32:35], a[100:103], v[120:123]\n\t" \
    "v_mfma_f32_16x16x32_bf16 v[124:127], a[36:39], a[100:103], v[124:127]\n\t" \
    "v_mfma_f32_16x16x32_bf16 v[128:131], a[40:43], a[100:103], v[128:131]\n\t" \
    "v_mfma_f32_16x16x32_bf16 v[132:135], a[44:47], a[100:103], v[132:135]\n\t" \
    "v_mfma_f32_16x16x32_bf16 v[136:139], a[48:51], a[100:103], v[136:139]\n\t" \
    "v_mfma_f32_16x16x32_bf16 v[140:143], a[52:55], a[100:103], v[140:143]\n\t" \
    "v_mfma_f32_16x16x32_bf16 v[144:147], a[56:59], a[100:103], v[144:147]\n\t" \
    "v_mfma_f32_16x16x32_bf16 v[148:151], a[60:63], a[100:103], v[148:151]\n\t" \
    "v_mfma_f32_16x16x32_bf16 v[152:155], a[32:35], a[104:107], v[152:155]\n\t" \
    "v_mfma_f32_16x16x32_bf16 v[156:159], a[36:39], a[104:107], v[156:159]\n\t" \
    "v_mfma_f32_16x16x32_bf16 v[160:163], a[40:43], a[104:107], v[160:163]\n\t" \
    "v_mfma_f32_16x16x32_bf16 v[164:167], a[44:47], a[104:107], v[164:167]\n\t" \
    "v_mfma_f32_16x16x32_bf16 v[168:171], a[48:51], a[104:107], v[168:171]\n\t" \
    "v_mfma_f32_16x16x32_bf16 v[172:175], a[52:55], a[104:107], v[172:175]\n\t" \
    "v_mfma_f32_16x16x32_bf16 v[176:179], a[56:59], a[104:107], v[176:179]\n\t" \
    "v_mfma_f32_16x16x32_bf16 v[180:183], a[60:63], a[104:107], v[180:183]\n\t" \
    "s_waitcnt lgkmcnt(0) vmcnt(10)\n\t" \
    "v_mfma_f32_16x16x32_bf16 v[24:27], a[0:3], a[64:67], v[24:27]\n\t" \
    "ds_read_b64_tr_b16 a[88:89], %[brd] offset:15360\n\t" \
    "v_mfma_f32_16x16x32_bf16 v[28:31], a[4:7], a[64:67], v[28:31]\n\t" \
    "ds_read_b64_tr_b16 a[90:91], %[brd] offset:16640\n\t" \
    "v_mfma_f32_16x16x32_bf16 v[32:35], a[8:11], a[64:67], v[32:35]\n\t" \
    "ds_read_b128 a[32:35], %[ard] offset:24576\n\t" \
    "v_mfma_f32_16x16x32_bf16 v[36:39], a[12:15], a[64:67], v[36:39]\n\t" \
    "ds_read_b128 a[36:39], %[ard] offset:25600\n\t" \
    "v_mfma_f32_16x16x32_bf16 v[40:43], a[16:19], a[64:67], v[40:43]\n\t" \
    "ds_read_b128 a[40:43], %[ard] offset:26624\n\t" \
    "v_mfma_f32_16x16x32_bf16 v[44:47], a[20:23], a[64:67], v[44:47]\n\t" \
    "ds_read_b128 a[44:47], %[ard] offset:27648\n\t" \
    "v_mfma_f32_16x16x32_bf16 v[48:51], a[24:27], a[64:67], v[48:51]\n\t" \
    "ds_read_b128 a[48:51], %[ard] offset:28672\n\t" \
    "v_mfma_f32_16x16x32_bf16 v[52:55], a[28:31], a[64:67], v[52:55]\n\t" \
    "ds_read_b128 a[52:55], %[ard] offset:29696\n\t" \
    "v_mfma_f32_16x16x32_bf16 v[56:59], a[0:3], a[68:71], v[56:59]\n\t" \
    "ds_read_b128 a[56:59], %[ard] offset:30720\n\t" \
    "v_mfma_f32_16x16x32_bf16 v[60:63], a[4:7], a[68:71], v[60:63]\n\t" \
    "ds_read_b128 a[60:63], %[ard] offset:31744\n\t" \
    "v_mfma_f32_16x16x32_bf16 v[64:67], a[8:11], a[68:71], v[64:67]\n\t" \
    "ds_read_b64_tr_b16 a[92:93], %[brd] offset:15616\n\t" \
    "v_mfma_f32_16x16x32_bf16 v[68:71], a[12:15], a[68:71], v[68:71]\n\t" \
    "ds_read_b64_tr_b16 a[94:95], %[brd] offset:16896\n\t" \
    "v_mfma_f32_16x16x32_bf16 v[72:75], a[16:19], a[68:71], v[72:75]\n\t" \
    "ds_read_b64_tr_b16 a[96:97], %[brd] offset:15872\n\t" \
    "v_mfma_f32_16x16x32_bf16 v[76:79], a[20:23], a[68:71], v[76:79]\n\t" \
    "ds_read_b64_tr_b16 a[98:99], %[brd] offset:17152\n\t" \
    "v_mfma_f32_16x16x32_bf16 v[80:83], a[24:27], a[68:71], v[80:83]\n\t" \
    "ds_read_b64_tr_b16 a[100:101], %[brd] offset:16128\n\t" \
    "v_mfma_f32_16x16x32_bf16 v[84:87], a[28:31], a[68:71], v[84:87]\n\t" \
    "ds_read_b64_tr_b16 a[102:103], %[brd] offset:17408\n\t" \
    "v_mfma_f32_16x16x32_bf16 v[88:91], a[0:3], a[72:75], v[88:91]\n\t" \
    "ds_read_b64_tr_b16 a[104:105], %[brd] offset:16384\n\t" \
    "v_mfma_f32_16x16x32_bf16 v[92:95], a[4:7], a[72:75], v[92:95]\n\t" \
    "ds_read_b64_tr_b16 a[106:107], %[brd] offset:17664\n\t" \
    "v_mfma_f32_16x16x32_bf16 v[96:99], a[8:11], a[72:75], v[96:99]\n\t" \
    "s_add_u32 m0, s48, 10240\n\t" \
    "s_add_u32 s46, %[boff], 640\n\t" \
    "buffer_load_dwordx4 %[bo0], %[srdb], s46 offen lds\n\t" \
    "v_mfma_f32_16x16x32_bf16 v[100:103], a[12:15], a[72:75], v[100:103]\n\t" \
    "buffer_load_dwordx4 %[bo1], %[srdb], s46 offen offset:1024 lds\n\t" \
    "v_mfma_f32_16x16x32_bf16 v[104:107], a[16:19], a[72:75], v[104:107]\n\t" \
    "buffer_load_dwordx4 %[bo2], %[srdb], s46 offen offset:2048 lds\n\t" \
    "v_mfma_f32_16x16x32_bf16 v[108:111], a[20:23], a[72:75], v[108:111]\n\t" \
    "buffer_load_dwordx4 %[bo3], %[srdb], s46 offen offset:3072 lds\n\t" \
    "v_mfma_f32_16x16x32_bf16 v[112:115], a[24:27], a[72:75], v[112:115]\n\t" \
    "s_add_u32 m0, s48, 14336\n\t" \
    "s_nop 0\n\t" \
    "buffer_load_dwordx4 %[bo4], %[srdb], s46 offen lds\n\t" \
    "v_mfma_f32_16x16x32_bf16 v[116:119], a[28:31], a[72:75], v[116:119]\n\t" \
    "v_mfma_f32_16x16x32_bf16 v[120:123], a[0:3], a[76:79], v[120:123]\n\t" \
    "v_mfma_f32_16x16x32_bf16 v[124:127], a[4:7], a[76:79], v[124:127]\n\t" \
    "v_mfma_f32_16x16x32_bf16 v[128:131], a[8:11], a[76:79], v[128:131]\n\t" \
    "v_mfma_f32_16x16x32_bf16 v[132:135], a[12:15], a[76:79], v[132:135]\n\t" \
    "v_mfma_f32_16x16x32_bf16 v[136:139], a[16:19], a[76:79], v[136:139]\n\t" \
    "v_mfma_f32_16x16x32_bf16 v[140:143], a[20:23], a[76:79], v[140:143]\n\t" \
    "v_mfma_f32_16x16x32_bf16 v[144:147], a[24:27], a[76:79], v[144:147]\n\t" \
    "v_mfma_f32_16x16x32_bf16 v[148:151], a[28:31], a[76:79], v[148:151]\n\t" \
    "v_mfma_f32_16x16x32_bf16 v[152:155], a[0:3], a[80:83], v[152:155]\n\t" \
    "v_mfma_f32_16x16x32_bf16 v[156:159], a[4:7], a[80:83], v[156:159]\n\t" \
    "v_mfma_f32_16x16x32_bf16 v[160:163], a[8:11], a[80:83], v[160:163]\n\t" \
    "v_mfma_f32_16x16x32_bf16 v[164:167], a[12:15], a[80:83], v[164:167]\n\t" \
    "v_mfma_f32_16x16x32_bf16 v[168:171], a[16:19], a[80:83], v[168:171]\n\t" \
    "v_mfma_f32_16x16x32_bf16 v[172:175], a[20:23], a[80:83], v[172:175]\n\t" \
    "v_mfma_f32_16x16x32_bf16 v[176:179], a[24:27], a[80:83], v[176:179]\n\t" \
    "v_mfma_f32_16x16x32_bf16 v[180:183], a[28:31], a[80:83], v[180:183]\n\t" \
    "s_waitcnt lgkmcnt(0) vmcnt(10)\n\t" \
    "v_mfma_f32_16x16x32_bf16 v[24:27], a[32:35], a[88:91], v[24:27]\n\t" \
    "ds_read_b64_tr_b16 a[64:65], %[brd] offset:0\n\t" \
    "v_mfma_f32_16x16x32_bf16 v[28:31], a[36:39], a[88:91], v[28:31]\n\t" \
    "ds_read_b64_tr_b16 a[66:67], %[brd] offset:1280\n\t" \
    "v_mfma_f32_16x16x32_bf16 v[32:35], a[40:43], a[88:91], v[32:35]\n\t" \
    "ds_read_b128 a[0:3], %[ard] offset:32768\n\t" \
    "v_mfma_f32_16x16x32_bf16 v[36:39], a[44:47], a[88:91], v[36:39]\n\t" \
    "ds_read_b128 a[4:7], %[ard] offset:33792\n\t" \
    "v_mfma_f32_16x16x32_bf16 v[40:43], a[48:51], a[88:91], v[40:43]\n\t" \
    "ds_read_b128 a[8:11], %[ard] offset:34816\n\t" \
    "v_mfma_f32_16x16x32_bf16 v[44:47], a[52:55], a[88:91], v[44:47]\n\t" \
    "ds_read_b128 a[12:15], %[ard] offset:35840\n\t" \
    "v_mfma_f32_16x16x32_bf16 v[48:51], a[56:59], a[88:91], v[48:51]\n\t" \
    "ds_read_b128 a[16:19], %[ard] offset:36864\n\t" \
    "v_mfma_f32_16x16x32_bf16 v[52:55], a[60:63], a[88:91], v[52:55]\n\t" \
    "ds_read_b128 a[20:23], %[ard] offset:37888\n\t" \
    "v_mfma_f32_16x16x32_bf16 v[56:59], a[32:35], a[92:95], v[56:59]\n\t" \
    "ds_read_b128 a[24:27], %[ard] offset:38912\n\t" \
    "v_mfma_f32_16x16x32_bf16 v[60:63], a[36:39], a[92:95], v[60:63]\n\t" \
    "ds_read_b128 a[28:31], %[ard] offset:39936\n\t" \
    "v_mfma_f32_16x16x32_bf16 v[64:67], a[40:43], a[92:95], v[64:67]\n\t" \
    "ds_read_b64_tr_b16 a[68:69], %[brd] offset:256\n\t" \
    "v_mfma_f32_16x16x32_bf16 v[68:71], a[44:47], a[92:95], v[68:71]\n\t" \
    "ds_read_b64_tr_b16 a[70:71], %[brd] offset:1536\n\t" \
    "v_mfma_f32_16x16x32_bf16 v[72:75], a[48:51], a[92:95], v[72:75]\n\t" \
    "ds_read_b64_tr_b16 a[72:73], %[brd] offset:512\n\t" \
    "v_mfma_f32_16x16x32_bf16 v[76:79], a[52:55], a[92:95], v[76:79]\n\t" \
    "ds_read_b64_tr_b16 a[74:75], %[brd] offset:1792\n\t" \
    "v_mfma_f32_16x16x32_bf16 v[80:83], a[56:59], a[92:95], v[80:83]\n\t" \
    "ds_read_b64_tr_b16 a[76:77], %[brd] offset:768\n\t" \
    "v_mfma_f32_16x16x32_bf16 v[84:87], a[60:63], a[92:95], v[84:87]\n\t" \
    "ds_read_b64_tr_b16 a[78:79], %[brd] offset:2048\n\t" \
    "v_mfma_f32_16x16x32_bf16 v[88:91], a[32:35], a[96:99], v[88:91]\n\t" \
    "ds_read_b64_tr_b16 a[80:81], %[brd] offset:1024\n\t" \
    "v_mfma_f32_16x16x32_bf16 v[92:95], a[36:39], a[96:99], v[92:95]\n\t" \
    "ds_read_b64_tr_b16 a[82:83], %[brd] offset:2304\n\t" \
    "v_mfma_f32_16x16x32_bf16 v[96:99], a[40:43], a[96:99], v[96:99]\n\t" \
    "s_add_u32 m0, s48, 15360\n\t" \
    "s_add_u32 s46, %[boff], 41600\n\t" \
    "buffer_load_dwordx4 %[bo0], %[srdb], s46 offen lds\n\t" \
    "v_mfma_f32_16x16x32_bf16 v[100:103], a[44:47], a[96:99], v[100:103]\n\t" \
    "buffer_load_dwordx4 %[bo1], %[srdb], s46 offen offset:1024 lds\n\t" \
    "v_mfma_f32_16x16x32_bf16 v[104:107], a[48:51], a[96:99], v[104:107]\n\t" \
    "buffer_load_dwordx4 %[bo2], %[srdb], s46 offen offset:2048 lds\n\t" \
    "v_mfma_f32_16x16x32_bf16 v[108:111], a[52:55], a[96:99], v[108:111]\n\t" \
    "buffer_load_dwordx4 %[bo3], %[srdb], s46 offen offset:3072 lds\n\t" \
    "v_mfma_f32_16x16x32_bf16 v[112:115], a[56:59], a[96:99], v[112:115]\n\t" \
    "s_add_u32 m0, s48, 19456\n\t" \
    "s_nop 0\n\t" \
    "buffer_load_dwordx4 %[bo4], %[srdb], s46 offen lds\n\t" \
    "v_mfma_f32_16x16x32_bf16 v[116:119], a[60:63], a[96:99], v[116:119]\n\t" \
    "v_mfma_f32_16x16x32_bf16 v[120:123], a[32:35], a[100:103], v[120:123]\n\t" \
    "v_mfma_f32_16x16x32_bf16 v[124:127], a[36:39], a[100:103], v[124:127]\n\t" \
    "v_mfma_f32_16x16x32_bf16 v[128:131], a[40:43], a[100:103], v[128:131]\n\t" \
    "v_mfma_f32_16x16x32_bf16 v[132:135], a[44:47], a[100:103], v[132:135]\n\t" \
    "v_mfma_f32_16x16x32_bf16 v[136:139], a[48:51], a[100:103], v[136:139]\n\t" \
    "v_mfma_f32_16x16x32_bf16 v[140:143], a[52:55], a[100:103], v[140:143]\n\t" \
    "v_mfma_f32_16x16x32_bf16 v[144:147], a[56:59], a[100:103], v[144:147]\n\t" \
    "v_mfma_f32_16x16x32_bf16 v[148:151], a[60:63], a[100:103], v[148:151]\n\t" \
    "v_mfma_f32_16x16x32_bf16 v[152:155], a[32:35], a[104:107], v[152:155]\n\t" \
    "v_mfma_f32_16x16x32_bf16 v[156:159], a[36:39], a[104:107], v[156:159]\n\t" \
    "v_mfma_f32_16x16x32_bf16 v[160:163], a[40:43], a[104:107], v[160:163]\n\t" \
    "v_mfma_f32_16x16x32_bf16 v[164:167], a[44:47], a[104:107], v[164:167]\n\t" \
    "v_mfma_f32_16x16x32_bf16 v[168:171], a[48:51], a[104:107], v[168:171]\n\t" \
    "v_mfma_f32_16x16x32_bf16 v[172:175], a[52:55], a[104:107], v[172:175]\n\t" \
    "v_mfma_f32_16x16x32_bf16 v[176:179], a[56:59], a[104:107], v[176:179]\n\t" \
    "v_mfma_f32_16x16x32_bf16 v[180:183], a[60:63], a[104:107], v[180:183]\n\t" \
    "s_waitcnt lgkmcnt(0) vmcnt(10)\n\t" \
    "v_mfma_f32_16x16x32_bf16 v[24:27], a[0:3], a[64:67], v[24:27]\n\t" \
    "ds_read_b64_tr_b16 a[88:89], %[brd] offset:5120\n\t" \
    "v_mfma_f32_16x16x32_bf16 v[28:31], a[4:7], a[64:67], v[28:31]\n\t" \
    "ds_read_b64_tr_b16 a[90:91], %[brd] offset:6400\n\t" \
    "v_mfma_f32_16x16x32_bf16 v[32:35], a[8:11], a[64:67], v[32:35]\n\t" \
    "ds_read_b128 a[32:35], %[ard] offset:40960\n\t" \
    "v_mfma_f32_16x16x32_bf16 v[36:39], a[12:15], a[64:67], v[36:39]\n\t" \
    "ds_read_b128 a[36:39], %[ard] offset:41984\n\t" \
    "v_mfma_f32_16x16x32_bf16 v[40:43], a[16:19], a[64:67], v[40:43]\n\t" \
    "ds_read_b128 a[40:43], %[ard] offset:43008\n\t" \
    "v_mfma_f32_16x16x32_bf16 v[44:47], a[20:23], a[64:67], v[44:47]\n\t" \
    "ds_read_b128 a[44:47], %[ard] offset:44032\n\t" \
    "v_mfma_f32_16x16x32_bf16 v[48:51], a[24:27], a[64:67], v[48:51]\n\t" \
    "ds_read_b128 a[48:51], %[ard] offset:45056\n\t" \
    "v_mfma_f32_16x16x32_bf16 v[52:55], a[28:31], a[64:67], v[52:55]\n\t" \
    "ds_read_b128 a[52:55], %[ard] offset:46080\n\t" \
    "v_mfma_f32_16x16x32_bf16 v[56:59], a[0:3], a[68:71], v[56:59]\n\t" \
    "ds_read_b128 a[56:59], %[ard] offset:47104\n\t" \
    "v_mfma_f32_16x16x32_bf16 v[60:63], a[4:7], a[68:71], v[60:63]\n\t" \
    "ds_read_b128 a[60:63], %[ard] offset:48128\n\t" \
    "v_mfma_f32_16x16x32_bf16 v[64:67], a[8:11], a[68:71], v[64:67]\n\t" \
    "ds_read_b64_tr_b16 a[92:93], %[brd] offset:5376\n\t" \
    "v_mfma_f32_16x16x32_bf16 v[68:71], a[12:15], a[68:71], v[68:71]\n\t" \
    "ds_read_b64_tr_b16 a[94:95], %[brd] offset:6656\n\t" \
    "v_mfma_f32_16x16x32_bf16 v[72:75], a[16:19], a[68:71], v[72:75]\n\t" \
    "ds_read_b64_tr_b16 a[96:97], %[brd] offset:5632\n\t" \
    "v_mfma_f32_16x16x32_bf16 v[76:79], a[20:23], a[68:71], v[76:79]\n\t" \
    "ds_read_b64_tr_b16 a[98:99], %[brd] offset:6912\n\t" \
    "v_mfma_f32_16x16x32_bf16 v[80:83], a[24:27], a[68:71], v[80:83]\n\t" \
    "ds_read_b64_tr_b16 a[100:101], %[brd] offset:5888\n\t" \
    "v_mfma_f32_16x16x32_bf16 v[84:87], a[28:31], a[68:71], v[84:87]\n\t" \
    "ds_read_b64_tr_b16 a[102:103], %[brd] offset:7168\n\t" \
    "v_mfma_f32_16x16x32_bf16 v[88:91], a[0:3], a[72:75], v[88:91]\n\t" \
    "ds_read_b64_tr_b16 a[104:105], %[brd] offset:6144\n\t" \
    "v_mfma_f32_16x16x32_bf16 v[92:95], a[4:7], a[72:75], v[92:95]\n\t" \
    "ds_read_b64_tr_b16 a[106:107], %[brd] offset:7424\n\t" \
    "v_mfma_f32_16x16x32_bf16 v[96:99], a[8:11], a[72:75], v[96:99]\n\t" \
    "s_add_u32 m0, s48, 0\n\t" \
    "s_add_u32 s46, %[boff], 82560\n\t" \
    "buffer_load_dwordx4 %[bo0], %[srdb], s46 offen lds\n\t" \
    "v_mfma_f32_16x16x32_bf16 v[100:103], a[12:15], a[72:75], v[100:103]\n\t" \
    "buffer_load_dwordx4 %[bo1], %[srdb], s46 offen offset:1024 lds\n\t" \
    "v_mfma_f32_16x16x32_bf16 v[104:107], a[16:19], a[72:75], v[104:107]\n\t" \
    "buffer_load_dwordx4 %[bo2], %[srdb], s46 offen offset:2048 lds\n\t" \
    "v_mfma_f32_16x16x32_bf16 v[108:111], a[20:23], a[72:75], v[108:111]\n\t" \
    "buffer_load_dwordx4 %[bo3], %[srdb], s46 offen offset:3072 lds\n\t" \
    "v_mfma_f32_16x16x32_bf16 v[112:115], a[24:27], a[72:75], v[112:115]\n\t" \
    "s_add_u32 m0, s48, 4096\n\t" \
    "s_nop 0\n\t" \
    "buffer_load_dwordx4 %[bo4], %[srdb], s46 offen lds\n\t" \
    "v_mfma_f32_16x16x32_bf16 v[116:119], a[28:31], a[72:75], v[116:119]\n\t" \
    "v_mfma_f32_16x16x32_bf16 v[120:123], a[0:3], a[76:79], v[120:123]\n\t" \
    "v_mfma_f32_16x16x32_bf16 v[124:127], a[4:7], a[76:79], v[124:127]\n\t" \
    "v_mfma_f32_16x16x32_bf16 v[128:131], a[8:11], a[76:79], v[128:131]\n\t" \
    "v_mfma_f32_16x16x32_bf16 v[132:135], a[12:15], a[76:79], v[132:135]\n\t" \
    "v_mfma_f32_16x16x32_bf16 v[136:139], a[16:19], a[76:79], v[136:139]\n\t" \
    "v_mfma_f32_16x16x32_bf16 v[140:143], a[20:23], a[76:79], v[140:143]\n\t" \
    "v_mfma_f32_16x16x32_bf16 v[144:147], a[24:27], a[76:79], v[144:147]\n\t" \
    "v_mfma_f32_16x16x32_bf16 v[148:151], a[28:31], a[76:79], v[148:151]\n\t" \
    "v_mfma_f32_16x16x32_bf16 v[152:155], a[0:3], a[80:83], v[152:155]\n\t" \
    "v_mfma_f32_16x16x32_bf16 v[156:159], a[4:7], a[80:83], v[156:159]\n\t" \
    "v_mfma_f32_16x16x32_bf16 v[160:163], a[8:11], a[80:83], v[160:163]\n\t" \
    "v_mfma_f32_16x16x32_bf16 v[164:167], a[12:15], a[80:83], v[164:167]\n\t" \
    "v_mfma_f32_16x16x32_bf16 v[168:171], a[16:19], a[80:83], v[168:171]\n\t" \
    "v_mfma_f32_16x16x32_bf16 v[172:175], a[20:23], a[80:83], v[172:175]\n\t" \
    "v_mfma_f32_16x16x32_bf16 v[176:179], a[24:27], a[80:83], v[176:179]\n\t" \
    "v_mfma_f32_16x16x32_bf16 v[180:183], a[28:31], a[80:83], v[180:183]\n\t" \
    "s_waitcnt lgkmcnt(0) vmcnt(10)\n\t" \
    "v_mfma_f32_16x16x32_bf16 v[24:27], a[32:35], a[88:91], v[24:27]\n\t" \
    "ds_read_b64_tr_b16 a[64:65], %[brd] offset:10240\n\t" \
    "v_mfma_f32_16x16x32_bf16 v[28:31], a[36:39], a[88:91], v[28:31]\n\t" \
    "ds_read_b64_tr_b16 a[66:67], %[brd] offset:11520\n\t" \
    "v_mfma_f32_16x16x32_bf16 v[32:35], a[40:43], a[88:91], v[32:35]\n\t" \
    "ds_read_b128 a[0:3], %[ard] offset:0\n\t" \
    "v_mfma_f32_16x16x32_bf16 v[36:39], a[44:47], a[88:91], v[36:39]\n\t" \
    "ds_read_b128 a[4:7], %[ard] offset:1024\n\t" \
    "v_mfma_f32_16x16x32_bf16 v[40:43], a[48:51], a[88:91], v[40:43]\n\t" \
    "ds_read_b128 a[8:11], %[ard] offset:2048\n\t" \
    "v_mfma_f32_16x16x32_bf16 v[44:47], a[52:55], a[88:91], v[44:47]\n\t" \
    "ds_read_b128 a[12:15], %[ard] offset:3072\n\t" \
    "v_mfma_f32_16x16x32_bf16 v[48:51], a[56:59], a[88:91], v[48:51]\n\t" \
    "ds_read_b128 a[16:19], %[ard] offset:4096\n\t" \
    "v_mfma_f32_16x16x32_bf16 v[52:55], a[60:63], a[88:91], v[52:55]\n\t" \
    "ds_read_b128 a[20:23], %[ard] offset:5120\n\t" \
    "v_mfma_f32_16x16x32_bf16 v[56:59], a[32:35], a[92:95], v[56:59]\n\t" \
    "ds_read_b128 a[24:27], %[ard] offset:6144\n\t" \
    "v_mfma_f32_16x16x32_bf16 v[60:63], a[36:39], a[92:95], v[60:63]\n\t" \
    "ds_read_b128 a[28:31], %[ard] offset:7168\n\t" \
    "v_mfma_f32_16x16x32_bf16 v[64:67], a[40:43], a[92:95], v[64:67]\n\t" \
    "ds_read_b64_tr_b16 a[68:69], %[brd] offset:10496\n\t" \
    "v_mfma_f32_16x16x32_bf16 v[68:71], a[44:47], a[92:95], v[68:71]\n\t" \
    "ds_read_b64_tr_b16 a[70:71], %[brd] offset:11776\n\t" \
    "v_mfma_f32_16x16x32_bf16 v[72:75], a[48:51], a[92:95], v[72:75]\n\t" \
    "ds_read_b64_tr_b16 a[72:73], %[brd] offset:10752\n\t" \
    "v_mfma_f32_16x16x32_bf16 v[76:79], a[52:55], a[92:95], v[76:79]\n\t" \
    "ds_read_b64_tr_b16 a[74:75], %[brd] offset:12032\n\t" \
    "v_mfma_f32_16x16x32_bf16 v[80:83], a[56:59], a[92:95], v[80:83]\n\t" \
    "ds_read_b64_tr_b16 a[76:77], %[brd] offset:11008\n\t" \
    "v_mfma_f32_16x16x32_bf16 v[84:87], a[60:63], a[92:95], v[84:87]\n\t" \
    "ds_read_b64_tr_b16 a[78:79], %[brd] offset:12288\n\t" \
    "v_mfma_f32_16x16x32_bf16 v[88:91], a[32:35], a[96:99], v[88:91]\n\t" \
    "ds_read_b64_tr_b16 a[80:81], %[brd] offset:11264\n\t" \
    "v_mfma_f32_16x16x32_bf16 v[92:95], a[36:39], a[96:99], v[92:95]\n\t" \
    "ds_read_b64_tr_b16 a[82:83], %[brd] offset:12544\n\t" \
    "v_mfma_f32_16x16x32_bf16 v[96:99], a[40:43], a[96:99], v[96:99]\n\t" \
    "s_add_u32 m0, s48, 5120\n\t" \
    "s_add_u32 s46, %[boff], 123520\n\t" \
    "buffer_load_dwordx4 %[bo0], %[srdb], s46 offen lds\n\t" \
    "v_mfma_f32_16x16x32_bf16 v[100:103], a[44:47], a[96:99], v[100:103]\n\t" \
    "buffer_load_dwordx4 %[bo1], %[srdb], s46 offen offset:1024 lds\n\t" \
    "v_mfma_f32_16x16x32_bf16 v[104:107], a[48:51], a[96:99], v[104:107]\n\t" \
    "buffer_load_dwordx4 %[bo2], %[srdb], s46 offen offset:2048 lds\n\t" \
    "v_mfma_f32_16x16x32_bf16 v[108:111], a[52:55], a[96:99], v[108:111]\n\t" \
    "buffer_load_dwordx4 %[bo3], %[srdb], s46 offen offset:3072 lds\n\t" \
    "v_mfma_f32_16x16x32_bf16 v[112:115], a[56:59], a[96:99], v[112:115]\n\t" \
    "s_add_u32 m0, s48, 9216\n\t" \
    "s_nop 0\n\t" \
    "buffer_load_dwordx4 %[bo4], %[srdb], s46 offen lds\n\t" \
    "v_mfma_f32_16x16x32_bf16 v[116:119], a[60:63], a[96:99], v[116:119]\n\t" \
    "v_mfma_f32_16x16x32_bf16 v[120:123], a[32:35], a[100:103], v[120:123]\n\t" \
    "v_mfma_f32_16x16x32_bf16 v[124:127], a[36:39], a[100:103], v[124:127]\n\t" \
    "v_mfma_f32_16x16x32_bf16 v[128:131], a[40:43], a[100:103], v[128:131]\n\t" \
    "v_mfma_f32_16x16x32_bf16 v[132:135], a[44:47], a[100:103], v[132:135]\n\t" \
    "v_mfma_f32_16x16x32_bf16 v[136:139], a[48:51], a[100:103], v[136:139]\n\t" \
    "v_mfma_f32_16x16x32_bf16 v[140:143], a[52:55], a[100:103], v[140:143]\n\t" \
    "v_mfma_f32_16x16x32_bf16 v[144:147], a[56:59], a[100:103], v[144:147]\n\t" \
    "v_mfma_f32_16x16x32_bf16 v[148:151], a[60:63], a[100:103], v[148:151]\n\t" \
    "v_mfma_f32_16x16x32_bf16 v[152:155], a[32:35], a[104:107], v[152:155]\n\t" \
    "v_mfma_f32_16x16x32_bf16 v[156:159], a[36:39], a[104:107], v[156:159]\n\t" \
    "v_mfma_f32_16x16x32_bf16 v[160:163], a[40:43], a[104:107], v[160:163]\n\t" \
    "v_mfma_f32_16x16x32_bf16 v[164:167], a[44:47], a[104:107], v[164:167]\n\t" \
    "v_mfma_f32_16x16x32_bf16 v[168:171], a[48:51], a[104:107], v[168:171]\n\t" \
    "v_mfma_f32_16x16x32_bf16 v[172:175], a[52:55], a[104:107], v[172:175]\n\t" \
    "v_mfma_f32_16x16x32_bf16 v[176:179], a[56:59], a[104:107], v[176:179]\n\t" \
    "v_mfma_f32_16x16x32_bf16 v[180:183], a[60:63], a[104:107], v[180:183]\n\t" \
    "s_nop 7\n\t" \
    "s_mul_i32 s45, %[osc], 0\n\t" \
    "s_add_u32 s45, s45, %[ooff]\n\t" \
    "v_cvt_pk_bf16_f32 v232, v24, v25\n\t" \
    "v_cvt_pk_bf16_f32 v233, v26, v27\n\t" \
    "v_cvt_pk_bf16_f32 v234, v28, v29\n\t" \
    "v_cvt_pk_bf16_f32 v235, v30, v31\n\t" \
    "buffer_store_dwordx4 v[232:235], %[ov], %[srda], s45 offen sc1\n\t" \
    "s_add_u32 s45, s45, %[osi]\n\t" \
    "v_cvt_pk_bf16_f32 v236, v32, v33\n\t" \
    "v_cvt_pk_bf16_f32 v237, v34, v35\n\t" \
    "v_cvt_pk_bf16_f32 v238, v36, v37\n\t" \
    "v_cvt_pk_bf16_f32 v239, v38, v39\n\t" \
    "buffer_store_dwordx4 v[236:239], %[ov], %[srda], s45 offen sc1\n\t" \
    "s_add_u32 s45, s45, %[osi]\n\t" \
    "v_cvt_pk_bf16_f32 v240, v40, v41\n\t" \
    "v_cvt_pk_bf16_f32 v241, v42, v43\n\t" \
    "v_cvt_pk_bf16_f32 v242, v44, v45\n\t" \
    "v_cvt_pk_bf16_f32 v243, v46, v47\n\t" \
    "buffer_store_dwordx4 v[240:243], %[ov], %[srda], s45 offen sc1\n\t" \
    "s_add_u32 s45, s45, %[osi]\n\t" \
    "v_cvt_pk_bf16_f32 v244, v48, v49\n\t" \
    "v_cvt_pk_bf16_f32 v245, v50, v51\n\t" \
    "v_cvt_pk_bf16_f32 v246, v52, v53\n\t" \
    "v_cvt_pk_bf16_f32 v247, v54, v55\n\t" \
    "buffer_store_dwordx4 v[244:247], %[ov], %[srda], s45 offen sc1\n\t" \
    "s_mul_i32 s45, %[osc], 2\n\t" \
    "s_add_u32 s45, s45, %[ooff]\n\t" \
    "v_cvt_pk_bf16_f32 v248, v56, v57\n\t" \
    "v_cvt_pk_bf16_f32 v249, v58, v59\n\t" \
    "v_cvt_pk_bf16_f32 v250, v60, v61\n\t" \
    "v_cvt_pk_bf16_f32 v251, v62, v63\n\t" \
    "buffer_store_dwordx4 v[248:251], %[ov], %[srda], s45 offen sc1\n\t" \
    "s_add_u32 s45, s45, %[osi]\n\t" \
    "v_cvt_pk_bf16_f32 v252, v64, v65\n\t" \
    "v_cvt_pk_bf16_f32 v253, v66, v67\n\t" \
    "v_cvt_pk_bf16_f32 v254, v68, v69\n\t" \
    "v_cvt_pk_bf16_f32 v255, v70, v71\n\t" \
    "buffer_store_dwordx4 v[252:255], %[ov], %[srda], s45 offen sc1\n\t" \
    "s_add_u32 s45, s45, %[osi]\n\t" \
    "v_cvt_pk_bf16_f32 v232, v72, v73\n\t" \
    "v_cvt_pk_bf16_f32 v233, v74, v75\n\t" \
    "v_cvt_pk_bf16_f32 v234, v76, v77\n\t" \
    "v_cvt_pk_bf16_f32 v235, v78, v79\n\t" \
    "buffer_store_dwordx4 v[232:235], %[ov], %[srda], s45 offen sc1\n\t" \
    "s_add_u32 s45, s45, %[osi]\n\t" \
    "v_cvt_pk_bf16_f32 v236, v80, v81\n\t" \
    "v_cvt_pk_bf16_f32 v237, v82, v83\n\t" \
    "v_cvt_pk_bf16_f32 v238, v84, v85\n\t" \
    "v_cvt_pk_bf16_f32 v239, v86, v87\n\t" \
    "buffer_store_dwordx4 v[236:239], %[ov], %[srda], s45 offen sc1\n\t" \
    "s_mul_i32 s45, %[osc], 4\n\t" \
    "s_add_u32 s45, s45, %[ooff]\n\t" \
    "v_cvt_pk_bf16_f32 v240, v88, v89\n\t" \
    "v_cvt_pk_bf16_f32 v241, v90, v91\n\t" \
    "v_cvt_pk_bf16_f32 v242, v92, v93\n\t" \
    "v_cvt_pk_bf16_f32 v243, v94, v95\n\t" \
    "buffer_store_dwordx4 v[240:243], %[ov], %[srda], s45 offen sc1\n\t" \
    "s_add_u32 s45, s45, %[osi]\n\t" \
    "v_cvt_pk_bf16_f32 v244, v96, v97\n\t" \
    "v_cvt_pk_bf16_f32 v245, v98, v99\n\t" \
    "v_cvt_pk_bf16_f32 v246, v100, v101\n\t" \
    "v_cvt_pk_bf16_f32 v247, v102, v103\n\t" \
    "buffer_store_dwordx4 v[244:247], %[ov], %[srda], s45 offen sc1\n\t" \
    "s_add_u32 s45, s45, %[osi]\n\t" \
    "v_cvt_pk_bf16_f32 v248, v104, v105\n\t" \
    "v_cvt_pk_bf16_f32 v249, v106, v107\n\t" \
    "v_cvt_pk_bf16_f32 v250, v108, v109\n\t" \
    "v_cvt_pk_bf16_f32 v251, v110, v111\n\t" \
    "buffer_store_dwordx4 v[248:251], %[ov], %[srda], s45 offen sc1\n\t" \
    "s_add_u32 s45, s45, %[osi]\n\t" \
    "v_cvt_pk_bf16_f32 v252, v112, v113\n\t" \
    "v_cvt_pk_bf16_f32 v253, v114, v115\n\t" \
    "v_cvt_pk_bf16_f32 v254, v116, v117\n\t" \
    "v_cvt_pk_bf16_f32 v255, v118, v119\n\t" \
    "buffer_store_dwordx4 v[252:255], %[ov], %[srda], s45 offen sc1\n\t" \
    "s_mul_i32 s45, %[osc], 6\n\t" \
    "s_add_u32 s45, s45, %[ooff]\n\t" \
    "v_cvt_pk_bf16_f32 v232, v120, v121\n\t" \
    "v_cvt_pk_bf16_f32 v233, v122, v123\n\t" \
    "v_cvt_pk_bf16_f32 v234, v124, v125\n\t" \
    "v_cvt_pk_bf16_f32 v235, v126, v127\n\t" \
    "buffer_store_dwordx4 v[232:235], %[ov], %[srda], s45 offen sc1\n\t" \
    "s_add_u32 s45, s45, %[osi]\n\t" \
    "v_cvt_pk_bf16_f32 v236, v128, v129\n\t" \
    "v_cvt_pk_bf16_f32 v237, v130, v131\n\t" \
    "v_cvt_pk_bf16_f32 v238, v132, v133\n\t" \
    "v_cvt_pk_bf16_f32 v239, v134, v135\n\t" \
    "buffer_store_dwordx4 v[236:239], %[ov], %[srda], s45 offen sc1\n\t" \
    "s_add_u32 s45, s45, %[osi]\n\t" \
    "v_cvt_pk_bf16_f32 v240, v136, v137\n\t" \
    "v_cvt_pk_bf16_f32 v241, v138, v139\n\t" \
    "v_cvt_pk_bf16_f32 v242, v140, v141\n\t" \
    "v_cvt_pk_bf16_f32 v243, v142, v143\n\t" \
    "buffer_store_dwordx4 v[240:243], %[ov], %[srda], s45 offen sc1\n\t" \
    "s_add_u32 s45, s45, %[osi]\n\t" \
    "v_cvt_pk_bf16_f32 v244, v144, v145\n\t" \
    "v_cvt_pk_bf16_f32 v245, v146, v147\n\t" \
    "v_cvt_pk_bf16_f32 v246, v148, v149\n\t" \
    "v_cvt_pk_bf16_f32 v247, v150, v151\n\t" \
    "buffer_store_dwordx4 v[244:247], %[ov], %[srda], s45 offen sc1\n\t" \
    "s_mul_i32 s45, %[osc], 8\n\t" \
    "s_add_u32 s45, s45, %[ooff]\n\t" \
    "v_cvt_pk_bf16_f32 v248, v152, v153\n\t" \
    "v_cvt_pk_bf16_f32 v249, v154, v155\n\t" \
    "v_cvt_pk_bf16_f32 v250, v156, v157\n\t" \
    "v_cvt_pk_bf16_f32 v251, v158, v159\n\t" \
    "buffer_store_dwordx4 v[248:251], %[ov], %[srda], s45 offen sc1\n\t" \
    "s_add_u32 s45, s45, %[osi]\n\t" \
    "v_cvt_pk_bf16_f32 v252, v160, v161\n\t" \
    "v_cvt_pk_bf16_f32 v253, v162, v163\n\t" \
    "v_cvt_pk_bf16_f32 v254, v164, v165\n\t" \
    "v_cvt_pk_bf16_f32 v255, v166, v167\n\t" \
    "buffer_store_dwordx4 v[252:255], %[ov], %[srda], s45 offen sc1\n\t" \
    "s_add_u32 s45, s45, %[osi]\n\t" \
    "v_cvt_pk_bf16_f32 v232, v168, v169\n\t" \
    "v_cvt_pk_bf16_f32 v233, v170, v171\n\t" \
    "v_cvt_pk_bf16_f32 v234, v172, v173\n\t" \
    "v_cvt_pk_bf16_f32 v235, v174, v175\n\t" \
    "buffer_store_dwordx4 v[232:235], %[ov], %[srda], s45 offen sc1\n\t" \
    "s_add_u32 s45, s45, %[osi]\n\t" \
    "v_cvt_pk_bf16_f32 v236, v176, v177\n\t" \
    "v_cvt_pk_bf16_f32 v237, v178, v179\n\t" \
    "v_cvt_pk_bf16_f32 v238, v180, v181\n\t" \
    "v_cvt_pk_bf16_f32 v239, v182, v183\n\t" \
    "buffer_store_dwordx4 v[236:239], %[ov], %[srda], s45 offen sc1\n\t" \
    "s_waitcnt lgkmcnt(0) vmcnt(30)\n\t" \
    "v_mfma_f32_16x16x32_bf16 v[24:27], a[0:3], a[64:67], 0\n\t" \
    "ds_read_b64_tr_b16 a[88:89], %[brd] offset:15360\n\t" \
    "v_mfma_f32_16x16x32_bf16 v[28:31], a[4:7], a[64:67], 0\n\t" \
    "ds_read_b64_tr_b16 a[90:91], %[brd] offset:16640\n\t" \
    "v_mfma_f32_16x16x32_bf16 v[32:35], a[8:11], a[64:67], 0\n\t" \
    "ds_read_b128 a[32:35], %[ard] offset:8192\n\t" \
    "v_mfma_f32_16x16x32_bf16 v[36:39], a[12:15], a[64:67], 0\n\t" \
    "ds_read_b128 a[36:39], %[ard] offset:9216\n\t" \
    "v_mfma_f32_16x16x32_bf16 v[40:43], a[16:19], a[64:67], 0\n\t" \
    "ds_read_b128 a[40:43], %[ard] offset:10240\n\t" \
    "v_mfma_f32_16x16x32_bf16 v[44:47], a[20:23], a[64:67], 0\n\t" \
    "ds_read_b128 a[44:47], %[ard] offset:11264\n\t" \
    "v_mfma_f32_16x16x32_bf16 v[48:51], a[24:27], a[64:67], 0\n\t" \
    "ds_read_b128 a[48:51], %[ard] offset:12288\n\t" \
    "v_mfma_f32_16x16x32_bf16 v[52:55], a[28:31], a[64:67], 0\n\t" \
    "ds_read_b128 a[52:55], %[ard] offset:13312\n\t" \
    "v_mfma_f32_16x16x32_bf16 v[56:59], a[0:3], a[68:71], 0\n\t" \
    "ds_read_b128 a[56:59], %[ard] offset:14336\n\t" \
    "v_mfma_f32_16x16x32_bf16 v[60:63], a[4:7], a[68:71], 0\n\t" \
    "ds_read_b128 a[60:63], %[ard] offset:15360\n\t" \
    "v_mfma_f32_16x16x32_bf16 v[64:67], a[8:11], a[68:71], 0\n\t" \
    "ds_read_b64_tr_b16 a[92:93], %[brd] offset:15616\n\t" \
    "v_mfma_f32_16x16x32_bf16 v[68:71], a[12:15], a[68:71], 0\n\t" \
    "ds_read_b64_tr_b16 a[94:95], %[brd] offset:16896\n\t" \
    "v_mfma_f32_16x16x32_bf16 v[72:75], a[16:19], a[68:71], 0\n\t" \
    "ds_read_b64_tr_b16 a[96:97], %[brd] offset:15872\n\t" \
    "v_mfma_f32_16x16x32_bf16 v[76:79], a[20:23], a[68:71], 0\n\t" \
    "ds_read_b64_tr_b16 a[98:99], %[brd] offset:17152\n\t" \
    "v_mfma_f32_16x16x32_bf16 v[80:83], a[24:27], a[68:71], 0\n\t" \
    "ds_read_b64_tr_b16 a[100:101], %[brd] offset:16128\n\t" \
    "v_mfma_f32_16x16x32_bf16 v[84:87], a[28:31], a[68:71], 0\n\t" \
    "ds_read_b64_tr_b16 a[102:103], %[brd] offset:17408\n\t" \
    "v_mfma_f32_16x16x32_bf16 v[88:91], a[0:3], a[72:75], 0\n\t" \
    "ds_read_b64_tr_b16 a[104:105], %[brd] offset:16384\n\t" \
    "v_mfma_f32_16x16x32_bf16 v[92:95], a[4:7], a[72:75], 0\n\t" \
    "ds_read_b64_tr_b16 a[106:107], %[brd] offset:17664\n\t" \
    "v_mfma_f32_16x16x32_bf16 v[96:99], a[8:11], a[72:75], 0\n\t" \
    "s_add_u32 m0, s48, 10240\n\t" \
    "s_add_u32 s46, %[boff], 164480\n\t" \
    "buffer_load_dwordx4 %[bo0], %[srdb], s46 offen lds\n\t" \
    "v_mfma_f32_16x16x32_bf16 v[100:103], a[12:15], a[72:75], 0\n\t" \
    "buffer_load_dwordx4 %[bo1], %[srdb], s46 offen offset:1024 lds\n\t" \
    "v_mfma_f32_16x16x32_bf16 v[104:107], a[16:19], a[72:75], 0\n\t" \
    "buffer_load_dwordx4 %[bo2], %[srdb], s46 offen offset:2048 lds\n\t" \
    "v_mfma_f32_16x16x32_bf16 v[108:111], a[20:23], a[72:75], 0\n\t" \
    "buffer_load_dwordx4 %[bo3], %[srdb], s46 offen offset:3072 lds\n\t" \
    "v_mfma_f32_16x16x32_bf16 v[112:115], a[24:27], a[72:75], 0\n\t" \
    "s_add_u32 m0, s48, 14336\n\t" \
    "s_nop 0\n\t" \
    "buffer_load_dwordx4 %[bo4], %[srdb], s46 offen lds\n\t" \
    "v_mfma_f32_16x16x32_bf16 v[116:119], a[28:31], a[72:75], 0\n\t" \
    "v_mfma_f32_16x16x32_bf16 v[120:123], a[0:3], a[76:79], 0\n\t" \
    "v_mfma_f32_16x16x32_bf16 v[124:127], a[4:7], a[76:79], 0\n\t" \
    "v_mfma_f32_16x16x32_bf16 v[128:131], a[8:11], a[76:79], 0\n\t" \
    "v_mfma_f32_16x16x32_bf16 v[132:135], a[12:15], a[76:79], 0\n\t" \
    "v_mfma_f32_16x16x32_bf16 v[136:139], a[16:19], a[76:79], 0\n\t" \
    "v_mfma_f32_16x16x32_bf16 v[140:143], a[20:23], a[76:79], 0\n\t" \
    "v_mfma_f32_16x16x32_bf16 v[144:147], a[24:27], a[76:79], 0\n\t" \
    "v_mfma_f32_16x16x32_bf16 v[148:151], a[28:31], a[76:79], 0\n\t" \
    "v_mfma_f32_16x16x32_bf16 v[152:155], a[0:3], a[80:83], 0\n\t" \
    "v_mfma_f32_16x16x32_bf16 v[156:159], a[4:7], a[80:83], 0\n\t" \
    "v_mfma_f32_16x16x32_bf16 v[160:163], a[8:11], a[80:83], 0\n\t" \
    "v_mfma_f32_16x16x32_bf16 v[164:167], a[12:15], a[80:83], 0\n\t" \
    "v_mfma_f32_16x16x32_bf16 v[168:171], a[16:19], a[80:83], 0\n\t" \
    "v_mfma_f32_16x16x32_bf16 v[172:175], a[20:23], a[80:83], 0\n\t" \
    "v_mfma_f32_16x16x32_bf16 v[176:179], a[24:27], a[80:83], 0\n\t" \
    "v_mfma_f32_16x16x32_bf16 v[180:183], a[28:31], a[80:83], 0\n\t" \
    "s_waitcnt lgkmcnt(0) vmcnt(30)\n\t" \
    "v_mfma_f32_16x16x32_bf16 v[24:27], a[32:35], a[88:91], v[24:27]\n\t" \
    "ds_read_b64_tr_b16 a[64:65], %[brd] offset:0\n\t" \
    "v_mfma_f32_16x16x32_bf16 v[28:31], a[36:39], a[88:91], v[28:31]\n\t" \
    "ds_read_b64_tr_b16 a[66:67], %[brd] offset:1280\n\t" \
    "v_mfma_f32_16x16x32_bf16 v[32:35], a[40:43], a[88:91], v[32:35]\n\t" \
    "ds_read_b128 a[0:3], %[ard] offset:16384\n\t" \
    "v_mfma_f32_16x16x32_bf16 v[36:39], a[44:47], a[88:91], v[36:39]\n\t" \
    "ds_read_b128 a[4:7], %[ard] offset:17408\n\t" \
    "v_mfma_f32_16x16x32_bf16 v[40:43], a[48:51], a[88:91], v[40:43]\n\t" \
    "ds_read_b128 a[8:11], %[ard] offset:18432\n\t" \
    "v_mfma_f32_16x16x32_bf16 v[44:47], a[52:55], a[88:91], v[44:47]\n\t" \
    "ds_read_b128 a[12:15], %[ard] offset:19456\n\t" \
    "v_mfma_f32_16x16x32_bf16 v[48:51], a[56:59], a[88:91], v[48:51]\n\t" \
    "ds_read_b128 a[16:19], %[ard] offset:20480\n\t" \
    "v_mfma_f32_16x16x32_bf16 v[52:55], a[60:63], a[88:91], v[52:55]\n\t" \
    "ds_read_b128 a[20:23], %[ard] offset:21504\n\t" \
    "v_mfma_f32_16x16x32_bf16 v[56:59], a[32:35], a[92:95], v[56:59]\n\t" \
    "ds_read_b128 a[24:27], %[ard] offset:22528\n\t" \
    "v_mfma_f32_16x16x32_bf16 v[60:63], a[36:39], a[92:95], v[60:63]\n\t" \
    "ds_read_b128 a[28:31], %[ard] offset:23552\n\t" \
    "v_mfma_f32_16x16x32_bf16 v[64:67], a[40:43], a[92:95], v[64:67]\n\t" \
    "ds_read_b64_tr_b16 a[68:69], %[brd] offset:256\n\t" \
    "v_mfma_f32_16x16x32_bf16 v[68:71], a[44:47], a[92:95], v[68:71]\n\t" \
    "ds_read_b64_tr_b16 a[70:71], %[brd] offset:1536\n\t" \
    "v_mfma_f32_16x16x32_bf16 v[72:75], a[48:51], a[92:95], v[72:75]\n\t" \
    "ds_read_b64_tr_b16 a[72:73], %[brd] offset:512\n\t" \
    "v_mfma_f32_16x16x32_bf16 v[76:79], a[52:55], a[92:95], v[76:79]\n\t" \
    "ds_read_b64_tr_b16 a[74:75], %[brd] offset:1792\n\t" \
    "v_mfma_f32_16x16x32_bf16 v[80:83], a[56:59], a[92:95], v[80:83]\n\t" \
    "ds_read_b64_tr_b16 a[76:77], %[brd] offset:768\n\t" \
    "v_mfma_f32_16x16x32_bf16 v[84:87], a[60:63], a[92:95], v[84:87]\n\t" \
    "ds_read_b64_tr_b16 a[78:79], %[brd] offset:2048\n\t" \
    "v_mfma_f32_16x16x32_bf16 v[88:91], a[32:35], a[96:99], v[88:91]\n\t" \
    "ds_read_b64_tr_b16 a[80:81], %[brd] offset:1024\n\t" \
    "v_mfma_f32_16x16x32_bf16 v[92:95], a[36:39], a[96:99], v[92:95]\n\t" \
    "ds_read_b64_tr_b16 a[82:83], %[brd] offset:2304\n\t" \
    "v_mfma_f32_16x16x32_bf16 v[96:99], a[40:43], a[96:99], v[96:99]\n\t" \
    "s_add_u32 m0, s48, 15360\n\t" \
    "s_add_u32 s46, %[boff], 205440\n\t" \
    "buffer_load_dwordx4 %[bo0], %[srdb], s46 offen lds\n\t" \
    "v_mfma_f32_16x16x32_bf16 v[100:103], a[44:47], a[96:99], v[100:103]\n\t" \
    "buffer_load_dwordx4 %[bo1], %[srdb], s46 offen offset:1024 lds\n\t" \
    "v_mfma_f32_16x16x32_bf16 v[104:107], a[48:51], a[96:99], v[104:107]\n\t" \
    "buffer_load_dwordx4 %[bo2], %[srdb], s46 offen offset:2048 lds\n\t" \
    "v_mfma_f32_16x16x32_bf16 v[108:111], a[52:55], a[96:99], v[108:111]\n\t" \
    "buffer_load_dwordx4 %[bo3], %[srdb], s46 offen offset:3072 lds\n\t" \
    "v_mfma_f32_16x16x32_bf16 v[112:115], a[56:59], a[96:99], v[112:115]\n\t" \
    "s_add_u32 m0, s48, 19456\n\t" \
    "s_nop 0\n\t" \
    "buffer_load_dwordx4 %[bo4], %[srdb], s46 offen lds\n\t" \
    "v_mfma_f32_16x16x32_bf16 v[116:119], a[60:63], a[96:99], v[116:119]\n\t" \
    "v_mfma_f32_16x16x32_bf16 v[120:123], a[32:35], a[100:103], v[120:123]\n\t" \
    "v_mfma_f32_16x16x32_bf16 v[124:127], a[36:39], a[100:103], v[124:127]\n\t" \
    "v_mfma_f32_16x16x32_bf16 v[128:131], a[40:43], a[100:103], v[128:131]\n\t" \
    "v_mfma_f32_16x16x32_bf16 v[132:135], a[44:47], a[100:103], v[132:135]\n\t" \
    "v_mfma_f32_16x16x32_bf16 v[136:139], a[48:51], a[100:103], v[136:139]\n\t" \
    "v_mfma_f32_16x16x32_bf16 v[140:143], a[52:55], a[100:103], v[140:143]\n\t" \
    "v_mfma_f32_16x16x32_bf16 v[144:147], a[56:59], a[100:103], v[144:147]\n\t" \
    "v_mfma_f32_16x16x32_bf16 v[148:151], a[60:63], a[100:103], v[148:151]\n\t" \
    "v_mfma_f32_16x16x32_bf16 v[152:155], a[32:35], a[104:107], v[152:155]\n\t" \
    "v_mfma_f32_16x16x32_bf16 v[156:159], a[36:39], a[104:107], v[156:159]\n\t" \
    "v_mfma_f32_16x16x32_bf16 v[160:163], a[40:43], a[104:107], v[160:163]\n\t" \
    "v_mfma_f32_16x16x32_bf16 v[164:167], a[44:47], a[104:107], v[164:167]\n\t" \
    "v_mfma_f32_16x16x32_bf16 v[168:171], a[48:51], a[104:107], v[168:171]\n\t" \
    "v_mfma_f32_16x16x32_bf16 v[172:175], a[52:55], a[104:107], v[172:175]\n\t" \
    "v_mfma_f32_16x16x32_bf16 v[176:179], a[56:59], a[104:107], v[176:179]\n\t" \
    "v_mfma_f32_16x16x32_bf16 v[180:183], a[60:63], a[104:107], v[180:183]\n\t" \
    "s_waitcnt lgkmcnt(0) vmcnt(30)\n\t" \
    "v_mfma_f32_16x16x32_bf16 v[24:27], a[0:3], a[64:67], v[24:27]\n\t" \
    "ds_read_b64_tr_b16 a[88:89], %[brd] offset:5120\n\t" \
    "v_mfma_f32_16x16x32_bf16 v[28:31], a[4:7], a[64:67], v[28:31]\n\t" \
    "ds_read_b64_tr_b16 a[90:91], %[brd] offset:6400\n\t" \
    "v_mfma_f32_16x16x32_bf16 v[32:35], a[8:11], a[64:67], v[32:35]\n\t" \
    "ds_read_b128 a[32:35], %[ard] offset:24576\n\t" \
    "v_mfma_f32_16x16x32_bf16 v[36:39], a[12:15], a[64:67], v[36:39]\n\t" \
    "ds_read_b128 a[36:39], %[ard] offset:25600\n\t" \
    "v_mfma_f32_16x16x32_bf16 v[40:43], a[16:19], a[64:67], v[40:43]\n\t" \
    "ds_read_b128 a[40:43], %[ard] offset:26624\n\t" \
    "v_mfma_f32_16x16x32_bf16 v[44:47], a[20:23], a[64:67], v[44:47]\n\t" \
    "ds_read_b128 a[44:47], %[ard] offset:27648\n\t" \
    "v_mfma_f32_16x16x32_bf16 v[48:51], a[24:27], a[64:67], v[48:51]\n\t" \
    "ds_read_b128 a[48:51], %[ard] offset:28672\n\t" \
    "v_mfma_f32_16x16x32_bf16 v[52:55], a[28:31], a[64:67], v[52:55]\n\t" \
    "ds_read_b128 a[52:55], %[ard] offset:29696\n\t" \
    "v_mfma_f32_16x16x32_bf16 v[56:59], a[0:3], a[68:71], v[56:59]\n\t" \
    "ds_read_b128 a[56:59], %[ard] offset:30720\n\t" \
    "v_mfma_f32_16x16x32_bf16 v[60:63], a[4:7], a[68:71], v[60:63]\n\t" \
    "ds_read_b128 a[60:63], %[ard] offset:31744\n\t" \
    "v_mfma_f32_16x16x32_bf16 v[64:67], a[8:11], a[68:71], v[64:67]\n\t" \
    "ds_read_b64_tr_b16 a[92:93], %[brd] offset:5376\n\t" \
    "v_mfma_f32_16x16x32_bf16 v[68:71], a[12:15], a[68:71], v[68:71]\n\t" \
    "ds_read_b64_tr_b16 a[94:95], %[brd] offset:6656\n\t" \
    "v_mfma_f32_16x16x32_bf16 v[72:75], a[16:19], a[68:71], v[72:75]\n\t" \
    "ds_read_b64_tr_b16 a[96:97], %[brd] offset:5632\n\t" \
    "v_mfma_f32_16x16x32_bf16 v[76:79], a[20:23], a[68:71], v[76:79]\n\t" \
    "ds_read_b64_tr_b16 a[98:99], %[brd] offset:6912\n\t" \
    "v_mfma_f32_16x16x32_bf16 v[80:83], a[24:27], a[68:71], v[80:83]\n\t" \
    "ds_read_b64_tr_b16 a[100:101], %[brd] offset:5888\n\t" \
    "v_mfma_f32_16x16x32_bf16 v[84:87], a[28:31], a[68:71], v[84:87]\n\t" \
    "ds_read_b64_tr_b16 a[102:103], %[brd] offset:7168\n\t" \
    "v_mfma_f32_16x16x32_bf16 v[88:91], a[0:3], a[72:75], v[88:91]\n\t" \
    "ds_read_b64_tr_b16 a[104:105], %[brd] offset:6144\n\t" \
    "v_mfma_f32_16x16x32_bf16 v[92:95], a[4:7], a[72:75], v[92:95]\n\t" \
    "ds_read_b64_tr_b16 a[106:107], %[brd] offset:7424\n\t" \
    "v_mfma_f32_16x16x32_bf16 v[96:99], a[8:11], a[72:75], v[96:99]\n\t" \
    "v_mfma_f32_16x16x32_bf16 v[100:103], a[12:15], a[72:75], v[100:103]\n\t" \
    "v_mfma_f32_16x16x32_bf16 v[104:107], a[16:19], a[72:75], v[104:107]\n\t" \
    "v_mfma_f32_16x16x32_bf16 v[108:111], a[20:23], a[72:75], v[108:111]\n\t" \
    "v_mfma_f32_16x16x32_bf16 v[112:115], a[24:27], a[72:75], v[112:115]\n\t" \
    "v_mfma_f32_16x16x32_bf16 v[116:119], a[28:31], a[72:75], v[116:119]\n\t" \
    "v_mfma_f32_16x16x32_bf16 v[120:123], a[0:3], a[76:79], v[120:123]\n\t" \
    "v_mfma_f32_16x16x32_bf16 v[124:127], a[4:7], a[76:79], v[124:127]\n\t" \
    "v_mfma_f32_16x16x32_bf16 v[128:131], a[8:11], a[76:79], v[128:131]\n\t" \
    "v_mfma_f32_16x16x32_bf16 v[132:135], a[12:15], a[76:79], v[132:135]\n\t" \
    "v_mfma_f32_16x16x32_bf16 v[136:139], a[16:19], a[76:79], v[136:139]\n\t" \
    "v_mfma_f32_16x16x32_bf16 v[140:143], a[20:23], a[76:79], v[140:143]\n\t" \
    "v_mfma_f32_16x16x32_bf16 v[144:147], a[24:27], a[76:79], v[144:147]\n\t" \
    "v_mfma_f32_16x16x32_bf16 v[148:151], a[28:31], a[76:79], v[148:151]\n\t" \
    "v_mfma_f32_16x16x32_bf16 v[152:155], a[0:3], a[80:83], v[152:155]\n\t" \
    "v_mfma_f32_16x16x32_bf16 v[156:159], a[4:7], a[80:83], v[156:159]\n\t" \
    "v_mfma_f32_16x16x32_bf16 v[160:163], a[8:11], a[80:83], v[160:163]\n\t" \
    "v_mfma_f32_16x16x32_bf16 v[164:167], a[12:15], a[80:83], v[164:167]\n\t" \
    "v_mfma_f32_16x16x32_bf16 v[168:171], a[16:19], a[80:83], v[168:171]\n\t" \
    "v_mfma_f32_16x16x32_bf16 v[172:175], a[20:23], a[80:83], v[172:175]\n\t" \
    "v_mfma_f32_16x16x32_bf16 v[176:179], a[24:27], a[80:83], v[176:179]\n\t" \
    "v_mfma_f32_16x16x32_bf16 v[180:183], a[28:31], a[80:83], v[180:183]\n\t" \
    "s_waitcnt lgkmcnt(0) vmcnt(5)\n\t" \
    "v_mfma_f32_16x16x32_bf16 v[24:27], a[32:35], a[88:91], v[24:27]\n\t" \
    "ds_read_b64_tr_b16 a[64:65], %[brd] offset:10240\n\t" \
    "v_mfma_f32_16x16x32_bf16 v[28:31], a[36:39], a[88:91], v[28:31]\n\t" \
    "ds_read_b64_tr_b16 a[66:67], %[brd] offset:11520\n\t" \
    "v_mfma_f32_16x16x32_bf16 v[32:35], a[40:43], a[88:91], v[32:35]\n\t" \
    "ds_read_b128 a[0:3], %[ard] offset:32768\n\t" \
    "v_mfma_f32_16x16x32_bf16 v[36:39], a[44:47], a[88:91], v[36:39]\n\t" \
    "ds_read_b128 a[4:7], %[ard] offset:33792\n\t" \
    "v_mfma_f32_16x16x32_bf16 v[40:43], a[48:51], a[88:91], v[40:43]\n\t" \
    "ds_read_b128 a[8:11], %[ard] offset:34816\n\t" \
    "v_mfma_f32_16x16x32_bf16 v[44:47], a[52:55], a[88:91], v[44:47]\n\t" \
    "ds_read_b128 a[12:15], %[ard] offset:35840\n\t" \
    "v_mfma_f32_16x16x32_bf16 v[48:51], a[56:59], a[88:91], v[48:51]\n\t" \
    "ds_read_b128 a[16:19], %[ard] offset:36864\n\t" \
    "v_mfma_f32_16x16x32_bf16 v[52:55], a[60:63], a[88:91], v[52:55]\n\t" \
    "ds_read_b128 a[20:23], %[ard] offset:37888\n\t" \
    "v_mfma_f32_16x16x32_bf16 v[56:59], a[32:35], a[92:95], v[56:59]\n\t" \
    "ds_read_b128 a[24:27], %[ard] offset:38912\n\t" \
    "v_mfma_f32_16x16x32_bf16 v[60:63], a[36:39], a[92:95], v[60:63]\n\t" \
    "ds_read_b128 a[28:31], %[ard] offset:39936\n\t" \
    "v_mfma_f32_16x16x32_bf16 v[64:67], a[40:43], a[92:95], v[64:67]\n\t" \
    "ds_read_b64_tr_b16 a[68:69], %[brd] offset:10496\n\t" \
    "v_mfma_f32_16x16x32_bf16 v[68:71], a[44:47], a[92:95], v[68:71]\n\t" \
    "ds_read_b64_tr_b16 a[70:71], %[brd] offset:11776\n\t" \
    "v_mfma_f32_16x16x32_bf16 v[72:75], a[48:51], a[92:95], v[72:75]\n\t" \
    "ds_read_b64_tr_b16 a[72:73], %[brd] offset:10752\n\t" \
    "v_mfma_f32_16x16x32_bf16 v[76:79], a[52:55], a[92:95], v[76:79]\n\t" \
    "ds_read_b64_tr_b16 a[74:75], %[brd] offset:12032\n\t" \
    "v_mfma_f32_16x16x32_bf16 v[80:83], a[56:59], a[92:95], v[80:83]\n\t" \
    "ds_read_b64_tr_b16 a[76:77], %[brd] offset:11008\n\t" \
    "v_mfma_f32_16x16x32_bf16 v[84:87], a[60:63], a[92:95], v[84:87]\n\t" \
    "ds_read_b64_tr_b16 a[78:79], %[brd] offset:12288\n\t" \
    "v_mfma_f32_16x16x32_bf16 v[88:91], a[32:35], a[96:99], v[88:91]\n\t" \
    "ds_read_b64_tr_b16 a[80:81], %[brd] offset:11264\n\t" \
    "v_mfma_f32_16x16x32_bf16 v[92:95], a[36:39], a[96:99], v[92:95]\n\t" \
    "ds_read_b64_tr_b16 a[82:83], %[brd] offset:12544\n\t" \
    "v_mfma_f32_16x16x32_bf16 v[96:99], a[40:43], a[96:99], v[96:99]\n\t" \
    "v_mfma_f32_16x16x32_bf16 v[100:103], a[44:47], a[96:99], v[100:103]\n\t" \
    "v_mfma_f32_16x16x32_bf16 v[104:107], a[48:51], a[96:99], v[104:107]\n\t" \
    "v_mfma_f32_16x16x32_bf16 v[108:111], a[52:55], a[96:99], v[108:111]\n\t" \
    "v_mfma_f32_16x16x32_bf16 v[112:115], a[56:59], a[96:99], v[112:115]\n\t" \
    "v_mfma_f32_16x16x32_bf16 v[116:119], a[60:63], a[96:99], v[116:119]\n\t" \
    "v_mfma_f32_16x16x32_bf16 v[120:123], a[32:35], a[100:103], v[120:123]\n\t" \
    "v_mfma_f32_16x16x32_bf16 v[124:127], a[36:39], a[100:103], v[124:127]\n\t" \
    "v_mfma_f32_16x16x32_bf16 v[128:131], a[40:43], a[100:103], v[128:131]\n\t" \
    "v_mfma_f32_16x16x32_bf16 v[132:135], a[44:47], a[100:103], v[132:135]\n\t" \
    "v_mfma_f32_16x16x32_bf16 v[136:139], a[48:51], a[100:103], v[136:139]\n\t" \
    "v_mfma_f32_16x16x32_bf16 v[140:143], a[52:55], a[100:103], v[140:143]\n\t" \
    "v_mfma_f32_16x16x32_bf16 v[144:147], a[56:59], a[100:103], v[144:147]\n\t" \
    "v_mfma_f32_16x16x32_bf16 v[148:151], a[60:63], a[100:103], v[148:151]\n\t" \
    "v_mfma_f32_16x16x32_bf16 v[152:155], a[32:35], a[104:107], v[152:155]\n\t" \
    "v_mfma_f32_16x16x32_bf16 v[156:159], a[36:39], a[104:107], v[156:159]\n\t" \
    "v_mfma_f32_16x16x32_bf16 v[160:163], a[40:43], a[104:107], v[160:163]\n\t" \
    "v_mfma_f32_16x16x32_bf16 v[164:167], a[44:47], a[104:107], v[164:167]\n\t" \
    "v_mfma_f32_16x16x32_bf16 v[168:171], a[48:51], a[104:107], v[168:171]\n\t" \
    "v_mfma_f32_16x16x32_bf16 v[172:175], a[52:55], a[104:107], v[172:175]\n\t" \
    "v_mfma_f32_16x16x32_bf16 v[176:179], a[56:59], a[104:107], v[176:179]\n\t" \
    "v_mfma_f32_16x16x32_bf16 v[180:183], a[60:63], a[104:107], v[180:183]\n\t" \
    "s_waitcnt lgkmcnt(0) vmcnt(0)\n\t" \
    "v_mfma_f32_16x16x32_bf16 v[24:27], a[0:3], a[64:67], v[24:27]\n\t" \
    "ds_read_b64_tr_b16 a[88:89], %[brd] offset:15360\n\t" \
    "v_mfma_f32_16x16x32_bf16 v[28:31], a[4:7], a[64:67], v[28:31]\n\t" \
    "ds_read_b64_tr_b16 a[90:91], %[brd] offset:16640\n\t" \
    "v_mfma_f32_16x16x32_bf16 v[32:35], a[8:11], a[64:67], v[32:35]\n\t" \
    "ds_read_b128 a[32:35], %[ard] offset:40960\n\t" \
    "v_mfma_f32_16x16x32_bf16 v[36:39], a[12:15], a[64:67], v[36:39]\n\t" \
    "ds_read_b128 a[36:39], %[ard] offset:41984\n\t" \
    "v_mfma_f32_16x16x32_bf16 v[40:43], a[16:19], a[64:67], v[40:43]\n\t" \
    "ds_read_b128 a[40:43], %[ard] offset:43008\n\t" \
    "v_mfma_f32_16x16x32_bf16 v[44:47], a[20:23], a[64:67], v[44:47]\n\t" \
    "ds_read_b128 a[44:47], %[ard] offset:44032\n\t" \
    "v_mfma_f32_16x16x32_bf16 v[48:51], a[24:27], a[64:67], v[48:51]\n\t" \
    "ds_read_b128 a[48:51], %[ard] offset:45056\n\t" \
    "v_mfma_f32_16x16x32_bf16 v[52:55], a[28:31], a[64:67], v[52:55]\n\t" \
    "ds_read_b128 a[52:55], %[ard] offset:46080\n\t" \
    "v_mfma_f32_16x16x32_bf16 v[56:59], a[0:3], a[68:71], v[56:59]\n\t" \
    "ds_read_b128 a[56:59], %[ard] offset:47104\n\t" \
    "v_mfma_f32_16x16x32_bf16 v[60:63], a[4:7], a[68:71], v[60:63]\n\t" \
    "ds_read_b128 a[60:63], %[ard] offset:48128\n\t" \
    "v_mfma_f32_16x16x32_bf16 v[64:67], a[8:11], a[68:71], v[64:67]\n\t" \
    "ds_read_b64_tr_b16 a[92:93], %[brd] offset:15616\n\t" \
    "v_mfma_f32_16x16x32_bf16 v[68:71], a[12:15], a[68:71], v[68:71]\n\t" \
    "ds_read_b64_tr_b16 a[94:95], %[brd] offset:16896\n\t" \
    "v_mfma_f32_16x16x32_bf16 v[72:75], a[16:19], a[68:71], v[72:75]\n\t" \
    "ds_read_b64_tr_b16 a[96:97], %[brd] offset:15872\n\t" \
    "v_mfma_f32_16x16x32_bf16 v[76:79], a[20:23], a[68:71], v[76:79]\n\t" \
    "ds_read_b64_tr_b16 a[98:99], %[brd] offset:17152\n\t" \
    "v_mfma_f32_16x16x32_bf16 v[80:83], a[24:27], a[68:71], v[80:83]\n\t" \
    "ds_read_b64_tr_b16 a[100:101], %[brd] offset:16128\n\t" \
    "v_mfma_f32_16x16x32_bf16 v[84:87], a[28:31], a[68:71], v[84:87]\n\t" \
    "ds_read_b64_tr_b16 a[102:103], %[brd] offset:17408\n\t" \
    "v_mfma_f32_16x16x32_bf16 v[88:91], a[0:3], a[72:75], v[88:91]\n\t" \
    "ds_read_b64_tr_b16 a[104:105], %[brd] offset:16384\n\t" \
    "v_mfma_f32_16x16x32_bf16 v[92:95], a[4:7], a[72:75], v[92:95]\n\t" \
    "ds_read_b64_tr_b16 a[106:107], %[brd] offset:17664\n\t" \
    "v_mfma_f32_16x16x32_bf16 v[96:99], a[8:11], a[72:75], v[96:99]\n\t" \
    "v_mfma_f32_16x16x32_bf16 v[100:103], a[12:15], a[72:75], v[100:103]\n\t" \
    "v_mfma_f32_16x16x32_bf16 v[104:107], a[16:19], a[72:75], v[104:107]\n\t" \
    "v_mfma_f32_16x16x32_bf16 v[108:111], a[20:23], a[72:75], v[108:111]\n\t" \
    "v_mfma_f32_16x16x32_bf16 v[112:115], a[24:27], a[72:75], v[112:115]\n\t" \
    "v_mfma_f32_16x16x32_bf16 v[116:119], a[28:31], a[72:75], v[116:119]\n\t" \
    "v_mfma_f32_16x16x32_bf16 v[120:123], a[0:3], a[76:79], v[120:123]\n\t" \
    "v_mfma_f32_16x16x32_bf16 v[124:127], a[4:7], a[76:79], v[124:127]\n\t" \
    "v_mfma_f32_16x16x32_bf16 v[128:131], a[8:11], a[76:79], v[128:131]\n\t" \
    "v_mfma_f32_16x16x32_bf16 v[132:135], a[12:15], a[76:79], v[132:135]\n\t" \
    "v_mfma_f32_16x16x32_bf16 v[136:139], a[16:19], a[76:79], v[136:139]\n\t" \
    "v_mfma_f32_16x16x32_bf16 v[140:143], a[20:23], a[76:79], v[140:143]\n\t" \
    "v_mfma_f32_16x16x32_bf16 v[144:147], a[24:27], a[76:79], v[144:147]\n\t" \
    "v_mfma_f32_16x16x32_bf16 v[148:151], a[28:31], a[76:79], v[148:151]\n\t" \
    "v_mfma_f32_16x16x32_bf16 v[152:155], a[0:3], a[80:83], v[152:155]\n\t" \
    "v_mfma_f32_16x16x32_bf16 v[156:159], a[4:7], a[80:83], v[156:159]\n\t" \
    "v_mfma_f32_16x16x32_bf16 v[160:163], a[8:11], a[80:83], v[160:163]\n\t" \
    "v_mfma_f32_16x16x32_bf16 v[164:167], a[12:15], a[80:83], v[164:167]\n\t" \
    "v_mfma_f32_16x16x32_bf16 v[168:171], a[16:19], a[80:83], v[168:171]\n\t" \
    "v_mfma_f32_16x16x32_bf16 v[172:175], a[20:23], a[80:83], v[172:175]\n\t" \
    "v_mfma_f32_16x16x32_bf16 v[176:179], a[24:27], a[80:83], v[176:179]\n\t" \
    "v_mfma_f32_16x16x32_bf16 v[180:183], a[28:31], a[80:83], v[180:183]\n\t" \
    "s_waitcnt lgkmcnt(0)\n\t" \
    "v_mfma_f32_16x16x32_bf16 v[24:27], a[32:35], a[88:91], v[24:27]\n\t" \
    "v_mfma_f32_16x16x32_bf16 v[28:31], a[36:39], a[88:91], v[28:31]\n\t" \
    "v_mfma_f32_16x16x32_bf16 v[32:35], a[40:43], a[88:91], v[32:35]\n\t" \
    "v_mfma_f32_16x16x32_bf16 v[36:39], a[44:47], a[88:91], v[36:39]\n\t" \
    "v_mfma_f32_16x16x32_bf16 v[40:43], a[48:51], a[88:91], v[40:43]\n\t" \
    "v_mfma_f32_16x16x32_bf16 v[44:47], a[52:55], a[88:91], v[44:47]\n\t" \
    "v_mfma_f32_16x16x32_bf16 v[48:51], a[56:59], a[88:91], v[48:51]\n\t" \
    "v_mfma_f32_16x16x32_bf16 v[52:55], a[60:63], a[88:91], v[52:55]\n\t" \
    "v_mfma_f32_16x16x32_bf16 v[56:59], a[32:35], a[92:95], v[56:59]\n\t" \
    "v_mfma_f32_16x16x32_bf16 v[60:63], a[36:39], a[92:95], v[60:63]\n\t" \
    "v_mfma_f32_16x16x32_bf16 v[64:67], a[40:43], a[92:95], v[64:67]\n\t" \
    "v_mfma_f32_16x16x32_bf16 v[68:71], a[44:47], a[92:95], v[68:71]\n\t" \
    "v_mfma_f32_16x16x32_bf16 v[72:75], a[48:51], a[92:95], v[72:75]\n\t" \
    "v_mfma_f32_16x16x32_bf16 v[76:79], a[52:55], a[92:95], v[76:79]\n\t" \
    "v_mfma_f32_16x16x32_bf16 v[80:83], a[56:59], a[92:95], v[80:83]\n\t" \
    "v_mfma_f32_16x16x32_bf16 v[84:87], a[60:63], a[92:95], v[84:87]\n\t" \
    "v_mfma_f32_16x16x32_bf16 v[88:91], a[32:35], a[96:99], v[88:91]\n\t" \
    "v_mfma_f32_16x16x32_bf16 v[92:95], a[36:39], a[96:99], v[92:95]\n\t" \
    "v_mfma_f32_16x16x32_bf16 v[96:99], a[40:43], a[96:99], v[96:99]\n\t" \
    "v_mfma_f32_16x16x32_bf16 v[100:103], a[44:47], a[96:99], v[100:103]\n\t" \
    "v_mfma_f32_16x16x32_bf16 v[104:107], a[48:51], a[96:99], v[104:107]\n\t" \
    "v_mfma_f32_16x16x32_bf16 v[108:111], a[52:55], a[96:99], v[108:111]\n\t" \
    "v_mfma_f32_16x16x32_bf16 v[112:115], a[56:59], a[96:99], v[112:115]\n\t" \
    "v_mfma_f32_16x16x32_bf16 v[116:119], a[60:63], a[96:99], v[116:119]\n\t" \
    "v_mfma_f32_16x16x32_bf16 v[120:123], a[32:35], a[100:103], v[120:123]\n\t" \
    "v_mfma_f32_16x16x32_bf16 v[124:127], a[36:39], a[100:103], v[124:127]\n\t" \
    "v_mfma_f32_16x16x32_bf16 v[128:131], a[40:43], a[100:103], v[128:131]\n\t" \
    "v_mfma_f32_16x16x32_bf16 v[132:135], a[44:47], a[100:103], v[132:135]\n\t" \
    "v_mfma_f32_16x16x32_bf16 v[136:139], a[48:51], a[100:103], v[136:139]\n\t" \
    "v_mfma_f32_16x16x32_bf16 v[140:143], a[52:55], a[100:103], v[140:143]\n\t" \
    "v_mfma_f32_16x16x32_bf16 v[144:147], a[56:59], a[100:103], v[144:147]\n\t" \
    "v_mfma_f32_16x16x32_bf16 v[148:151], a[60:63], a[100:103], v[148:151]\n\t" \
    "v_mfma_f32_16x16x32_bf16 v[152:155], a[32:35], a[104:107], v[152:155]\n\t" \
    "v_mfma_f32_16x16x32_bf16 v[156:159], a[36:39], a[104:107], v[156:159]\n\t" \
    "v_mfma_f32_16x16x32_bf16 v[160:163], a[40:43], a[104:107], v[160:163]\n\t" \
    "v_mfma_f32_16x16x32_bf16 v[164:167], a[44:47], a[104:107], v[164:167]\n\t" \
    "v_mfma_f32_16x16x32_bf16 v[168:171], a[48:51], a[104:107], v[168:171]\n\t" \
    "v_mfma_f32_16x16x32_bf16 v[172:175], a[52:55], a[104:107], v[172:175]\n\t" \
    "v_mfma_f32_16x16x32_bf16 v[176:179], a[56:59], a[104:107], v[176:179]\n\t" \
    "v_mfma_f32_16x16x32_bf16 v[180:183], a[60:63], a[104:107], v[180:183]\n\t" \
    "s_nop 7\n\t" \
    "s_mul_i32 s45, %[osc], 40\n\t" \
    "s_add_u32 s45, s45, %[ooff]\n\t" \
    "v_cvt_pk_bf16_f32 v232, v24, v25\n\t" \
    "v_cvt_pk_bf16_f32 v233, v26, v27\n\t" \
    "v_cvt_pk_bf16_f32 v234, v28, v29\n\t" \
    "v_cvt_pk_bf16_f32 v235, v30, v31\n\t" \
    "buffer_store_dwordx4 v[232:235], %[ov], %[srda], s45 offen sc1\n\t" \
    "s_add_u32 s45, s45, %[osi]\n\t" \
    "v_cvt_pk_bf16_f32 v236, v32, v33\n\t" \
    "v_cvt_pk_bf16_f32 v237, v34, v35\n\t" \
    "v_cvt_pk_bf16_f32 v238, v36, v37\n\t" \
    "v_cvt_pk_bf16_f32 v239, v38, v39\n\t" \
    "buffer_store_dwordx4 v[236:239], %[ov], %[srda], s45 offen sc1\n\t" \
    "s_add_u32 s45, s45, %[osi]\n\t" \
    "v_cvt_pk_bf16_f32 v240, v40, v41\n\t" \
    "v_cvt_pk_bf16_f32 v241, v42, v43\n\t" \
    "v_cvt_pk_bf16_f32 v242, v44, v45\n\t" \
    "v_cvt_pk_bf16_f32 v243, v46, v47\n\t" \
    "buffer_store_dwordx4 v[240:243], %[ov], %[srda], s45 offen sc1\n\t" \
    "s_add_u32 s45, s45, %[osi]\n\t" \
    "v_cvt_pk_bf16_f32 v244, v48, v49\n\t" \
    "v_cvt_pk_bf16_f32 v245, v50, v51\n\t" \
    "v_cvt_pk_bf16_f32 v246, v52, v53\n\t" \
    "v_cvt_pk_bf16_f32 v247, v54, v55\n\t" \
    "buffer_store_dwordx4 v[244:247], %[ov], %[srda], s45 offen sc1\n\t" \
    "s_mul_i32 s45, %[osc], 42\n\t" \
    "s_add_u32 s45, s45, %[ooff]\n\t" \
    "v_cvt_pk_bf16_f32 v248, v56, v57\n\t" \
    "v_cvt_pk_bf16_f32 v249, v58, v59\n\t" \
    "v_cvt_pk_bf16_f32 v250, v60, v61\n\t" \
    "v_cvt_pk_bf16_f32 v251, v62, v63\n\t" \
    "buffer_store_dwordx4 v[248:251], %[ov], %[srda], s45 offen sc1\n\t" \
    "s_add_u32 s45, s45, %[osi]\n\t" \
    "v_cvt_pk_bf16_f32 v252, v64, v65\n\t" \
    "v_cvt_pk_bf16_f32 v253, v66, v67\n\t" \
    "v_cvt_pk_bf16_f32 v254, v68, v69\n\t" \
    "v_cvt_pk_bf16_f32 v255, v70, v71\n\t" \
    "buffer_store_dwordx4 v[252:255], %[ov], %[srda], s45 offen sc1\n\t" \
    "s_add_u32 s45, s45, %[osi]\n\t" \
    "v_cvt_pk_bf16_f32 v232, v72, v73\n\t" \
    "v_cvt_pk_bf16_f32 v233, v74, v75\n\t" \
    "v_cvt_pk_bf16_f32 v234, v76, v77\n\t" \
    "v_cvt_pk_bf16_f32 v235, v78, v79\n\t" \
    "buffer_store_dwordx4 v[232:235], %[ov], %[srda], s45 offen sc1\n\t" \
    "s_add_u32 s45, s45, %[osi]\n\t" \
    "v_cvt_pk_bf16_f32 v236, v80, v81\n\t" \
    "v_cvt_pk_bf16_f32 v237, v82, v83\n\t" \
    "v_cvt_pk_bf16_f32 v238, v84, v85\n\t" \
    "v_cvt_pk_bf16_f32 v239, v86, v87\n\t" \
    "buffer_store_dwordx4 v[236:239], %[ov], %[srda], s45 offen sc1\n\t" \
    "s_mul_i32 s45, %[osc], 44\n\t" \
    "s_add_u32 s45, s45, %[ooff]\n\t" \
    "v_cvt_pk_bf16_f32 v240, v88, v89\n\t" \
    "v_cvt_pk_bf16_f32 v241, v90, v91\n\t" \
    "v_cvt_pk_bf16_f32 v242, v92, v93\n\t" \
    "v_cvt_pk_bf16_f32 v243, v94, v95\n\t" \
    "buffer_store_dwordx4 v[240:243], %[ov], %[srda], s45 offen sc1\n\t" \
    "s_add_u32 s45, s45, %[osi]\n\t" \
    "v_cvt_pk_bf16_f32 v244, v96, v97\n\t" \
    "v_cvt_pk_bf16_f32 v245, v98, v99\n\t" \
    "v_cvt_pk_bf16_f32 v246, v100, v101\n\t" \
    "v_cvt_pk_bf16_f32 v247, v102, v103\n\t" \
    "buffer_store_dwordx4 v[244:247], %[ov], %[srda], s45 offen sc1\n\t" \
    "s_add_u32 s45, s45, %[osi]\n\t" \
    "v_cvt_pk_bf16_f32 v248, v104, v105\n\t" \
    "v_cvt_pk_bf16_f32 v249, v106, v107\n\t" \
    "v_cvt_pk_bf16_f32 v250, v108, v109\n\t" \
    "v_cvt_pk_bf16_f32 v251, v110, v111\n\t" \
    "buffer_store_dwordx4 v[248:251], %[ov], %[srda], s45 offen sc1\n\t" \
    "s_add_u32 s45, s45, %[osi]\n\t" \
    "v_cvt_pk_bf16_f32 v252, v112, v113\n\t" \
    "v_cvt_pk_bf16_f32 v253, v114, v115\n\t" \
    "v_cvt_pk_bf16_f32 v254, v116, v117\n\t" \
    "v_cvt_pk_bf16_f32 v255, v118, v119\n\t" \
    "buffer_store_dwordx4 v[252:255], %[ov], %[srda], s45 offen sc1\n\t" \
    "s_mul_i32 s45, %[osc], 46\n\t" \
    "s_add_u32 s45, s45, %[ooff]\n\t" \
    "v_cvt_pk_bf16_f32 v232, v120, v121\n\t" \
    "v_cvt_pk_bf16_f32 v233, v122, v123\n\t" \
    "v_cvt_pk_bf16_f32 v234, v124, v125\n\t" \
    "v_cvt_pk_bf16_f32 v235, v126, v127\n\t" \
    "buffer_store_dwordx4 v[232:235], %[ov], %[srda], s45 offen sc1\n\t" \
    "s_add_u32 s45, s45, %[osi]\n\t" \
    "v_cvt_pk_bf16_f32 v236, v128, v129\n\t" \
    "v_cvt_pk_bf16_f32 v237, v130, v131\n\t" \
    "v_cvt_pk_bf16_f32 v238, v132, v133\n\t" \
    "v_cvt_pk_bf16_f32 v239, v134, v135\n\t" \
    "buffer_store_dwordx4 v[236:239], %[ov], %[srda], s45 offen sc1\n\t" \
    "s_add_u32 s45, s45, %[osi]\n\t" \
    "v_cvt_pk_bf16_f32 v240, v136, v137\n\t" \
    "v_cvt_pk_bf16_f32 v241, v138, v139\n\t" \
    "v_cvt_pk_bf16_f32 v242, v140, v141\n\t" \
    "v_cvt_pk_bf16_f32 v243, v142, v143\n\t" \
    "buffer_store_dwordx4 v[240:243], %[ov], %[srda], s45 offen sc1\n\t" \
    "s_add_u32 s45, s45, %[osi]\n\t" \
    "v_cvt_pk_bf16_f32 v244, v144, v145\n\t" \
    "v_cvt_pk_bf16_f32 v245, v146, v147\n\t" \
    "v_cvt_pk_bf16_f32 v246, v148, v149\n\t" \
    "v_cvt_pk_bf16_f32 v247, v150, v151\n\t" \
    "buffer_store_dwordx4 v[244:247], %[ov], %[srda], s45 offen sc1\n\t" \
    "s_mul_i32 s45, %[osc], 48\n\t" \
    "s_add_u32 s45, s45, %[ooff]\n\t" \
    "v_cvt_pk_bf16_f32 v248, v152, v153\n\t" \
    "v_cvt_pk_bf16_f32 v249, v154, v155\n\t" \
    "v_cvt_pk_bf16_f32 v250, v156, v157\n\t" \
    "v_cvt_pk_bf16_f32 v251, v158, v159\n\t" \
    "buffer_store_dwordx4 v[248:251], %[ov], %[srda], s45 offen sc1\n\t" \
    "s_add_u32 s45, s45, %[osi]\n\t" \
    "v_cvt_pk_bf16_f32 v252, v160, v161\n\t" \
    "v_cvt_pk_bf16_f32 v253, v162, v163\n\t" \
    "v_cvt_pk_bf16_f32 v254, v164, v165\n\t" \
    "v_cvt_pk_bf16_f32 v255, v166, v167\n\t" \
    "buffer_store_dwordx4 v[252:255], %[ov], %[srda], s45 offen sc1\n\t" \
    "s_add_u32 s45, s45, %[osi]\n\t" \
    "v_cvt_pk_bf16_f32 v232, v168, v169\n\t" \
    "v_cvt_pk_bf16_f32 v233, v170, v171\n\t" \
    "v_cvt_pk_bf16_f32 v234, v172, v173\n\t" \
    "v_cvt_pk_bf16_f32 v235, v174, v175\n\t" \
    "buffer_store_dwordx4 v[232:235], %[ov], %[srda], s45 offen sc1\n\t" \
    "s_add_u32 s45, s45, %[osi]\n\t" \
    "v_cvt_pk_bf16_f32 v236, v176, v177\n\t" \
    "v_cvt_pk_bf16_f32 v237, v178, v179\n\t" \
    "v_cvt_pk_bf16_f32 v238, v180, v181\n\t" \
    "v_cvt_pk_bf16_f32 v239, v182, v183\n\t" \
    "buffer_store_dwordx4 v[236:239], %[ov], %[srda], s45 offen sc1\n\t"
#define RK_ASM_GU1 \
    "s_nop 4\n\t" \
    "s_mov_b32 s48, %[bm]\n\t" \
    "s_add_u32 m0, s48, 0\n\t" \
    "s_add_u32 s46, %[boff], 0\n\t" \
    "buffer_load_dwordx4 %[bo0], %[srdb], s46 offen lds\n\t" \
    "buffer_load_dwordx4 %[bo1], %[srdb], s46 offen offset:1024 lds\n\t" \
    "buffer_load_dwordx4 %[bo2], %[srdb], s46 offen offset:2048 lds\n\t" \
    "buffer_load_dwordx4 %[bo3], %[srdb], s46 offen offset:3072 lds\n\t" \
    "s_add_u32 m0, s48, 4096\n\t" \
    "s_nop 0\n\t" \
    "buffer_load_dwordx4 %[bo4], %[srdb], s46 offen lds\n\t" \
    "s_add_u32 m0, s48, 5120\n\t" \
    "s_add_u32 s46, %[boff], 40960\n\t" \
    "buffer_load_dwordx4 %[bo0], %[srdb], s46 offen lds\n\t" \
    "buffer_load_dwordx4 %[bo1], %[srdb], s46 offen offset:1024 lds\n\t" \
    "buffer_load_dwordx4 %[bo2], %[srdb], s46 offen offset:2048 lds\n\t" \
    "buffer_load_dwordx4 %[bo3], %[srdb], s46 offen offset:3072 lds\n\t" \
    "s_add_u32 m0, s48, 9216\n\t" \
    "s_nop 0\n\t" \
    "buffer_load_dwordx4 %[bo4], %[srdb], s46 offen lds\n\t" \
    "s_add_u32 m0, s48, 10240\n\t" \
    "s_add_u32 s46, %[boff], 81920\n\t" \
    "buffer_load_dwordx4 %[bo0], %[srdb], s46 offen lds\n\t" \
    "buffer_load_dwordx4 %[bo1], %[srdb], s46 offen offset:1024 lds\n\t" \
    "buffer_load_dwordx4 %[bo2], %[srdb], s46 offen offset:2048 lds\n\t" \
    "buffer_load_dwordx4 %[bo3], %[srdb], s46 offen offset:3072 lds\n\t" \
    "s_add_u32 m0, s48, 14336\n\t" \
    "s_nop 0\n\t" \
    "buffer_load_dwordx4 %[bo4], %[srdb], s46 offen lds\n\t" \
    "s_add_u32 m0, s48, 15360\n\t" \
    "s_add_u32 s46, %[boff], 122880\n\t" \
    "buffer_load_dwordx4 %[bo0], %[srdb], s46 offen lds\n\t" \
    "buffer_load_dwordx4 %[bo1], %[srdb], s46 offen offset:1024 lds\n\t" \
    "buffer_load_dwordx4 %[bo2], %[srdb], s46 offen offset:2048 lds\n\t" \
    "buffer_load_dwordx4 %[bo3], %[srdb], s46 offen offset:3072 lds\n\t" \
    "s_add_u32 m0, s48, 19456\n\t" \
    "s_nop 0\n\t" \
    "buffer_load_dwordx4 %[bo4], %[srdb], s46 offen lds\n\t" \
    "s_waitcnt vmcnt(15) lgkmcnt(0)\n\t" \
    "s_barrier\n\t" \
    "ds_read_b64_tr_b16 a[64:65], %[brd] offset:0\n\t" \
    "ds_read_b64_tr_b16 a[66:67], %[brd] offset:1280\n\t" \
    "ds_read_b128 a[0:3], %[ard] offset:0\n\t" \
    "ds_read_b128 a[4:7], %[ard] offset:1024\n\t" \
    "ds_read_b128 a[8:11], %[ard] offset:2048\n\t" \
    "ds_read_b128 a[12:15], %[ard] offset:3072\n\t" \
    "ds_read_b128 a[16:19], %[ard] offset:4096\n\t" \
    "ds_read_b128 a[20:23], %[ard] offset:5120\n\t" \
    "ds_read_b128 a[24:27], %[ard] offset:6144\n\t" \
    "ds_read_b128 a[28:31], %[ard] offset:7168\n\t" \
    "ds_read_b64_tr_b16 a[68:69], %[brd] offset:256\n\t" \
    "ds_read_b64_tr_b16 a[70:71], %[brd] offset:1536\n\t" \
    "ds_read_b64_tr_b16 a[72:73], %[brd] offset:512\n\t" \
    "ds_read_b64_tr_b16 a[74:75], %[brd] offset:1792\n\t" \
    "ds_read_b64_tr_b16 a[76:77], %[brd] offset:768\n\t" \
    "ds_read_b64_tr_b16 a[78:79], %[brd] offset:2048\n\t" \
    "ds_read_b64_tr_b16 a[80:81], %[brd] offset:1024\n\t" \
    "ds_read_b64_tr_b16 a[82:83], %[brd] offset:2304\n\t" \
    "s_waitcnt lgkmcnt(0) vmcnt(10)\n\t" \
    "v_mfma_f32_16x16x32_bf16 v[24:27], a[0:3], a[64:67], 0\n\t" \
    "ds_read_b64_tr_b16 a[88:89], %[brd] offset:5120\n\t" \
    "v_mfma_f32_16x16x32_bf16 v[28:31], a[4:7], a[64:67], 0\n\t" \
    "ds_read_b64_tr_b16 a[90:91], %[brd] offset:6400\n\t" \
    "v_mfma_f32_16x16x32_bf16 v[32:35], a[8:11], a[64:67], 0\n\t" \
    "ds_read_b128 a[32:35], %[ard] offset:8192\n\t" \
    "v_mfma_f32_16x16x32_bf16 v[36:39], a[12:15], a[64:67], 0\n\t" \
    "ds_read_b128 a[36:39], %[ard] offset:9216\n\t" \
    "v_mfma_f32_16x16x32_bf16 v[40:43], a[16:19], a[64:67], 0\n\t" \
    "ds_read_b128 a[40:43], %[ard] offset:10240\n\t" \
    "v_mfma_f32_16x16x32_bf16 v[44:47], a[20:23], a[64:67], 0\n\t" \
    "ds_read_b128 a[44:47], %[ard] offset:11264\n\t" \
    "v_mfma_f32_16x16x32_bf16 v[48:51], a[24:27], a[64:67], 0\n\t" \
    "ds_read_b128 a[48:51], %[ard] offset:12288\n\t" \
    "v_mfma_f32_16x16x32_bf16 v[52:55], a[28:31], a[64:67], 0\n\t" \
    "ds_read_b128 a[52:55], %[ard] offset:13312\n\t" \
    "v_mfma_f32_16x16x32_bf16 v[56:59], a[0:3], a[68:71], 0\n\t" \
    "ds_read_b128 a[56:59], %[ard] offset:14336\n\t" \
    "v_mfma_f32_16x16x32_bf16 v[60:63], a[4:7], a[68:71], 0\n\t" \
    "ds_read_b128 a[60:63], %[ard] offset:15360\n\t" \
    "v_mfma_f32_16x16x32_bf16 v[64:67], a[8:11], a[68:71], 0\n\t" \
    "ds_read_b64_tr_b16 a[92:93], %[brd] offset:5376\n\t" \
    "v_mfma_f32_16x16x32_bf16 v[68:71], a[12:15], a[68:71], 0\n\t" \
    "ds_read_b64_tr_b16 a[94:95], %[brd] offset:6656\n\t" \
    "v_mfma_f32_16x16x32_bf16 v[72:75], a[16:19], a[68:71], 0\n\t" \
    "ds_read_b64_tr_b16 a[96:97], %[brd] offset:5632\n\t" \
    "v_mfma_f32_16x16x32_bf16 v[76:79], a[20:23], a[68:71], 0\n\t" \
    "ds_read_b64_tr_b16 a[98:99], %[brd] offset:6912\n\t" \
    "v_mfma_f32_16x16x32_bf16 v[80:83], a[24:27], a[68:71], 0\n\t" \
    "ds_read_b64_tr_b16 a[100:101], %[brd] offset:5888\n\t" \
    "v_mfma_f32_16x16x32_bf16 v[84:87], a[28:31], a[68:71], 0\n\t" \
    "ds_read_b64_tr_b16 a[102:103], %[brd] offset:7168\n\t" \
    "v_mfma_f32_16x16x32_bf16 v[88:91], a[0:3], a[72:75], 0\n\t" \
    "ds_read_b64_tr_b16 a[104:105], %[brd] offset:6144\n\t" \
    "v_mfma_f32_16x16x32_bf16 v[92:95], a[4:7], a[72:75], 0\n\t" \
    "ds_read_b64_tr_b16 a[106:107], %[brd] offset:7424\n\t" \
    "v_mfma_f32_16x16x32_bf16 v[96:99], a[8:11], a[72:75], 0\n\t" \
    "s_add_u32 m0, s48, 0\n\t" \
    "s_add_u32 s46, %[boff], 163840\n\t" \
    "buffer_load_dwordx4 %[bo0], %[srdb], s46 offen lds\n\t" \
    "v_mfma_f32_16x16x32_bf16 v[100:103], a[12:15], a[72:75], 0\n\t" \
    "buffer_load_dwordx4 %[bo1], %[srdb], s46 offen offset:1024 lds\n\t" \
    "v_mfma_f32_16x16x32_bf16 v[104:107], a[16:19], a[72:75], 0\n\t" \
    "buffer_load_dwordx4 %[bo2], %[srdb], s46 offen offset:2048 lds\n\t" \
    "v_mfma_f32_16x16x32_bf16 v[108:111], a[20:23], a[72:75], 0\n\t" \
    "buffer_load_dwordx4 %[bo3], %[srdb], s46 offen offset:3072 lds\n\t" \
    "v_mfma_f32_16x16x32_bf16 v[112:115], a[24:27], a[72:75], 0\n\t" \
    "s_add_u32 m0, s48, 4096\n\t" \
    "s_nop 0\n\t" \
    "buffer_load_dwordx4 %[bo4], %[srdb], s46 offen lds\n\t" \
    "v_mfma_f32_16x16x32_bf16 v[116:119], a[28:31], a[72:75], 0\n\t" \
    "v_mfma_f32_16x16x32_bf16 v[120:123], a[0:3], a[76:79], 0\n\t" \
    "v_mfma_f32_16x16x32_bf16 v[124:127], a[4:7], a[76:79], 0\n\t" \
    "v_mfma_f32_16x16x32_bf16 v[128:131], a[8:11], a[76:79], 0\n\t" \
    "v_mfma_f32_16x16x32_bf16 v[132:135], a[12:15], a[76:79], 0\n\t" \
    "v_mfma_f32_16x16x32_bf16 v[136:139], a[16:19], a[76:79], 0\n\t" \
    "v_mfma_f32_16x16x32_bf16 v[140:143], a[20:23], a[76:79], 0\n\t" \
    "v_mfma_f32_16x16x32_bf16 v[144:147], a[24:27], a[76:79], 0\n\t" \
    "v_mfma_f32_16x16x32_bf16 v[148:151], a[28:31], a[76:79], 0\n\t" \
    "v_mfma_f32_16x16x32_bf16 v[152:155], a[0:3], a[80:83], 0\n\t" \
    "v_mfma_f32_16x16x32_bf16 v[156:159], a[4:7], a[80:83], 0\n\t" \
    "v_mfma_f32_16x16x32_bf16 v[160:163], a[8:11], a[80:83], 0\n\t" \
    "v_mfma_f32_16x16x32_bf16 v[164:167], a[12:15], a[80:83], 0\n\t" \
    "v_mfma_f32_16x16x32_bf16 v[168:171], a[16:19], a[80:83], 0\n\t" \
    "v_mfma_f32_16x16x32_bf16 v[172:175], a[20:23], a[80:83], 0\n\t" \
    "v_mfma_f32_16x16x32_bf16 v[176:179], a[24:27], a[80:83], 0\n\t" \
    "v_mfma_f32_16x16x32_bf16 v[180:183], a[28:31], a[80:83], 0\n\t" \
    "s_waitcnt lgkmcnt(0) vmcnt(10)\n\t" \
    "v_mfma_f32_16x16x32_bf16 v[24:27], a[32:35], a[88:91], v[24:27]\n\t" \
    "ds_read_b64_tr_b16 a[64:65], %[brd] offset:10240\n\t" \
    "v_mfma_f32_16x16x32_bf16 v[28:31], a[36:39], a[88:91], v[28:31]\n\t" \
    "ds_read_b64_tr_b16 a[66:67], %[brd] offset:11520\n\t" \
    "v_mfma_f32_16x16x32_bf16 v[32:35], a[40:43], a[88:91], v[32:35]\n\t" \
    "ds_read_b128 a[0:3], %[ard] offset:16384\n\t" \
    "v_mfma_f32_16x16x32_bf16 v[36:39], a[44:47], a[88:91], v[36:39]\n\t" \
    "ds_read_b128 a[4:7], %[ard] offset:17408\n\t" \
    "v_mfma_f32_16x16x32_bf16 v[40:43], a[48:51], a[88:91], v[40:43]\n\t" \
    "ds_read_b128 a[8:11], %[ard] offset:18432\n\t" \
    "v_mfma_f32_16x16x32_bf16 v[44:47], a[52:55], a[88:91], v[44:47]\n\t" \
    "ds_read_b128 a[12:15], %[ard] offset:19456\n\t" \
    "v_mfma_f32_16x16x32_bf16 v[48:51], a[56:59], a[88:91], v[48:51]\n\t" \
    "ds_read_b128 a[16:19], %[ard] offset:20480\n\t" \
    "v_mfma_f32_16x16x32_bf16 v[52:55], a[60:63], a[88:91], v[52:55]\n\t" \
    "ds_read_b128 a[20:23], %[ard] offset:21504\n\t" \
    "v_mfma_f32_16x16x32_bf16 v[56:59], a[32:35], a[92:95], v[56:59]\n\t" \
    "ds_read_b128 a[24:27], %[ard] offset:22528\n\t" \
    "v_mfma_f32_16x16x32_bf16 v[60:63], a[36:39], a[92:95], v[60:63]\n\t" \
    "ds_read_b128 a[28:31], %[ard] offset:23552\n\t" \
    "v_mfma_f32_16x16x32_bf16 v[64:67], a[40:43], a[92:95], v[64:67]\n\t" \
    "ds_read_b64_tr_b16 a[68:69], %[brd] offset:10496\n\t" \
    "v_mfma_f32_16x16x32_bf16 v[68:71], a[44:47], a[92:95], v[68:71]\n\t" \
    "ds_read_b64_tr_b16 a[70:71], %[brd] offset:11776\n\t" \
    "v_mfma_f32_16x16x32_bf16 v[72:75], a[48:51], a[92:95], v[72:75]\n\t" \
    "ds_read_b64_tr_b16 a[72:73], %[brd] offset:10752\n\t" \
    "v_mfma_f32_16x16x32_bf16 v[76:79], a[52:55], a[92:95], v[76:79]\n\t" \
    "ds_read_b64_tr_b16 a[74:75], %[brd] offset:12032\n\t" \
    "v_mfma_f32_16x16x32_bf16 v[80:83], a[56:59], a[92:95], v[80:83]\n\t" \
    "ds_read_b64_tr_b16 a[76:77], %[brd] offset:11008\n\t" \
    "v_mfma_f32_16x16x32_bf16 v[84:87], a[60:63], a[92:95], v[84:87]\n\t" \
    "ds_read_b64_tr_b16 a[78:79], %[brd] offset:12288\n\t" \
    "v_mfma_f32_16x16x32_bf16 v[88:91], a[32:35], a[96:99], v[88:91]\n\t" \
    "ds_read_b64_tr_b16 a[80:81], %[brd] offset:11264\n\t" \
    "v_mfma_f32_16x16x32_bf16 v[92:95], a[36:39], a[96:99], v[92:95]\n\t" \
    "ds_read_b64_tr_b16 a[82:83], %[brd] offset:12544\n\t" \
    "v_mfma_f32_16x16x32_bf16 v[96:99], a[40:43], a[96:99], v[96:99]\n\t" \
    "s_add_u32 m0, s48, 5120\n\t" \
    "s_add_u32 s46, %[boff], 204800\n\t" \
    "buffer_load_dwordx4 %[bo0], %[srdb], s46 offen lds\n\t" \
    "v_mfma_f32_16x16x32_bf16 v[100:103], a[44:47], a[96:99], v[100:103]\n\t" \
    "buffer_load_dwordx4 %[bo1], %[srdb], s46 offen offset:1024 lds\n\t" \
    "v_mfma_f32_16x16x32_bf16 v[104:107], a[48:51], a[96:99], v[104:107]\n\t" \
    "buffer_load_dwordx4 %[bo2], %[srdb], s46 offen offset:2048 lds\n\t" \
    "v_mfma_f32_16x16x32_bf16 v[108:111], a[52:55], a[96:99], v[108:111]\n\t" \
    "buffer_load_dwordx4 %[bo3], %[srdb], s46 offen offset:3072 lds\n\t" \
    "v_mfma_f32_16x16x32_bf16 v[112:115], a[56:59], a[96:99], v[112:115]\n\t" \
    "s_add_u32 m0, s48, 9216\n\t" \
    "s_nop 0\n\t" \
    "buffer_load_dwordx4 %[bo4], %[srdb], s46 offen lds\n\t" \
    "v_mfma_f32_16x16x32_bf16 v[116:119], a[60:63], a[96:99], v[116:119]\n\t" \
    "v_mfma_f32_16x16x32_bf16 v[120:123], a[32:35], a[100:103], v[120:123]\n\t" \
    "v_mfma_f32_16x16x32_bf16 v[124:127], a[36:39], a[100:103], v[124:127]\n\t" \
    "v_mfma_f32_16x16x32_bf16 v[128:131], a[40:43], a[100:103], v[128:131]\n\t" \
    "v_mfma_f32_16x16x32_bf16 v[132:135], a[44:47], a[100:103], v[132:135]\n\t" \
    "v_mfma_f32_16x16x32_bf16 v[136:139], a[48:51], a[100:103], v[136:139]\n\t" \
    "v_mfma_f32_16x16x32_bf16 v[140:143], a[52:55], a[100:103], v[140:143]\n\t" \
    "v_mfma_f32_16x16x32_bf16 v[144:147], a[56:59], a[100:103], v[144:147]\n\t" \
    "v_mfma_f32_16x16x32_bf16 v[148:151], a[60:63], a[100:103], v[148:151]\n\t" \
    "v_mfma_f32_16x16x32_bf16 v[152:155], a[32:35], a[104:107], v[152:155]\n\t" \
    "v_mfma_f32_16x16x32_bf16 v[156:159], a[36:39], a[104:107], v[156:159]\n\t" \
    "v_mfma_f32_16x16x32_bf16 v[160:163], a[40:43], a[104:107], v[160:163]\n\t" \
    "v_mfma_f32_16x16x32_bf16 v[164:167], a[44:47], a[104:107], v[164:167]\n\t" \
    "v_mfma_f32_16x16x32_bf16 v[168:171], a[48:51], a[104:107], v[168:171]\n\t" \
    "v_mfma_f32_16x16x32_bf16 v[172:175], a[52:55], a[104:107], v[172:175]\n\t" \
    "v_mfma_f32_16x16x32_bf16 v[176:179], a[56:59], a[104:107], v[176:179]\n\t" \
    "v_mfma_f32_16x16x32_bf16 v[180:183], a[60:63], a[104:107], v[180:183]\n\t" \
    "s_waitcnt lgkmcnt(0) vmcnt(10)\n\t" \
    "v_mfma_f32_16x16x32_bf16 v[24:27], a[0:3], a[64:67], v[24:27]\n\t" \
    "ds_read_b64_tr_b16 a[88:89], %[brd] offset:15360\n\t" \
    "v_mfma_f32_16x16x32_bf16 v[28:31], a[4:7], a[64:67], v[28:31]\n\t" \
    "ds_read_b64_tr_b16 a[90:91], %[brd] offset:16640\n\t" \
    "v_mfma_f32_16x16x32_bf16 v[32:35], a[8:11], a[64:67], v[32:35]\n\t" \
    "ds_read_b128 a[32:35], %[ard] offset:24576\n\t" \
    "v_mfma_f32_16x16x32_bf16 v[36:39], a[12:15], a[64:67], v[36:39]\n\t" \
    "ds_read_b128 a[36:39], %[ard] offset:25600\n\t" \
    "v_mfma_f32_16x16x32_bf16 v[40:43], a[16:19], a[64:67], v[40:43]\n\t" \
    "ds_read_b128 a[40:43], %[ard] offset:26624\n\t" \
    "v_mfma_f32_16x16x32_bf16 v[44:47], a[20:23], a[64:67], v[44:47]\n\t" \
    "ds_read_b128 a[44:47], %[ard] offset:27648\n\t" \
    "v_mfma_f32_16x16x32_bf16 v[48:51], a[24:27], a[64:67], v[48:51]\n\t" \
    "ds_read_b128 a[48:51], %[ard] offset:28672\n\t" \
    "v_mfma_f32_16x16x32_bf16 v[52:55], a[28:31], a[64:67], v[52:55]\n\t" \
    "ds_read_b128 a[52:55], %[ard] offset:29696\n\t" \
    "v_mfma_f32_16x16x32_bf16 v[56:59], a[0:3], a[68:71], v[56:59]\n\t" \
    "ds_read_b128 a[56:59], %[ard] offset:30720\n\t" \
    "v_mfma_f32_16x16x32_bf16 v[60:63], a[4:7], a[68:71], v[60:63]\n\t" \
    "ds_read_b128 a[60:63], %[ard] offset:31744\n\t" \
    "v_mfma_f32_16x16x32_bf16 v[64:67], a[8:11], a[68:71], v[64:67]\n\t" \
    "ds_read_b64_tr_b16 a[92:93], %[brd] offset:15616\n\t" \
    "v_mfma_f32_16x16x32_bf16 v[68:71], a[12:15], a[68:71], v[68:71]\n\t" \
    "ds_read_b64_tr_b16 a[94:95], %[brd] offset:16896\n\t" \
    "v_mfma_f32_16x16x32_bf16 v[72:75], a[16:19], a[68:71], v[72:75]\n\t" \
    "ds_read_b64_tr_b16 a[96:97], %[brd] offset:15872\n\t" \
    "v_mfma_f32_16x16x32_bf16 v[76:79], a[20:23], a[68:71], v[76:79]\n\t" \
    "ds_read_b64_tr_b16 a[98:99], %[brd] offset:17152\n\t" \
    "v_mfma_f32_16x16x32_bf16 v[80:83], a[24:27], a[68:71], v[80:83]\n\t" \
    "ds_read_b64_tr_b16 a[100:101], %[brd] offset:16128\n\t" \
    "v_mfma_f32_16x16x32_bf16 v[84:87], a[28:31], a[68:71], v[84:87]\n\t" \
    "ds_read_b64_tr_b16 a[102:103], %[brd] offset:17408\n\t" \
    "v_mfma_f32_16x16x32_bf16 v[88:91], a[0:3], a[72:75], v[88:91]\n\t" \
    "ds_read_b64_tr_b16 a[104:105], %[brd] offset:16384\n\t" \
    "v_mfma_f32_16x16x32_bf16 v[92:95], a[4:7], a[72:75], v[92:95]\n\t" \
    "ds_read_b64_tr_b16 a[106:107], %[brd] offset:17664\n\t" \
    "v_mfma_f32_16x16x32_bf16 v[96:99], a[8:11], a[72:75], v[96:99]\n\t" \
    "s_add_u32 m0, s48, 10240\n\t" \
    "s_add_u32 s46, %[boff], 640\n\t" \
    "buffer_load_dwordx4 %[bo0], %[srdb], s46 offen lds\n\t" \
    "v_mfma_f32_16x16x32_bf16 v[100:103], a[12:15], a[72:75], v[100:103]\n\t" \
    "buffer_load_dwordx4 %[bo1], %[srdb], s46 offen offset:1024 lds\n\t" \
    "v_mfma_f32_16x16x32_bf16 v[104:107], a[16:19], a[72:75], v[104:107]\n\t" \
    "buffer_load_dwordx4 %[bo2], %[srdb], s46 offen offset:2048 lds\n\t" \
    "v_mfma_f32_16x16x32_bf16 v[108:111], a[20:23], a[72:75], v[108:111]\n\t" \
    "buffer_load_dwordx4 %[bo3], %[srdb], s46 offen offset:3072 lds\n\t" \
    "v_mfma_f32_16x16x32_bf16 v[112:115], a[24:27], a[72:75], v[112:115]\n\t" \
    "s_add_u32 m0, s48, 14336\n\t" \
    "s_nop 0\n\t" \
    "buffer_load_dwordx4 %[bo4], %[srdb], s46 offen lds\n\t" \
    "v_mfma_f32_16x16x32_bf16 v[116:119], a[28:31], a[72:75], v[116:119]\n\t" \
    "v_mfma_f32_16x16x32_bf16 v[120:123], a[0:3], a[76:79], v[120:123]\n\t" \
    "v_mfma_f32_16x16x32_bf16 v[124:127], a[4:7], a[76:79], v[124:127]\n\t" \
    "v_mfma_f32_16x16x32_bf16 v[128:131], a[8:11], a[76:79], v[128:131]\n\t" \
    "v_mfma_f32_16x16x32_bf16 v[132:135], a[12:15], a[76:79], v[132:135]\n\t" \
    "v_mfma_f32_16x16x32_bf16 v[136:139], a[16:19], a[76:79], v[136:139]\n\t" \
    "v_mfma_f32_16x16x32_bf16 v[140:143], a[20:23], a[76:79], v[140:143]\n\t" \
    "v_mfma_f32_16x16x32_bf16 v[144:147], a[24:27], a[76:79], v[144:147]\n\t" \
    "v_mfma_f32_16x16x32_bf16 v[148:151], a[28:31], a[76:79], v[148:151]\n\t" \
    "v_mfma_f32_16x16x32_bf16 v[152:155], a[0:3], a[80:83], v[152:155]\n\t" \
    "v_mfma_f32_16x16x32_bf16 v[156:159], a[4:7], a[80:83], v[156:159]\n\t" \
    "v_mfma_f32_16x16x32_bf16 v[160:163], a[8:11], a[80:83], v[160:163]\n\t" \
    "v_mfma_f32_16x16x32_bf16 v[164:167], a[12:15], a[80:83], v[164:167]\n\t" \
    "v_mfma_f32_16x16x32_bf16 v[168:171], a[16:19], a[80:83], v[168:171]\n\t" \
    "v_mfma_f32_16x16x32_bf16 v[172:175], a[20:23], a[80:83], v[172:175]\n\t" \
    "v_mfma_f32_16x16x32_bf16 v[176:179], a[24:27], a[80:83], v[176:179]\n\t" \
    "v_mfma_f32_16x16x32_bf16 v[180:183], a[28:31], a[80:83], v[180:183]\n\t" \
    "s_waitcnt lgkmcnt(0) vmcnt(10)\n\t" \
    "v_mfma_f32_16x16x32_bf16 v[24:27], a[32:35], a[88:91], v[24:27]\n\t" \
    "ds_read_b64_tr_b16 a[64:65], %[brd] offset:0\n\t" \
    "v_mfma_f32_16x16x32_bf16 v[28:31], a[36:39], a[88:91], v[28:31]\n\t" \
    "ds_read_b64_tr_b16 a[66:67], %[brd] offset:1280\n\t" \
    "v_mfma_f32_16x16x32_bf16 v[32:35], a[40:43], a[88:91], v[32:35]\n\t" \
    "ds_read_b128 a[0:3], %[ard] offset:32768\n\t" \
    "v_mfma_f32_16x16x32_bf16 v[36:39], a[44:47], a[88:91], v[36:39]\n\t" \
    "ds_read_b128 a[4:7], %[ard] offset:33792\n\t" \
    "v_mfma_f32_16x16x32_bf16 v[40:43], a[48:51], a[88:91], v[40:43]\n\t" \
    "ds_read_b128 a[8:11], %[ard] offset:34816\n\t" \
    "v_mfma_f32_16x16x32_bf16 v[44:47], a[52:55], a[88:91], v[44:47]\n\t" \
    "ds_read_b128 a[12:15], %[ard] offset:35840\n\t" \
    "v_mfma_f32_16x16x32_bf16 v[48:51], a[56:59], a[88:91], v[48:51]\n\t" \
    "ds_read_b128 a[16:19], %[ard] offset:36864\n\t" \
    "v_mfma_f32_16x16x32_bf16 v[52:55], a[60:63], a[88:91], v[52:55]\n\t" \
    "ds_read_b128 a[20:23], %[ard] offset:37888\n\t" \
    "v_mfma_f32_16x16x32_bf16 v[56:59], a[32:35], a[92:95], v[56:59]\n\t" \
    "ds_read_b128 a[24:27], %[ard] offset:38912\n\t" \
    "v_mfma_f32_16x16x32_bf16 v[60:63], a[36:39], a[92:95], v[60:63]\n\t" \
    "ds_read_b128 a[28:31], %[ard] offset:39936\n\t" \
    "v_mfma_f32_16x16x32_bf16 v[64:67], a[40:43], a[92:95], v[64:67]\n\t" \
    "ds_read_b64_tr_b16 a[68:69], %[brd] offset:256\n\t" \
    "v_mfma_f32_16x16x32_bf16 v[68:71], a[44:47], a[92:95], v[68:71]\n\t" \
    "ds_read_b64_tr_b16 a[70:71], %[brd] offset:1536\n\t" \
    "v_mfma_f32_16x16x32_bf16 v[72:75], a[48:51], a[92:95], v[72:75]\n\t" \
    "ds_read_b64_tr_b16 a[72:73], %[brd] offset:512\n\t" \
    "v_mfma_f32_16x16x32_bf16 v[76:79], a[52:55], a[92:95], v[76:79]\n\t" \
    "ds_read_b64_tr_b16 a[74:75], %[brd] offset:1792\n\t" \
    "v_mfma_f32_16x16x32_bf16 v[80:83], a[56:59], a[92:95], v[80:83]\n\t" \
    "ds_read_b64_tr_b16 a[76:77], %[brd] offset:768\n\t" \
    "v_mfma_f32_16x16x32_bf16 v[84:87], a[60:63], a[92:95], v[84:87]\n\t" \
    "ds_read_b64_tr_b16 a[78:79], %[brd] offset:2048\n\t" \
    "v_mfma_f32_16x16x32_bf16 v[88:91], a[32:35], a[96:99], v[88:91]\n\t" \
    "ds_read_b64_tr_b16 a[80:81], %[brd] offset:1024\n\t" \
    "v_mfma_f32_16x16x32_bf16 v[92:95], a[36:39], a[96:99], v[92:95]\n\t" \
    "ds_read_b64_tr_b16 a[82:83], %[brd] offset:2304\n\t" \
    "v_mfma_f32_16x16x32_bf16 v[96:99], a[40:43], a[96:99], v[96:99]\n\t" \
    "s_add_u32 m0, s48, 15360\n\t" \
    "s_add_u32 s46, %[boff], 41600\n\t" \
    "buffer_load_dwordx4 %[bo0], %[srdb], s46 offen lds\n\t" \
    "v_mfma_f32_16x16x32_bf16 v[100:103], a[44:47], a[96:99], v[100:103]\n\t" \
    "buffer_load_dwordx4 %[bo1], %[srdb], s46 offen offset:1024 lds\n\t" \
    "v_mfma_f32_16x16x32_bf16 v[104:107], a[48:51], a[96:99], v[104:107]\n\t" \
    "buffer_load_dwordx4 %[bo2], %[srdb], s46 offen offset:2048 lds\n\t" \
    "v_mfma_f32_16x16x32_bf16 v[108:111], a[52:55], a[96:99], v[108:111]\n\t" \
    "buffer_load_dwordx4 %[bo3], %[srdb], s46 offen offset:3072 lds\n\t" \
    "v_mfma_f32_16x16x32_bf16 v[112:115], a[56:59], a[96:99], v[112:115]\n\t" \
    "s_add_u32 m0, s48, 19456\n\t" \
    "s_nop 0\n\t" \
    "buffer_load_dwordx4 %[bo4], %[srdb], s46 offen lds\n\t" \
    "v_mfma_f32_16x16x32_bf16 v[116:119], a[60:63], a[96:99], v[116:119]\n\t" \
    "v_mfma_f32_16x16x32_bf16 v[120:123], a[32:35], a[100:103], v[120:123]\n\t" \
    "v_mfma_f32_16x16x32_bf16 v[124:127], a[36:39], a[100:103], v[124:127]\n\t" \
    "v_mfma_f32_16x16x32_bf16 v[128:131], a[40:43], a[100:103], v[128:131]\n\t" \
    "v_mfma_f32_16x16x32_bf16 v[132:135], a[44:47], a[100:103], v[132:135]\n\t" \
    "v_mfma_f32_16x16x32_bf16 v[136:139], a[48:51], a[100:103], v[136:139]\n\t" \
    "v_mfma_f32_16x16x32_bf16 v[140:143], a[52:55], a[100:103], v[140:143]\n\t" \
    "v_mfma_f32_16x16x32_bf16 v[144:147], a[56:59], a[100:103], v[144:147]\n\t" \
    "v_mfma_f32_16x16x32_bf16 v[148:151], a[60:63], a[100:103], v[148:151]\n\t" \
    "v_mfma_f32_16x16x32_bf16 v[152:155], a[32:35], a[104:107], v[152:155]\n\t" \
    "v_mfma_f32_16x16x32_bf16 v[156:159], a[36:39], a[104:107], v[156:159]\n\t" \
    "v_mfma_f32_16x16x32_bf16 v[160:163], a[40:43], a[104:107], v[160:163]\n\t" \
    "v_mfma_f32_16x16x32_bf16 v[164:167], a[44:47], a[104:107], v[164:167]\n\t" \
    "v_mfma_f32_16x16x32_bf16 v[168:171], a[48:51], a[104:107], v[168:171]\n\t" \
    "v_mfma_f32_16x16x32_bf16 v[172:175], a[52:55], a[104:107], v[172:175]\n\t" \
    "v_mfma_f32_16x16x32_bf16 v[176:179], a[56:59], a[104:107], v[176:179]\n\t" \
    "v_mfma_f32_16x16x32_bf16 v[180:183], a[60:63], a[104:107], v[180:183]\n\t" \
    "s_waitcnt lgkmcnt(0) vmcnt(10)\n\t" \
    "v_mfma_f32_16x16x32_bf16 v[24:27], a[0:3], a[64:67], v[24:27]\n\t" \
    "ds_read_b64_tr_b16 a[88:89], %[brd] offset:5120\n\t" \
    "v_mfma_f32_16x16x32_bf16 v[28:31], a[4:7], a[64:67], v[28:31]\n\t" \
    "ds_read_b64_tr_b16 a[90:91], %[brd] offset:6400\n\t" \
    "v_mfma_f32_16x16x32_bf16 v[32:35], a[8:11], a[64:67], v[32:35]\n\t" \
    "ds_read_b128 a[32:35], %[ard] offset:40960\n\t" \
    "v_mfma_f32_16x16x32_bf16 v[36:39], a[12:15], a[64:67], v[36:39]\n\t" \
    "ds_read_b128 a[36:39], %[ard] offset:41984\n\t" \
    "v_mfma_f32_16x16x32_bf16 v[40:43], a[16:19], a[64:67], v[40:43]\n\t" \
    "ds_read_b128 a[40:43], %[ard] offset:43008\n\t" \
    "v_mfma_f32_16x16x32_bf16 v[44:47], a[20:23], a[64:67], v[44:47]\n\t" \
    "ds_read_b128 a[44:47], %[ard] offset:44032\n\t" \
    "v_mfma_f32_16x16x32_bf16 v[48:51], a[24:27], a[64:67], v[48:51]\n\t" \
    "ds_read_b128 a[48:51], %[ard] offset:45056\n\t" \
    "v_mfma_f32_16x16x32_bf16 v[52:55], a[28:31], a[64:67], v[52:55]\n\t" \
    "ds_read_b128 a[52:55], %[ard] offset:46080\n\t" \
    "v_mfma_f32_16x16x32_bf16 v[56:59], a[0:3], a[68:71], v[56:59]\n\t" \
    "ds_read_b128 a[56:59], %[ard] offset:47104\n\t" \
    "v_mfma_f32_16x16x32_bf16 v[60:63], a[4:7], a[68:71], v[60:63]\n\t" \
    "ds_read_b128 a[60:63], %[ard] offset:48128\n\t" \
    "v_mfma_f32_16x16x32_bf16 v[64:67], a[8:11], a[68:71], v[64:67]\n\t" \
    "ds_read_b64_tr_b16 a[92:93], %[brd] offset:5376\n\t" \
    "v_mfma_f32_16x16x32_bf16 v[68:71], a[12:15], a[68:71], v[68:71]\n\t" \
    "ds_read_b64_tr_b16 a[94:95], %[brd] offset:6656\n\t" \
    "v_mfma_f32_16x16x32_bf16 v[72:75], a[16:19], a[68:71], v[72:75]\n\t" \
    "ds_read_b64_tr_b16 a[96:97], %[brd] offset:5632\n\t" \
    "v_mfma_f32_16x16x32_bf16 v[76:79], a[20:23], a[68:71], v[76:79]\n\t" \
    "ds_read_b64_tr_b16 a[98:99], %[brd] offset:6912\n\t" \
    "v_mfma_f32_16x16x32_bf16 v[80:83], a[24:27], a[68:71], v[80:83]\n\t" \
    "ds_read_b64_tr_b16 a[100:101], %[brd] offset:5888\n\t" \
    "v_mfma_f32_16x16x32_bf16 v[84:87], a[28:31], a[68:71], v[84:87]\n\t" \
    "ds_read_b64_tr_b16 a[102:103], %[brd] offset:7168\n\t" \
    "v_mfma_f32_16x16x32_bf16 v[88:91], a[0:3], a[72:75], v[88:91]\n\t" \
    "ds_read_b64_tr_b16 a[104:105], %[brd] offset:6144\n\t" \
    "v_mfma_f32_16x16x32_bf16 v[92:95], a[4:7], a[72:75], v[92:95]\n\t" \
    "ds_read_b64_tr_b16 a[106:107], %[brd] offset:7424\n\t" \
    "v_mfma_f32_16x16x32_bf16 v[96:99], a[8:11], a[72:75], v[96:99]\n\t" \
    "s_add_u32 m0, s48, 0\n\t" \
    "s_add_u32 s46, %[boff], 82560\n\t" \
    "buffer_load_dwordx4 %[bo0], %[srdb], s46 offen lds\n\t" \
    "v_mfma_f32_16x16x32_bf16 v[100:103], a[12:15], a[72:75], v[100:103]\n\t" \
    "buffer_load_dwordx4 %[bo1], %[srdb], s46 offen offset:1024 lds\n\t" \
    "v_mfma_f32_16x16x32_bf16 v[104:107], a[16:19], a[72:75], v[104:107]\n\t" \
    "buffer_load_dwordx4 %[bo2], %[srdb], s46 offen offset:2048 lds\n\t" \
    "v_mfma_f32_16x16x32_bf16 v[108:111], a[20:23], a[72:75], v[108:111]\n\t" \
    "buffer_load_dwordx4 %[bo3], %[srdb], s46 offen offset:3072 lds\n\t" \
    "v_mfma_f32_16x16x32_bf16 v[112:115], a[24:27], a[72:75], v[112:115]\n\t" \
    "s_add_u32 m0, s48, 4096\n\t" \
    "s_nop 0\n\t" \
    "buffer_load_dwordx4 %[bo4], %[srdb], s46 offen lds\n\t" \
    "v_mfma_f32_16x16x32_bf16 v[116:119], a[28:31], a[72:75], v[116:119]\n\t" \
    "v_mfma_f32_16x16x32_bf16 v[120:123], a[0:3], a[76:79], v[120:123]\n\t" \
    "v_mfma_f32_16x16x32_bf16 v[124:127], a[4:7], a[76:79], v[124:127]\n\t" \
    "v_mfma_f32_16x16x32_bf16 v[128:131], a[8:11], a[76:79], v[128:131]\n\t" \
    "v_mfma_f32_16x16x32_bf16 v[132:135], a[12:15], a[76:79], v[132:135]\n\t" \
    "v_mfma_f32_16x16x32_bf16 v[136:139], a[16:19], a[76:79], v[136:139]\n\t" \
    "v_mfma_f32_16x16x32_bf16 v[140:143], a[20:23], a[76:79], v[140:143]\n\t" \
    "v_mfma_f32_16x16x32_bf16 v[144:147], a[24:27], a[76:79], v[144:147]\n\t" \
    "v_mfma_f32_16x16x32_bf16 v[148:151], a[28:31], a[76:79], v[148:151]\n\t" \
    "v_mfma_f32_16x16x32_bf16 v[152:155], a[0:3], a[80:83], v[152:155]\n\t" \
    "v_mfma_f32_16x16x32_bf16 v[156:159], a[4:7], a[80:83], v[156:159]\n\t" \
    "v_mfma_f32_16x16x32_bf16 v[160:163], a[8:11], a[80:83], v[160:163]\n\t" \
    "v_mfma_f32_16x16x32_bf16 v[164:167], a[12:15], a[80:83], v[164:167]\n\t" \
    "v_mfma_f32_16x16x32_bf16 v[168:171], a[16:19], a[80:83], v[168:171]\n\t" \
    "v_mfma_f32_16x16x32_bf16 v[172:175], a[20:23], a[80:83], v[172:175]\n\t" \
    "v_mfma_f32_16x16x32_bf16 v[176:179], a[24:27], a[80:83], v[176:179]\n\t" \
    "v_mfma_f32_16x16x32_bf16 v[180:183], a[28:31], a[80:83], v[180:183]\n\t" \
    "s_waitcnt lgkmcnt(0) vmcnt(10)\n\t" \
    "v_mfma_f32_16x16x32_bf16 v[24:27], a[32:35], a[88:91], v[24:27]\n\t" \
    "ds_read_b64_tr_b16 a[64:65], %[brd] offset:10240\n\t" \
    "v_mfma_f32_16x16x32_bf16 v[28:31], a[36:39], a[88:91], v[28:31]\n\t" \
    "ds_read_b64_tr_b16 a[66:67], %[brd] offset:11520\n\t" \
    "v_mfma_f32_16x16x32_bf16 v[32:35], a[40:43], a[88:91], v[32:35]\n\t" \
    "ds_read_b128 a[0:3], %[ard] offset:0\n\t" \
    "v_mfma_f32_16x16x32_bf16 v[36:39], a[44:47], a[88:91], v[36:39]\n\t" \
    "ds_read_b128 a[4:7], %[ard] offset:1024\n\t" \
    "v_mfma_f32_16x16x32_bf16 v[40:43], a[48:51], a[88:91], v[40:43]\n\t" \
    "ds_read_b128 a[8:11], %[ard] offset:2048\n\t" \
    "v_mfma_f32_16x16x32_bf16 v[44:47], a[52:55], a[88:91], v[44:47]\n\t" \
    "ds_read_b128 a[12:15], %[ard] offset:3072\n\t" \
    "v_mfma_f32_16x16x32_bf16 v[48:51], a[56:59], a[88:91], v[48:51]\n\t" \
    "ds_read_b128 a[16:19], %[ard] offset:4096\n\t" \
    "v_mfma_f32_16x16x32_bf16 v[52:55], a[60:63], a[88:91], v[52:55]\n\t" \
    "ds_read_b128 a[20:23], %[ard] offset:5120\n\t" \
    "v_mfma_f32_16x16x32_bf16 v[56:59], a[32:35], a[92:95], v[56:59]\n\t" \
    "ds_read_b128 a[24:27], %[ard] offset:6144\n\t" \
    "v_mfma_f32_16x16x32_bf16 v[60:63], a[36:39], a[92:95], v[60:63]\n\t" \
    "ds_read_b128 a[28:31], %[ard] offset:7168\n\t" \
    "v_mfma_f32_16x16x32_bf16 v[64:67], a[40:43], a[92:95], v[64:67]\n\t" \
    "ds_read_b64_tr_b16 a[68:69], %[brd] offset:10496\n\t" \
    "v_mfma_f32_16x16x32_bf16 v[68:71], a[44:47], a[92:95], v[68:71]\n\t" \
    "ds_read_b64_tr_b16 a[70:71], %[brd] offset:11776\n\t" \
    "v_mfma_f32_16x16x32_bf16 v[72:75], a[48:51], a[92:95], v[72:75]\n\t" \
    "ds_read_b64_tr_b16 a[72:73], %[brd] offset:10752\n\t" \
    "v_mfma_f32_16x16x32_bf16 v[76:79], a[52:55], a[92:95], v[76:79]\n\t" \
    "ds_read_b64_tr_b16 a[74:75], %[brd] offset:12032\n\t" \
    "v_mfma_f32_16x16x32_bf16 v[80:83], a[56:59], a[92:95], v[80:83]\n\t" \
    "ds_read_b64_tr_b16 a[76:77], %[brd] offset:11008\n\t" \
    "v_mfma_f32_16x16x32_bf16 v[84:87], a[60:63], a[92:95], v[84:87]\n\t" \
    "ds_read_b64_tr_b16 a[78:79], %[brd] offset:12288\n\t" \
    "v_mfma_f32_16x16x32_bf16 v[88:91], a[32:35], a[96:99], v[88:91]\n\t" \
    "ds_read_b64_tr_b16 a[80:81], %[brd] offset:11264\n\t" \
    "v_mfma_f32_16x16x32_bf16 v[92:95], a[36:39], a[96:99], v[92:95]\n\t" \
    "ds_read_b64_tr_b16 a[82:83], %[brd] offset:12544\n\t" \
    "v_mfma_f32_16x16x32_bf16 v[96:99], a[40:43], a[96:99], v[96:99]\n\t" \
    "s_add_u32 m0, s48, 5120\n\t" \
    "s_add_u32 s46, %[boff], 123520\n\t" \
    "buffer_load_dwordx4 %[bo0], %[srdb], s46 offen lds\n\t" \
    "v_mfma_f32_16x16x32_bf16 v[100:103], a[44:47], a[96:99], v[100:103]\n\t" \
    "buffer_load_dwordx4 %[bo1], %[srdb], s46 offen offset:1024 lds\n\t" \
    "v_mfma_f32_16x16x32_bf16 v[104:107], a[48:51], a[96:99], v[104:107]\n\t" \
    "buffer_load_dwordx4 %[bo2], %[srdb], s46 offen offset:2048 lds\n\t" \
    "v_mfma_f32_16x16x32_bf16 v[108:111], a[52:55], a[96:99], v[108:111]\n\t" \
    "buffer_load_dwordx4 %[bo3], %[srdb], s46 offen offset:3072 lds\n\t" \
    "v_mfma_f32_16x16x32_bf16 v[112:115], a[56:59], a[96:99], v[112:115]\n\t" \
    "s_add_u32 m0, s48, 9216\n\t" \
    "s_nop 0\n\t" \
    "buffer_load_dwordx4 %[bo4], %[srdb], s46 offen lds\n\t" \
    "v_mfma_f32_16x16x32_bf16 v[116:119], a[60:63], a[96:99], v[116:119]\n\t" \
    "v_mfma_f32_16x16x32_bf16 v[120:123], a[32:35], a[100:103], v[120:123]\n\t" \
    "v_mfma_f32_16x16x32_bf16 v[124:127], a[36:39], a[100:103], v[124:127]\n\t" \
    "v_mfma_f32_16x16x32_bf16 v[128:131], a[40:43], a[100:103], v[128:131]\n\t" \
    "v_mfma_f32_16x16x32_bf16 v[132:135], a[44:47], a[100:103], v[132:135]\n\t" \
    "v_mfma_f32_16x16x32_bf16 v[136:139], a[48:51], a[100:103], v[136:139]\n\t" \
    "v_mfma_f32_16x16x32_bf16 v[140:143], a[52:55], a[100:103], v[140:143]\n\t" \
    "v_mfma_f32_16x16x32_bf16 v[144:147], a[56:59], a[100:103], v[144:147]\n\t" \
    "v_mfma_f32_16x16x32_bf16 v[148:151], a[60:63], a[100:103], v[148:151]\n\t" \
    "v_mfma_f32_16x16x32_bf16 v[152:155], a[32:35], a[104:107], v[152:155]\n\t" \
    "v_mfma_f32_16x16x32_bf16 v[156:159], a[36:39], a[104:107], v[156:159]\n\t" \
    "v_mfma_f32_16x16x32_bf16 v[160:163], a[40:43], a[104:107], v[160:163]\n\t" \
    "v_mfma_f32_16x16x32_bf16 v[164:167], a[44:47], a[104:107], v[164:167]\n\t" \
    "v_mfma_f32_16x16x32_bf16 v[168:171], a[48:51], a[104:107], v[168:171]\n\t" \
    "v_mfma_f32_16x16x32_bf16 v[172:175], a[52:55], a[104:107], v[172:175]\n\t" \
    "v_mfma_f32_16x16x32_bf16 v[176:179], a[56:59], a[104:107], v[176:179]\n\t" \
    "v_mfma_f32_16x16x32_bf16 v[180:183], a[60:63], a[104:107], v[180:183]\n\t" \
    "s_nop 7\n\t" \
    "s_mul_i32 s45, %[osc], 0\n\t" \
    "s_add_u32 s45, s45, %[ooff]\n\t" \
    "v_cvt_pk_bf16_f32 v232, v24, v25\n\t" \
    "v_cvt_pk_bf16_f32 v234, v26, v27\n\t" \
    "v_cvt_pk_bf16_f32 v233, v28, v29\n\t" \
    "v_cvt_pk_bf16_f32 v235, v30, v31\n\t" \
    "buffer_store_dwordx4 v[232:235], %[ov], %[srda], s45 offen sc1\n\t" \
    "s_add_u32 s45, s45, %[osi]\n\t" \
    "v_cvt_pk_bf16_f32 v236, v32, v33\n\t" \
    "v_cvt_pk_bf16_f32 v238, v34, v35\n\t" \
    "v_cvt_pk_bf16_f32 v237, v36, v37\n\t" \
    "v_cvt_pk_bf16_f32 v239, v38, v39\n\t" \
    "buffer_store_dwordx4 v[236:239], %[ov], %[srda], s45 offen sc1\n\t" \
    "s_add_u32 s45, s45, %[osi]\n\t" \
    "v_cvt_pk_bf16_f32 v240, v40, v41\n\t" \
    "v_cvt_pk_bf16_f32 v242, v42, v43\n\t" \
    "v_cvt_pk_bf16_f32 v241, v44, v45\n\t" \
    "v_cvt_pk_bf16_f32 v243, v46, v47\n\t" \
    "buffer_store_dwordx4 v[240:243], %[ov], %[srda], s45 offen sc1\n\t" \
    "s_add_u32 s45, s45, %[osi]\n\t" \
    "v_cvt_pk_bf16_f32 v244, v48, v49\n\t" \
    "v_cvt_pk_bf16_f32 v246, v50, v51\n\t" \
    "v_cvt_pk_bf16_f32 v245, v52, v53\n\t" \
    "v_cvt_pk_bf16_f32 v247, v54, v55\n\t" \
    "buffer_store_dwordx4 v[244:247], %[ov], %[srda], s45 offen sc1\n\t" \
    "s_mul_i32 s45, %[osc], 2\n\t" \
    "s_add_u32 s45, s45, %[ooff]\n\t" \
    "v_cvt_pk_bf16_f32 v248, v56, v57\n\t" \
    "v_cvt_pk_bf16_f32 v250, v58, v59\n\t" \
    "v_cvt_pk_bf16_f32 v249, v60, v61\n\t" \
    "v_cvt_pk_bf16_f32 v251, v62, v63\n\t" \
    "buffer_store_dwordx4 v[248:251], %[ov], %[srda], s45 offen sc1\n\t" \
    "s_add_u32 s45, s45, %[osi]\n\t" \
    "v_cvt_pk_bf16_f32 v252, v64, v65\n\t" \
    "v_cvt_pk_bf16_f32 v254, v66, v67\n\t" \
    "v_cvt_pk_bf16_f32 v253, v68, v69\n\t" \
    "v_cvt_pk_bf16_f32 v255, v70, v71\n\t" \
    "buffer_store_dwordx4 v[252:255], %[ov], %[srda], s45 offen sc1\n\t" \
    "s_add_u32 s45, s45, %[osi]\n\t" \
    "v_cvt_pk_bf16_f32 v232, v72, v73\n\t" \
    "v_cvt_pk_bf16_f32 v234, v74, v75\n\t" \
    "v_cvt_pk_bf16_f32 v233, v76, v77\n\t" \
    "v_cvt_pk_bf16_f32 v235, v78, v79\n\t" \
    "buffer_store_dwordx4 v[232:235], %[ov], %[srda], s45 offen sc1\n\t" \
    "s_add_u32 s45, s45, %[osi]\n\t" \
    "v_cvt_pk_bf16_f32 v236, v80, v81\n\t" \
    "v_cvt_pk_bf16_f32 v238, v82, v83\n\t" \
    "v_cvt_pk_bf16_f32 v237, v84, v85\n\t" \
    "v_cvt_pk_bf16_f32 v239, v86, v87\n\t" \
    "buffer_store_dwordx4 v[236:239], %[ov], %[srda], s45 offen sc1\n\t" \
    "s_mul_i32 s45, %[osc], 4\n\t" \
    "s_add_u32 s45, s45, %[ooff]\n\t" \
    "v_cvt_pk_bf16_f32 v240, v88, v89\n\t" \
    "v_cvt_pk_bf16_f32 v242, v90, v91\n\t" \
    "v_cvt_pk_bf16_f32 v241, v92, v93\n\t" \
    "v_cvt_pk_bf16_f32 v243, v94, v95\n\t" \
    "buffer_store_dwordx4 v[240:243], %[ov], %[srda], s45 offen sc1\n\t" \
    "s_add_u32 s45, s45, %[osi]\n\t" \
    "v_cvt_pk_bf16_f32 v244, v96, v97\n\t" \
    "v_cvt_pk_bf16_f32 v246, v98, v99\n\t" \
    "v_cvt_pk_bf16_f32 v245, v100, v101\n\t" \
    "v_cvt_pk_bf16_f32 v247, v102, v103\n\t" \
    "buffer_store_dwordx4 v[244:247], %[ov], %[srda], s45 offen sc1\n\t" \
    "s_add_u32 s45, s45, %[osi]\n\t" \
    "v_cvt_pk_bf16_f32 v248, v104, v105\n\t" \
    "v_cvt_pk_bf16_f32 v250, v106, v107\n\t" \
    "v_cvt_pk_bf16_f32 v249, v108, v109\n\t" \
    "v_cvt_pk_bf16_f32 v251, v110, v111\n\t" \
    "buffer_store_dwordx4 v[248:251], %[ov], %[srda], s45 offen sc1\n\t" \
    "s_add_u32 s45, s45, %[osi]\n\t" \
    "v_cvt_pk_bf16_f32 v252, v112, v113\n\t" \
    "v_cvt_pk_bf16_f32 v254, v114, v115\n\t" \
    "v_cvt_pk_bf16_f32 v253, v116, v117\n\t" \
    "v_cvt_pk_bf16_f32 v255, v118, v119\n\t" \
    "buffer_store_dwordx4 v[252:255], %[ov], %[srda], s45 offen sc1\n\t" \
    "s_mul_i32 s45, %[osc], 6\n\t" \
    "s_add_u32 s45, s45, %[ooff]\n\t" \
    "v_cvt_pk_bf16_f32 v232, v120, v121\n\t" \
    "v_cvt_pk_bf16_f32 v234, v122, v123\n\t" \
    "v_cvt_pk_bf16_f32 v233, v124, v125\n\t" \
    "v_cvt_pk_bf16_f32 v235, v126, v127\n\t" \
    "buffer_store_dwordx4 v[232:235], %[ov], %[srda], s45 offen sc1\n\t" \
    "s_add_u32 s45, s45, %[osi]\n\t" \
    "v_cvt_pk_bf16_f32 v236, v128, v129\n\t" \
    "v_cvt_pk_bf16_f32 v238, v130, v131\n\t" \
    "v_cvt_pk_bf16_f32 v237, v132, v133\n\t" \
    "v_cvt_pk_bf16_f32 v239, v134, v135\n\t" \
    "buffer_store_dwordx4 v[236:239], %[ov], %[srda], s45 offen sc1\n\t" \
    "s_add_u32 s45, s45, %[osi]\n\t" \
    "v_cvt_pk_bf16_f32 v240, v136, v137\n\t" \
    "v_cvt_pk_bf16_f32 v242, v138, v139\n\t" \
    "v_cvt_pk_bf16_f32 v241, v140, v141\n\t" \
    "v_cvt_pk_bf16_f32 v243, v142, v143\n\t" \
    "buffer_store_dwordx4 v[240:243], %[ov], %[srda], s45 offen sc1\n\t" \
    "s_add_u32 s45, s45, %[osi]\n\t" \
    "v_cvt_pk_bf16_f32 v244, v144, v145\n\t" \
    "v_cvt_pk_bf16_f32 v246, v146, v147\n\t" \
    "v_cvt_pk_bf16_f32 v245, v148, v149\n\t" \
    "v_cvt_pk_bf16_f32 v247, v150, v151\n\t" \
    "buffer_store_dwordx4 v[244:247], %[ov], %[srda], s45 offen sc1\n\t" \
    "s_mul_i32 s45, %[osc], 8\n\t" \
    "s_add_u32 s45, s45, %[ooff]\n\t" \
    "v_cvt_pk_bf16_f32 v248, v152, v153\n\t" \
    "v_cvt_pk_bf16_f32 v250, v154, v155\n\t" \
    "v_cvt_pk_bf16_f32 v249, v156, v157\n\t" \
    "v_cvt_pk_bf16_f32 v251, v158, v159\n\t" \
    "buffer_store_dwordx4 v[248:251], %[ov], %[srda], s45 offen sc1\n\t" \
    "s_add_u32 s45, s45, %[osi]\n\t" \
    "v_cvt_pk_bf16_f32 v252, v160, v161\n\t" \
    "v_cvt_pk_bf16_f32 v254, v162, v163\n\t" \
    "v_cvt_pk_bf16_f32 v253, v164, v165\n\t" \
    "v_cvt_pk_bf16_f32 v255, v166, v167\n\t" \
    "buffer_store_dwordx4 v[252:255], %[ov], %[srda], s45 offen sc1\n\t" \
    "s_add_u32 s45, s45, %[osi]\n\t" \
    "v_cvt_pk_bf16_f32 v232, v168, v169\n\t" \
    "v_cvt_pk_bf16_f32 v234, v170, v171\n\t" \
    "v_cvt_pk_bf16_f32 v233, v172, v173\n\t" \
    "v_cvt_pk_bf16_f32 v235, v174, v175\n\t" \
    "buffer_store_dwordx4 v[232:235], %[ov], %[srda], s45 offen sc1\n\t" \
    "s_add_u32 s45, s45, %[osi]\n\t" \
    "v_cvt_pk_bf16_f32 v236, v176, v177\n\t" \
    "v_cvt_pk_bf16_f32 v238, v178, v179\n\t" \
    "v_cvt_pk_bf16_f32 v237, v180, v181\n\t" \
    "v_cvt_pk_bf16_f32 v239, v182, v183\n\t" \
    "buffer_store_dwordx4 v[236:239], %[ov], %[srda], s45 offen sc1\n\t" \
    "s_waitcnt lgkmcnt(0) vmcnt(30)\n\t" \
    "v_mfma_f32_16x16x32_bf16 v[24:27], a[0:3], a[64:67], 0\n\t" \
    "ds_read_b64_tr_b16 a[88:89], %[brd] offset:15360\n\t" \
    "v_mfma_f32_16x16x32_bf16 v[28:31], a[4:7], a[64:67], 0\n\t" \
    "ds_read_b64_tr_b16 a[90:91], %[brd] offset:16640\n\t" \
    "v_mfma_f32_16x16x32_bf16 v[32:35], a[8:11], a[64:67], 0\n\t" \
    "ds_read_b128 a[32:35], %[ard] offset:8192\n\t" \
    "v_mfma_f32_16x16x32_bf16 v[36:39], a[12:15], a[64:67], 0\n\t" \
    "ds_read_b128 a[36:39], %[ard] offset:9216\n\t" \
    "v_mfma_f32_16x16x32_bf16 v[40:43], a[16:19], a[64:67], 0\n\t" \
    "ds_read_b128 a[40:43], %[ard] offset:10240\n\t" \
    "v_mfma_f32_16x16x32_bf16 v[44:47], a[20:23], a[64:67], 0\n\t" \
    "ds_read_b128 a[44:47], %[ard] offset:11264\n\t" \
    "v_mfma_f32_16x16x32_bf16 v[48:51], a[24:27], a[64:67], 0\n\t" \
    "ds_read_b128 a[48:51], %[ard] offset:12288\n\t" \
    "v_mfma_f32_16x16x32_bf16 v[52:55], a[28:31], a[64:67], 0\n\t" \
    "ds_read_b128 a[52:55], %[ard] offset:13312\n\t" \
    "v_mfma_f32_16x16x32_bf16 v[56:59], a[0:3], a[68:71], 0\n\t" \
    "ds_read_b128 a[56:59], %[ard] offset:14336\n\t" \
    "v_mfma_f32_16x16x32_bf16 v[60:63], a[4:7], a[68:71], 0\n\t" \
    "ds_read_b128 a[60:63], %[ard] offset:15360\n\t" \
    "v_mfma_f32_16x16x32_bf16 v[64:67], a[8:11], a[68:71], 0\n\t" \
    "ds_read_b64_tr_b16 a[92:93], %[brd] offset:15616\n\t" \
    "v_mfma_f32_16x16x32_bf16 v[68:71], a[12:15], a[68:71], 0\n\t" \
    "ds_read_b64_tr_b16 a[94:95], %[brd] offset:16896\n\t" \
    "v_mfma_f32_16x16x32_bf16 v[72:75], a[16:19], a[68:71], 0\n\t" \
    "ds_read_b64_tr_b16 a[96:97], %[brd] offset:15872\n\t" \
    "v_mfma_f32_16x16x32_bf16 v[76:79], a[20:23], a[68:71], 0\n\t" \
    "ds_read_b64_tr_b16 a[98:99], %[brd] offset:17152\n\t" \
    "v_mfma_f32_16x16x32_bf16 v[80:83], a[24:27], a[68:71], 0\n\t" \
    "ds_read_b64_tr_b16 a[100:101], %[brd] offset:16128\n\t" \
    "v_mfma_f32_16x16x32_bf16 v[84:87], a[28:31], a[68:71], 0\n\t" \
    "ds_read_b64_tr_b16 a[102:103], %[brd] offset:17408\n\t" \
    "v_mfma_f32_16x16x32_bf16 v[88:91], a[0:3], a[72:75], 0\n\t" \
    "ds_read_b64_tr_b16 a[104:105], %[brd] offset:16384\n\t" \
    "v_mfma_f32_16x16x32_bf16 v[92:95], a[4:7], a[72:75], 0\n\t" \
    "ds_read_b64_tr_b16 a[106:107], %[brd] offset:17664\n\t" \
    "v_mfma_f32_16x16x32_bf16 v[96:99], a[8:11], a[72:75], 0\n\t" \
    "s_add_u32 m0, s48, 10240\n\t" \
    "s_add_u32 s46, %[boff], 164480\n\t" \
    "buffer_load_dwordx4 %[bo0], %[srdb], s46 offen lds\n\t" \
    "v_mfma_f32_16x16x32_bf16 v[100:103], a[12:15], a[72:75], 0\n\t" \
    "buffer_load_dwordx4 %[bo1], %[srdb], s46 offen offset:1024 lds\n\t" \
    "v_mfma_f32_16x16x32_bf16 v[104:107], a[16:19], a[72:75], 0\n\t" \
    "buffer_load_dwordx4 %[bo2], %[srdb], s46 offen offset:2048 lds\n\t" \
    "v_mfma_f32_16x16x32_bf16 v[108:111], a[20:23], a[72:75], 0\n\t" \
    "buffer_load_dwordx4 %[bo3], %[srdb], s46 offen offset:3072 lds\n\t" \
    "v_mfma_f32_16x16x32_bf16 v[112:115], a[24:27], a[72:75], 0\n\t" \
    "s_add_u32 m0, s48, 14336\n\t" \
    "s_nop 0\n\t" \
    "buffer_load_dwordx4 %[bo4], %[srdb], s46 offen lds\n\t" \
    "v_mfma_f32_16x16x32_bf16 v[116:119], a[28:31], a[72:75], 0\n\t" \
    "v_mfma_f32_16x16x32_bf16 v[120:123], a[0:3], a[76:79], 0\n\t" \
    "v_mfma_f32_16x16x32_bf16 v[124:127], a[4:7], a[76:79], 0\n\t" \
    "v_mfma_f32_16x16x32_bf16 v[128:131], a[8:11], a[76:79], 0\n\t" \
    "v_mfma_f32_16x16x32_bf16 v[132:135], a[12:15], a[76:79], 0\n\t" \
    "v_mfma_f32_16x16x32_bf16 v[136:139], a[16:19], a[76:79], 0\n\t" \
    "v_mfma_f32_16x16x32_bf16 v[140:143], a[20:23], a[76:79], 0\n\t" \
    "v_mfma_f32_16x16x32_bf16 v[144:147], a[24:27], a[76:79], 0\n\t" \
    "v_mfma_f32_16x16x32_bf16 v[148:151], a[28:31], a[76:79], 0\n\t" \
    "v_mfma_f32_16x16x32_bf16 v[152:155], a[0:3], a[80:83], 0\n\t" \
    "v_mfma_f32_16x16x32_bf16 v[156:159], a[4:7], a[80:83], 0\n\t" \
    "v_mfma_f32_16x16x32_bf16 v[160:163], a[8:11], a[80:83], 0\n\t" \
    "v_mfma_f32_16x16x32_bf16 v[164:167], a[12:15], a[80:83], 0\n\t" \
    "v_mfma_f32_16x16x32_bf16 v[168:171], a[16:19], a[80:83], 0\n\t" \
    "v_mfma_f32_16x16x32_bf16 v[172:175], a[20:23], a[80:83], 0\n\t" \
    "v_mfma_f32_16x16x32_bf16 v[176:179], a[24:27], a[80:83], 0\n\t" \
    "v_mfma_f32_16x16x32_bf16 v[180:183], a[28:31], a[80:83], 0\n\t" \
    "s_waitcnt lgkmcnt(0) vmcnt(30)\n\t" \
    "v_mfma_f32_16x16x32_bf16 v[24:27], a[32:35], a[88:91], v[24:27]\n\t" \
    "ds_read_b64_tr_b16 a[64:65], %[brd] offset:0\n\t" \
    "v_mfma_f32_16x16x32_bf16 v[28:31], a[36:39], a[88:91], v[28:31]\n\t" \
    "ds_read_b64_tr_b16 a[66:67], %[brd] offset:1280\n\t" \
    "v_mfma_f32_16x16x32_bf16 v[32:35], a[40:43], a[88:91], v[32:35]\n\t" \
    "ds_read_b128 a[0:3], %[ard] offset:16384\n\t" \
    "v_mfma_f32_16x16x32_bf16 v[36:39], a[44:47], a[88:91], v[36:39]\n\t" \
    "ds_read_b128 a[4:7], %[ard] offset:17408\n\t" \
    "v_mfma_f32_16x16x32_bf16 v[40:43], a[48:51], a[88:91], v[40:43]\n\t" \
    "ds_read_b128 a[8:11], %[ard] offset:18432\n\t" \
    "v_mfma_f32_16x16x32_bf16 v[44:47], a[52:55], a[88:91], v[44:47]\n\t" \
    "ds_read_b128 a[12:15], %[ard] offset:19456\n\t" \
    "v_mfma_f32_16x16x32_bf16 v[48:51], a[56:59], a[88:91], v[48:51]\n\t" \
    "ds_read_b128 a[16:19], %[ard] offset:20480\n\t" \
    "v_mfma_f32_16x16x32_bf16 v[52:55], a[60:63], a[88:91], v[52:55]\n\t" \
    "ds_read_b128 a[20:23], %[ard] offset:21504\n\t" \
    "v_mfma_f32_16x16x32_bf16 v[56:59], a[32:35], a[92:95], v[56:59]\n\t" \
    "ds_read_b128 a[24:27], %[ard] offset:22528\n\t" \
    "v_mfma_f32_16x16x32_bf16 v[60:63], a[36:39], a[92:95], v[60:63]\n\t" \
    "ds_read_b128 a[28:31], %[ard] offset:23552\n\t" \
    "v_mfma_f32_16x16x32_bf16 v[64:67], a[40:43], a[92:95], v[64:67]\n\t" \
    "ds_read_b64_tr_b16 a[68:69], %[brd] offset:256\n\t" \
    "v_mfma_f32_16x16x32_bf16 v[68:71], a[44:47], a[92:95], v[68:71]\n\t" \
    "ds_read_b64_tr_b16 a[70:71], %[brd] offset:1536\n\t" \
    "v_mfma_f32_16x16x32_bf16 v[72:75], a[48:51], a[92:95], v[72:75]\n\t" \
    "ds_read_b64_tr_b16 a[72:73], %[brd] offset:512\n\t" \
    "v_mfma_f32_16x16x32_bf16 v[76:79], a[52:55], a[92:95], v[76:79]\n\t" \
    "ds_read_b64_tr_b16 a[74:75], %[brd] offset:1792\n\t" \
    "v_mfma_f32_16x16x32_bf16 v[80:83], a[56:59], a[92:95], v[80:83]\n\t" \
    "ds_read_b64_tr_b16 a[76:77], %[brd] offset:768\n\t" \
    "v_mfma_f32_16x16x32_bf16 v[84:87], a[60:63], a[92:95], v[84:87]\n\t" \
    "ds_read_b64_tr_b16 a[78:79], %[brd] offset:2048\n\t" \
    "v_mfma_f32_16x16x32_bf16 v[88:91], a[32:35], a[96:99], v[88:91]\n\t" \
    "ds_read_b64_tr_b16 a[80:81], %[brd] offset:1024\n\t" \
    "v_mfma_f32_16x16x32_bf16 v[92:95], a[36:39], a[96:99], v[92:95]\n\t" \
    "ds_read_b64_tr_b16 a[82:83], %[brd] offset:2304\n\t" \
    "v_mfma_f32_16x16x32_bf16 v[96:99], a[40:43], a[96:99], v[96:99]\n\t" \
    "s_add_u32 m0, s48, 15360\n\t" \
    "s_add_u32 s46, %[boff], 205440\n\t" \
    "buffer_load_dwordx4 %[bo0], %[srdb], s46 offen lds\n\t" \
    "v_mfma_f32_16x16x32_bf16 v[100:103], a[44:47], a[96:99], v[100:103]\n\t" \
    "buffer_load_dwordx4 %[bo1], %[srdb], s46 offen offset:1024 lds\n\t" \
    "v_mfma_f32_16x16x32_bf16 v[104:107], a[48:51], a[96:99], v[104:107]\n\t" \
    "buffer_load_dwordx4 %[bo2], %[srdb], s46 offen offset:2048 lds\n\t" \
    "v_mfma_f32_16x16x32_bf16 v[108:111], a[52:55], a[96:99], v[108:111]\n\t" \
    "buffer_load_dwordx4 %[bo3], %[srdb], s46 offen offset:3072 lds\n\t" \
    "v_mfma_f32_16x16x32_bf16 v[112:115], a[56:59], a[96:99], v[112:115]\n\t" \
    "s_add_u32 m0, s48, 19456\n\t" \
    "s_nop 0\n\t" \
    "buffer_load_dwordx4 %[bo4], %[srdb], s46 offen lds\n\t" \
    "v_mfma_f32_16x16x32_bf16 v[116:119], a[60:63], a[96:99], v[116:119]\n\t" \
    "v_mfma_f32_16x16x32_bf16 v[120:123], a[32:35], a[100:103], v[120:123]\n\t" \
    "v_mfma_f32_16x16x32_bf16 v[124:127], a[36:39], a[100:103], v[124:127]\n\t" \
    "v_mfma_f32_16x16x32_bf16 v[128:131], a[40:43], a[100:103], v[128:131]\n\t" \
    "v_mfma_f32_16x16x32_bf16 v[132:135], a[44:47], a[100:103], v[132:135]\n\t" \
    "v_mfma_f32_16x16x32_bf16 v[136:139], a[48:51], a[100:103], v[136:139]\n\t" \
    "v_mfma_f32_16x16x32_bf16 v[140:143], a[52:55], a[100:103], v[140:143]\n\t" \
    "v_mfma_f32_16x16x32_bf16 v[144:147], a[56:59], a[100:103], v[144:147]\n\t" \
    "v_mfma_f32_16x16x32_bf16 v[148:151], a[60:63], a[100:103], v[148:151]\n\t" \
    "v_mfma_f32_16x16x32_bf16 v[152:155], a[32:35], a[104:107], v[152:155]\n\t" \
    "v_mfma_f32_16x16x32_bf16 v[156:159], a[36:39], a[104:107], v[156:159]\n\t" \
    "v_mfma_f32_16x16x32_bf16 v[160:163], a[40:43], a[104:107], v[160:163]\n\t" \
    "v_mfma_f32_16x16x32_bf16 v[164:167], a[44:47], a[104:107], v[164:167]\n\t" \
    "v_mfma_f32_16x16x32_bf16 v[168:171], a[48:51], a[104:107], v[168:171]\n\t" \
    "v_mfma_f32_16x16x32_bf16 v[172:175], a[52:55], a[104:107], v[172:175]\n\t" \
    "v_mfma_f32_16x16x32_bf16 v[176:179], a[56:59], a[104:107], v[176:179]\n\t" \
    "v_mfma_f32_16x16x32_bf16 v[180:183], a[60:63], a[104:107], v[180:183]\n\t" \
    "s_waitcnt lgkmcnt(0) vmcnt(30)\n\t" \
    "v_mfma_f32_16x16x32_bf16 v[24:27], a[0:3], a[64:67], v[24:27]\n\t" \
    "ds_read_b64_tr_b16 a[88:89], %[brd] offset:5120\n\t" \
    "v_mfma_f32_16x16x32_bf16 v[28:31], a[4:7], a[64:67], v[28:31]\n\t" \
    "ds_read_b64_tr_b16 a[90:91], %[brd] offset:6400\n\t" \
    "v_mfma_f32_16x16x32_bf16 v[32:35], a[8:11], a[64:67], v[32:35]\n\t" \
    "ds_read_b128 a[32:35], %[ard] offset:24576\n\t" \
    "v_mfma_f32_16x16x32_bf16 v[36:39], a[12:15], a[64:67], v[36:39]\n\t" \
    "ds_read_b128 a[36:39], %[ard] offset:25600\n\t" \
    "v_mfma_f32_16x16x32_bf16 v[40:43], a[16:19], a[64:67], v[40:43]\n\t" \
    "ds_read_b128 a[40:43], %[ard] offset:26624\n\t" \
    "v_mfma_f32_16x16x32_bf16 v[44:47], a[20:23], a[64:67], v[44:47]\n\t" \
    "ds_read_b128 a[44:47], %[ard] offset:27648\n\t" \
    "v_mfma_f32_16x16x32_bf16 v[48:51], a[24:27], a[64:67], v[48:51]\n\t" \
    "ds_read_b128 a[48:51], %[ard] offset:28672\n\t" \
    "v_mfma_f32_16x16x32_bf16 v[52:55], a[28:31], a[64:67], v[52:55]\n\t" \
    "ds_read_b128 a[52:55], %[ard] offset:29696\n\t" \
    "v_mfma_f32_16x16x32_bf16 v[56:59], a[0:3], a[68:71], v[56:59]\n\t" \
    "ds_read_b128 a[56:59], %[ard] offset:30720\n\t" \
    "v_mfma_f32_16x16x32_bf16 v[60:63], a[4:7], a[68:71], v[60:63]\n\t" \
    "ds_read_b128 a[60:63], %[ard] offset:31744\n\t" \
    "v_mfma_f32_16x16x32_bf16 v[64:67], a[8:11], a[68:71], v[64:67]\n\t" \
    "ds_read_b64_tr_b16 a[92:93], %[brd] offset:5376\n\t" \
    "v_mfma_f32_16x16x32_bf16 v[68:71], a[12:15], a[68:71], v[68:71]\n\t" \
    "ds_read_b64_tr_b16 a[94:95], %[brd] offset:6656\n\t" \
    "v_mfma_f32_16x16x32_bf16 v[72:75], a[16:19], a[68:71], v[72:75]\n\t" \
    "ds_read_b64_tr_b16 a[96:97], %[brd] offset:5632\n\t" \
    "v_mfma_f32_16x16x32_bf16 v[76:79], a[20:23], a[68:71], v[76:79]\n\t" \
    "ds_read_b64_tr_b16 a[98:99], %[brd] offset:6912\n\t" \
    "v_mfma_f32_16x16x32_bf16 v[80:83], a[24:27], a[68:71], v[80:83]\n\t" \
    "ds_read_b64_tr_b16 a[100:101], %[brd] offset:5888\n\t" \
    "v_mfma_f32_16x16x32_bf16 v[84:87], a[28:31], a[68:71], v[84:87]\n\t" \
    "ds_read_b64_tr_b16 a[102:103], %[brd] offset:7168\n\t" \
    "v_mfma_f32_16x16x32_bf16 v[88:91], a[0:3], a[72:75], v[88:91]\n\t" \
    "ds_read_b64_tr_b16 a[104:105], %[brd] offset:6144\n\t" \
    "v_mfma_f32_16x16x32_bf16 v[92:95], a[4:7], a[72:75], v[92:95]\n\t" \
    "ds_read_b64_tr_b16 a[106:107], %[brd] offset:7424\n\t" \
    "v_mfma_f32_16x16x32_bf16 v[96:99], a[8:11], a[72:75], v[96:99]\n\t" \
    "v_mfma_f32_16x16x32_bf16 v[100:103], a[12:15], a[72:75], v[100:103]\n\t" \
    "v_mfma_f32_16x16x32_bf16 v[104:107], a[16:19], a[72:75], v[104:107]\n\t" \
    "v_mfma_f32_16x16x32_bf16 v[108:111], a[20:23], a[72:75], v[108:111]\n\t" \
    "v_mfma_f32_16x16x32_bf16 v[112:115], a[24:27], a[72:75], v[112:115]\n\t" \
    "v_mfma_f32_16x16x32_bf16 v[116:119], a[28:31], a[72:75], v[116:119]\n\t" \
    "v_mfma_f32_16x16x32_bf16 v[120:123], a[0:3], a[76:79], v[120:123]\n\t" \
    "v_mfma_f32_16x16x32_bf16 v[124:127], a[4:7], a[76:79], v[124:127]\n\t" \
    "v_mfma_f32_16x16x32_bf16 v[128:131], a[8:11], a[76:79], v[128:131]\n\t" \
    "v_mfma_f32_16x16x32_bf16 v[132:135], a[12:15], a[76:79], v[132:135]\n\t" \
    "v_mfma_f32_16x16x32_bf16 v[136:139], a[16:19], a[76:79], v[136:139]\n\t" \
    "v_mfma_f32_16x16x32_bf16 v[140:143], a[20:23], a[76:79], v[140:143]\n\t" \
    "v_mfma_f32_16x16x32_bf16 v[144:147], a[24:27], a[76:79], v[144:147]\n\t" \
    "v_mfma_f32_16x16x32_bf16 v[148:151], a[28:31], a[76:79], v[148:151]\n\t" \
    "v_mfma_f32_16x16x32_bf16 v[152:155], a[0:3], a[80:83], v[152:155]\n\t" \
    "v_mfma_f32_16x16x32_bf16 v[156:159], a[4:7], a[80:83], v[156:159]\n\t" \
    "v_mfma_f32_16x16x32_bf16 v[160:163], a[8:11], a[80:83], v[160:163]\n\t" \
    "v_mfma_f32_16x16x32_bf16 v[164:167], a[12:15], a[80:83], v[164:167]\n\t" \
    "v_mfma_f32_16x16x32_bf16 v[168:171], a[16:19], a[80:83], v[168:171]\n\t" \
    "v_mfma_f32_16x16x32_bf16 v[172:175], a[20:23], a[80:83], v[172:175]\n\t" \
    "v_mfma_f32_16x16x32_bf16 v[176:179], a[24:27], a[80:83], v[176:179]\n\t" \
    "v_mfma_f32_16x16x32_bf16 v[180:183], a[28:31], a[80:83], v[180:183]\n\t" \
    "s_waitcnt lgkmcnt(0) vmcnt(5)\n\t" \
    "v_mfma_f32_16x16x32_bf16 v[24:27], a[32:35], a[88:91], v[24:27]\n\t" \
    "ds_read_b64_tr_b16 a[64:65], %[brd] offset:10240\n\t" \
    "v_mfma_f32_16x16x32_bf16 v[28:31], a[36:39], a[88:91], v[28:31]\n\t" \
    "ds_read_b64_tr_b16 a[66:67], %[brd] offset:11520\n\t" \
    "v_mfma_f32_16x16x32_bf16 v[32:35], a[40:43], a[88:91], v[32:35]\n\t" \
    "ds_read_b128 a[0:3], %[ard] offset:32768\n\t" \
    "v_mfma_f32_16x16x32_bf16 v[36:39], a[44:47], a[88:91], v[36:39]\n\t" \
    "ds_read_b128 a[4:7], %[ard] offset:33792\n\t" \
    "v_mfma_f32_16x16x32_bf16 v[40:43], a[48:51], a[88:91], v[40:43]\n\t" \
    "ds_read_b128 a[8:11], %[ard] offset:34816\n\t" \
    "v_mfma_f32_16x16x32_bf16 v[44:47], a[52:55], a[88:91], v[44:47]\n\t" \
    "ds_read_b128 a[12:15], %[ard] offset:35840\n\t" \
    "v_mfma_f32_16x16x32_bf16 v[48:51], a[56:59], a[88:91], v[48:51]\n\t" \
    "ds_read_b128 a[16:19], %[ard] offset:36864\n\t" \
    "v_mfma_f32_16x16x32_bf16 v[52:55], a[60:63], a[88:91], v[52:55]\n\t" \
    "ds_read_b128 a[20:23], %[ard] offset:37888\n\t" \
    "v_mfma_f32_16x16x32_bf16 v[56:59], a[32:35], a[92:95], v[56:59]\n\t" \
    "ds_read_b128 a[24:27], %[ard] offset:38912\n\t" \
    "v_mfma_f32_16x16x32_bf16 v[60:63], a[36:39], a[92:95], v[60:63]\n\t" \
    "ds_read_b128 a[28:31], %[ard] offset:39936\n\t" \
    "v_mfma_f32_16x16x32_bf16 v[64:67], a[40:43], a[92:95], v[64:67]\n\t" \
    "ds_read_b64_tr_b16 a[68:69], %[brd] offset:10496\n\t" \
    "v_mfma_f32_16x16x32_bf16 v[68:71], a[44:47], a[92:95], v[68:71]\n\t" \
    "ds_read_b64_tr_b16 a[70:71], %[brd] offset:11776\n\t" \
    "v_mfma_f32_16x16x32_bf16 v[72:75], a[48:51], a[92:95], v[72:75]\n\t" \
    "ds_read_b64_tr_b16 a[72:73], %[brd] offset:10752\n\t" \
    "v_mfma_f32_16x16x32_bf16 v[76:79], a[52:55], a[92:95], v[76:79]\n\t" \
    "ds_read_b64_tr_b16 a[74:75], %[brd] offset:12032\n\t" \
    "v_mfma_f32_16x16x32_bf16 v[80:83], a[56:59], a[92:95], v[80:83]\n\t" \
    "ds_read_b64_tr_b16 a[76:77], %[brd] offset:11008\n\t" \
    "v_mfma_f32_16x16x32_bf16 v[84:87], a[60:63], a[92:95], v[84:87]\n\t" \
    "ds_read_b64_tr_b16 a[78:79], %[brd] offset:12288\n\t" \
    "v_mfma_f32_16x16x32_bf16 v[88:91], a[32:35], a[96:99], v[88:91]\n\t" \
    "ds_read_b64_tr_b16 a[80:81], %[brd] offset:11264\n\t" \
    "v_mfma_f32_16x16x32_bf16 v[92:95], a[36:39], a[96:99], v[92:95]\n\t" \
    "ds_read_b64_tr_b16 a[82:83], %[brd] offset:12544\n\t" \
    "v_mfma_f32_16x16x32_bf16 v[96:99], a[40:43], a[96:99], v[96:99]\n\t" \
    "v_mfma_f32_16x16x32_bf16 v[100:103], a[44:47], a[96:99], v[100:103]\n\t" \
    "v_mfma_f32_16x16x32_bf16 v[104:107], a[48:51], a[96:99], v[104:107]\n\t" \
    "v_mfma_f32_16x16x32_bf16 v[108:111], a[52:55], a[96:99], v[108:111]\n\t" \
    "v_mfma_f32_16x16x32_bf16 v[112:115], a[56:59], a[96:99], v[112:115]\n\t" \
    "v_mfma_f32_16x16x32_bf16 v[116:119], a[60:63], a[96:99], v[116:119]\n\t" \
    "v_mfma_f32_16x16x32_bf16 v[120:123], a[32:35], a[100:103], v[120:123]\n\t" \
    "v_mfma_f32_16x16x32_bf16 v[124:127], a[36:39], a[100:103], v[124:127]\n\t" \
    "v_mfma_f32_16x16x32_bf16 v[128:131], a[40:43], a[100:103], v[128:131]\n\t" \
    "v_mfma_f32_16x16x32_bf16 v[132:135], a[44:47], a[100:103], v[132:135]\n\t" \
    "v_mfma_f32_16x16x32_bf16 v[136:139], a[48:51], a[100:103], v[136:139]\n\t" \
    "v_mfma_f32_16x16x32_bf16 v[140:143], a[52:55], a[100:103], v[140:143]\n\t" \
    "v_mfma_f32_16x16x32_bf16 v[144:147], a[56:59], a[100:103], v[144:147]\n\t" \
    "v_mfma_f32_16x16x32_bf16 v[148:151], a[60:63], a[100:103], v[148:151]\n\t" \
    "v_mfma_f32_16x16x32_bf16 v[152:155], a[32:35], a[104:107], v[152:155]\n\t" \
    "v_mfma_f32_16x16x32_bf16 v[156:159], a[36:39], a[104:107], v[156:159]\n\t" \
    "v_mfma_f32_16x16x32_bf16 v[160:163], a[40:43], a[104:107], v[160:163]\n\t" \
    "v_mfma_f32_16x16x32_bf16 v[164:167], a[44:47], a[104:107], v[164:167]\n\t" \
    "v_mfma_f32_16x16x32_bf16 v[168:171], a[48:51], a[104:107], v[168:171]\n\t" \
    "v_mfma_f32_16x16x32_bf16 v[172:175], a[52:55], a[104:107], v[172:175]\n\t" \
    "v_mfma_f32_16x16x32_bf16 v[176:179], a[56:59], a[104:107], v[176:179]\n\t" \
    "v_mfma_f32_16x16x32_bf16 v[180:183], a[60:63], a[104:107], v[180:183]\n\t" \
    "s_waitcnt lgkmcnt(0) vmcnt(0)\n\t" \
    "v_mfma_f32_16x16x32_bf16 v[24:27], a[0:3], a[64:67], v[24:27]\n\t" \
    "ds_read_b64_tr_b16 a[88:89], %[brd] offset:15360\n\t" \
    "v_mfma_f32_16x16x32_bf16 v[28:31], a[4:7], a[64:67], v[28:31]\n\t" \
    "ds_read_b64_tr_b16 a[90:91], %[brd] offset:16640\n\t" \
    "v_mfma_f32_16x16x32_bf16 v[32:35], a[8:11], a[64:67], v[32:35]\n\t" \
    "ds_read_b128 a[32:35], %[ard] offset:40960\n\t" \
    "v_mfma_f32_16x16x32_bf16 v[36:39], a[12:15], a[64:67], v[36:39]\n\t" \
    "ds_read_b128 a[36:39], %[ard] offset:41984\n\t" \
    "v_mfma_f32_16x16x32_bf16 v[40:43], a[16:19], a[64:67], v[40:43]\n\t" \
    "ds_read_b128 a[40:43], %[ard] offset:43008\n\t" \
    "v_mfma_f32_16x16x32_bf16 v[44:47], a[20:23], a[64:67], v[44:47]\n\t" \
    "ds_read_b128 a[44:47], %[ard] offset:44032\n\t" \
    "v_mfma_f32_16x16x32_bf16 v[48:51], a[24:27], a[64:67], v[48:51]\n\t" \
    "ds_read_b128 a[48:51], %[ard] offset:45056\n\t" \
    "v_mfma_f32_16x16x32_bf16 v[52:55], a[28:31], a[64:67], v[52:55]\n\t" \
    "ds_read_b128 a[52:55], %[ard] offset:46080\n\t" \
    "v_mfma_f32_16x16x32_bf16 v[56:59], a[0:3], a[68:71], v[56:59]\n\t" \
    "ds_read_b128 a[56:59], %[ard] offset:47104\n\t" \
    "v_mfma_f32_16x16x32_bf16 v[60:63], a[4:7], a[68:71], v[60:63]\n\t" \
    "ds_read_b128 a[60:63], %[ard] offset:48128\n\t" \
    "v_mfma_f32_16x16x32_bf16 v[64:67], a[8:11], a[68:71], v[64:67]\n\t" \
    "ds_read_b64_tr_b16 a[92:93], %[brd] offset:15616\n\t" \
    "v_mfma_f32_16x16x32_bf16 v[68:71], a[12:15], a[68:71], v[68:71]\n\t" \
    "ds_read_b64_tr_b16 a[94:95], %[brd] offset:16896\n\t" \
    "v_mfma_f32_16x16x32_bf16 v[72:75], a[16:19], a[68:71], v[72:75]\n\t" \
    "ds_read_b64_tr_b16 a[96:97], %[brd] offset:15872\n\t" \
    "v_mfma_f32_16x16x32_bf16 v[76:79], a[20:23], a[68:71], v[76:79]\n\t" \
    "ds_read_b64_tr_b16 a[98:99], %[brd] offset:17152\n\t" \
    "v_mfma_f32_16x16x32_bf16 v[80:83], a[24:27], a[68:71], v[80:83]\n\t" \
    "ds_read_b64_tr_b16 a[100:101], %[brd] offset:16128\n\t" \
    "v_mfma_f32_16x16x32_bf16 v[84:87], a[28:31], a[68:71], v[84:87]\n\t" \
    "ds_read_b64_tr_b16 a[102:103], %[brd] offset:17408\n\t" \
    "v_mfma_f32_16x16x32_bf16 v[88:91], a[0:3], a[72:75], v[88:91]\n\t" \
    "ds_read_b64_tr_b16 a[104:105], %[brd] offset:16384\n\t" \
    "v_mfma_f32_16x16x32_bf16 v[92:95], a[4:7], a[72:75], v[92:95]\n\t" \
    "ds_read_b64_tr_b16 a[106:107], %[brd] offset:17664\n\t" \
    "v_mfma_f32_16x16x32_bf16 v[96:99], a[8:11], a[72:75], v[96:99]\n\t" \
    "v_mfma_f32_16x16x32_bf16 v[100:103], a[12:15], a[72:75], v[100:103]\n\t" \
    "v_mfma_f32_16x16x32_bf16 v[104:107], a[16:19], a[72:75], v[104:107]\n\t" \
    "v_mfma_f32_16x16x32_bf16 v[108:111], a[20:23], a[72:75], v[108:111]\n\t" \
    "v_mfma_f32_16x16x32_bf16 v[112:115], a[24:27], a[72:75], v[112:115]\n\t" \
    "v_mfma_f32_16x16x32_bf16 v[116:119], a[28:31], a[72:75], v[116:119]\n\t" \
    "v_mfma_f32_16x16x32_bf16 v[120:123], a[0:3], a[76:79], v[120:123]\n\t" \
    "v_mfma_f32_16x16x32_bf16 v[124:127], a[4:7], a[76:79], v[124:127]\n\t" \
    "v_mfma_f32_16x16x32_bf16 v[128:131], a[8:11], a[76:79], v[128:131]\n\t" \
    "v_mfma_f32_16x16x32_bf16 v[132:135], a[12:15], a[76:79], v[132:135]\n\t" \
    "v_mfma_f32_16x16x32_bf16 v[136:139], a[16:19], a[76:79], v[136:139]\n\t" \
    "v_mfma_f32_16x16x32_bf16 v[140:143], a[20:23], a[76:79], v[140:143]\n\t" \
    "v_mfma_f32_16x16x32_bf16 v[144:147], a[24:27], a[76:79], v[144:147]\n\t" \
    "v_mfma_f32_16x16x32_bf16 v[148:151], a[28:31], a[76:79], v[148:151]\n\t" \
    "v_mfma_f32_16x16x32_bf16 v[152:155], a[0:3], a[80:83], v[152:155]\n\t" \
    "v_mfma_f32_16x16x32_bf16 v[156:159], a[4:7], a[80:83], v[156:159]\n\t" \
    "v_mfma_f32_16x16x32_bf16 v[160:163], a[8:11], a[80:83], v[160:163]\n\t" \
    "v_mfma_f32_16x16x32_bf16 v[164:167], a[12:15], a[80:83], v[164:167]\n\t" \
    "v_mfma_f32_16x16x32_bf16 v[168:171], a[16:19], a[80:83], v[168:171]\n\t" \
    "v_mfma_f32_16x16x32_bf16 v[172:175], a[20:23], a[80:83], v[172:175]\n\t" \
    "v_mfma_f32_16x16x32_bf16 v[176:179], a[24:27], a[80:83], v[176:179]\n\t" \
    "v_mfma_f32_16x16x32_bf16 v[180:183], a[28:31], a[80:83], v[180:183]\n\t" \
    "s_waitcnt lgkmcnt(0)\n\t" \
    "v_mfma_f32_16x16x32_bf16 v[24:27], a[32:35], a[88:91], v[24:27]\n\t" \
    "v_mfma_f32_16x16x32_bf16 v[28:31], a[36:39], a[88:91], v[28:31]\n\t" \
    "v_mfma_f32_16x16x32_bf16 v[32:35], a[40:43], a[88:91], v[32:35]\n\t" \
    "v_mfma_f32_16x16x32_bf16 v[36:39], a[44:47], a[88:91], v[36:39]\n\t" \
    "v_mfma_f32_16x16x32_bf16 v[40:43], a[48:51], a[88:91], v[40:43]\n\t" \
    "v_mfma_f32_16x16x32_bf16 v[44:47], a[52:55], a[88:91], v[44:47]\n\t" \
    "v_mfma_f32_16x16x32_bf16 v[48:51], a[56:59], a[88:91], v[48:51]\n\t" \
    "v_mfma_f32_16x16x32_bf16 v[52:55], a[60:63], a[88:91], v[52:55]\n\t" \
    "v_mfma_f32_16x16x32_bf16 v[56:59], a[32:35], a[92:95], v[56:59]\n\t" \
    "v_mfma_f32_16x16x32_bf16 v[60:63], a[36:39], a[92:95], v[60:63]\n\t" \
    "v_mfma_f32_16x16x32_bf16 v[64:67], a[40:43], a[92:95], v[64:67]\n\t" \
    "v_mfma_f32_16x16x32_bf16 v[68:71], a[44:47], a[92:95], v[68:71]\n\t" \
    "v_mfma_f32_16x16x32_bf16 v[72:75], a[48:51], a[92:95], v[72:75]\n\t" \
    "v_mfma_f32_16x16x32_bf16 v[76:79], a[52:55], a[92:95], v[76:79]\n\t" \
    "v_mfma_f32_16x16x32_bf16 v[80:83], a[56:59], a[92:95], v[80:83]\n\t" \
    "v_mfma_f32_16x16x32_bf16 v[84:87], a[60:63], a[92:95], v[84:87]\n\t" \
    "v_mfma_f32_16x16x32_bf16 v[88:91], a[32:35], a[96:99], v[88:91]\n\t" \
    "v_mfma_f32_16x16x32_bf16 v[92:95], a[36:39], a[96:99], v[92:95]\n\t" \
    "v_mfma_f32_16x16x32_bf16 v[96:99], a[40:43], a[96:99], v[96:99]\n\t" \
    "v_mfma_f32_16x16x32_bf16 v[100:103], a[44:47], a[96:99], v[100:103]\n\t" \
    "v_mfma_f32_16x16x32_bf16 v[104:107], a[48:51], a[96:99], v[104:107]\n\t" \
    "v_mfma_f32_16x16x32_bf16 v[108:111], a[52:55], a[96:99], v[108:111]\n\t" \
    "v_mfma_f32_16x16x32_bf16 v[112:115], a[56:59], a[96:99], v[112:115]\n\t" \
    "v_mfma_f32_16x16x32_bf16 v[116:119], a[60:63], a[96:99], v[116:119]\n\t" \
    "v_mfma_f32_16x16x32_bf16 v[120:123], a[32:35], a[100:103], v[120:123]\n\t" \
    "v_mfma_f32_16x16x32_bf16 v[124:127], a[36:39], a[100:103], v[124:127]\n\t" \
    "v_mfma_f32_16x16x32_bf16 v[128:131], a[40:43], a[100:103], v[128:131]\n\t" \
    "v_mfma_f32_16x16x32_bf16 v[132:135], a[44:47], a[100:103], v[132:135]\n\t" \
    "v_mfma_f32_16x16x32_bf16 v[136:139], a[48:51], a[100:103], v[136:139]\n\t" \
    "v_mfma_f32_16x16x32_bf16 v[140:143], a[52:55], a[100:103], v[140:143]\n\t" \
    "v_mfma_f32_16x16x32_bf16 v[144:147], a[56:59], a[100:103], v[144:147]\n\t" \
    "v_mfma_f32_16x16x32_bf16 v[148:151], a[60:63], a[100:103], v[148:151]\n\t" \
    "v_mfma_f32_16x16x32_bf16 v[152:155], a[32:35], a[104:107], v[152:155]\n\t" \
    "v_mfma_f32_16x16x32_bf16 v[156:159], a[36:39], a[104:107], v[156:159]\n\t" \
    "v_mfma_f32_16x16x32_bf16 v[160:163], a[40:43], a[104:107], v[160:163]\n\t" \
    "v_mfma_f32_16x16x32_bf16 v[164:167], a[44:47], a[104:107], v[164:167]\n\t" \
    "v_mfma_f32_16x16x32_bf16 v[168:171], a[48:51], a[104:107], v[168:171]\n\t" \
    "v_mfma_f32_16x16x32_bf16 v[172:175], a[52:55], a[104:107], v[172:175]\n\t" \
    "v_mfma_f32_16x16x32_bf16 v[176:179], a[56:59], a[104:107], v[176:179]\n\t" \
    "v_mfma_f32_16x16x32_bf16 v[180:183], a[60:63], a[104:107], v[180:183]\n\t" \
    "s_nop 7\n\t" \
    "s_mul_i32 s45, %[osc], 40\n\t" \
    "s_add_u32 s45, s45, %[ooff]\n\t" \
    "v_cvt_pk_bf16_f32 v232, v24, v25\n\t" \
    "v_cvt_pk_bf16_f32 v234, v26, v27\n\t" \
    "v_cvt_pk_bf16_f32 v233, v28, v29\n\t" \
    "v_cvt_pk_bf16_f32 v235, v30, v31\n\t" \
    "buffer_store_dwordx4 v[232:235], %[ov], %[srda], s45 offen sc1\n\t" \
    "s_add_u32 s45, s45, %[osi]\n\t" \
    "v_cvt_pk_bf16_f32 v236, v32, v33\n\t" \
    "v_cvt_pk_bf16_f32 v238, v34, v35\n\t" \
    "v_cvt_pk_bf16_f32 v237, v36, v37\n\t" \
    "v_cvt_pk_bf16_f32 v239, v38, v39\n\t" \
    "buffer_store_dwordx4 v[236:239], %[ov], %[srda], s45 offen sc1\n\t" \
    "s_add_u32 s45, s45, %[osi]\n\t" \
    "v_cvt_pk_bf16_f32 v240, v40, v41\n\t" \
    "v_cvt_pk_bf16_f32 v242, v42, v43\n\t" \
    "v_cvt_pk_bf16_f32 v241, v44, v45\n\t" \
    "v_cvt_pk_bf16_f32 v243, v46, v47\n\t" \
    "buffer_store_dwordx4 v[240:243], %[ov], %[srda], s45 offen sc1\n\t" \
    "s_add_u32 s45, s45, %[osi]\n\t" \
    "v_cvt_pk_bf16_f32 v244, v48, v49\n\t" \
    "v_cvt_pk_bf16_f32 v246, v50, v51\n\t" \
    "v_cvt_pk_bf16_f32 v245, v52, v53\n\t" \
    "v_cvt_pk_bf16_f32 v247, v54, v55\n\t" \
    "buffer_store_dwordx4 v[244:247], %[ov], %[srda], s45 offen sc1\n\t" \
    "s_mul_i32 s45, %[osc], 42\n\t" \
    "s_add_u32 s45, s45, %[ooff]\n\t" \
    "v_cvt_pk_bf16_f32 v248, v56, v57\n\t" \
    "v_cvt_pk_bf16_f32 v250, v58, v59\n\t" \
    "v_cvt_pk_bf16_f32 v249, v60, v61\n\t" \
    "v_cvt_pk_bf16_f32 v251, v62, v63\n\t" \
    "buffer_store_dwordx4 v[248:251], %[ov], %[srda], s45 offen sc1\n\t" \
    "s_add_u32 s45, s45, %[osi]\n\t" \
    "v_cvt_pk_bf16_f32 v252, v64, v65\n\t" \
    "v_cvt_pk_bf16_f32 v254, v66, v67\n\t" \
    "v_cvt_pk_bf16_f32 v253, v68, v69\n\t" \
    "v_cvt_pk_bf16_f32 v255, v70, v71\n\t" \
    "buffer_store_dwordx4 v[252:255], %[ov], %[srda], s45 offen sc1\n\t" \
    "s_add_u32 s45, s45, %[osi]\n\t" \
    "v_cvt_pk_bf16_f32 v232, v72, v73\n\t" \
    "v_cvt_pk_bf16_f32 v234, v74, v75\n\t" \
    "v_cvt_pk_bf16_f32 v233, v76, v77\n\t" \
    "v_cvt_pk_bf16_f32 v235, v78, v79\n\t" \
    "buffer_store_dwordx4 v[232:235], %[ov], %[srda], s45 offen sc1\n\t" \
    "s_add_u32 s45, s45, %[osi]\n\t" \
    "v_cvt_pk_bf16_f32 v236, v80, v81\n\t" \
    "v_cvt_pk_bf16_f32 v238, v82, v83\n\t" \
    "v_cvt_pk_bf16_f32 v237, v84, v85\n\t" \
    "v_cvt_pk_bf16_f32 v239, v86, v87\n\t" \
    "buffer_store_dwordx4 v[236:239], %[ov], %[srda], s45 offen sc1\n\t" \
    "s_mul_i32 s45, %[osc], 44\n\t" \
    "s_add_u32 s45, s45, %[ooff]\n\t" \
    "v_cvt_pk_bf16_f32 v240, v88, v89\n\t" \
    "v_cvt_pk_bf16_f32 v242, v90, v91\n\t" \
    "v_cvt_pk_bf16_f32 v241, v92, v93\n\t" \
    "v_cvt_pk_bf16_f32 v243, v94, v95\n\t" \
    "buffer_store_dwordx4 v[240:243], %[ov], %[srda], s45 offen sc1\n\t" \
    "s_add_u32 s45, s45, %[osi]\n\t" \
    "v_cvt_pk_bf16_f32 v244, v96, v97\n\t" \
    "v_cvt_pk_bf16_f32 v246, v98, v99\n\t" \
    "v_cvt_pk_bf16_f32 v245, v100, v101\n\t" \
    "v_cvt_pk_bf16_f32 v247, v102, v103\n\t" \
    "buffer_store_dwordx4 v[244:247], %[ov], %[srda], s45 offen sc1\n\t" \
    "s_add_u32 s45, s45, %[osi]\n\t" \
    "v_cvt_pk_bf16_f32 v248, v104, v105\n\t" \
    "v_cvt_pk_bf16_f32 v250, v106, v107\n\t" \
    "v_cvt_pk_bf16_f32 v249, v108, v109\n\t" \
    "v_cvt_pk_bf16_f32 v251, v110, v111\n\t" \
    "buffer_store_dwordx4 v[248:251], %[ov], %[srda], s45 offen sc1\n\t" \
    "s_add_u32 s45, s45, %[osi]\n\t" \
    "v_cvt_pk_bf16_f32 v252, v112, v113\n\t" \
    "v_cvt_pk_bf16_f32 v254, v114, v115\n\t" \
    "v_cvt_pk_bf16_f32 v253, v116, v117\n\t" \
    "v_cvt_pk_bf16_f32 v255, v118, v119\n\t" \
    "buffer_store_dwordx4 v[252:255], %[ov], %[srda], s45 offen sc1\n\t" \
    "s_mul_i32 s45, %[osc], 46\n\t" \
    "s_add_u32 s45, s45, %[ooff]\n\t" \
    "v_cvt_pk_bf16_f32 v232, v120, v121\n\t" \
    "v_cvt_pk_bf16_f32 v234, v122, v123\n\t" \
    "v_cvt_pk_bf16_f32 v233, v124, v125\n\t" \
    "v_cvt_pk_bf16_f32 v235, v126, v127\n\t" \
    "buffer_store_dwordx4 v[232:235], %[ov], %[srda], s45 offen sc1\n\t" \
    "s_add_u32 s45, s45, %[osi]\n\t" \
    "v_cvt_pk_bf16_f32 v236, v128, v129\n\t" \
    "v_cvt_pk_bf16_f32 v238, v130, v131\n\t" \
    "v_cvt_pk_bf16_f32 v237, v132, v133\n\t" \
    "v_cvt_pk_bf16_f32 v239, v134, v135\n\t" \
    "buffer_store_dwordx4 v[236:239], %[ov], %[srda], s45 offen sc1\n\t" \
    "s_add_u32 s45, s45, %[osi]\n\t" \
    "v_cvt_pk_bf16_f32 v240, v136, v137\n\t" \
    "v_cvt_pk_bf16_f32 v242, v138, v139\n\t" \
    "v_cvt_pk_bf16_f32 v241, v140, v141\n\t" \
    "v_cvt_pk_bf16_f32 v243, v142, v143\n\t" \
    "buffer_store_dwordx4 v[240:243], %[ov], %[srda], s45 offen sc1\n\t" \
    "s_add_u32 s45, s45, %[osi]\n\t" \
    "v_cvt_pk_bf16_f32 v244, v144, v145\n\t" \
    "v_cvt_pk_bf16_f32 v246, v146, v147\n\t" \
    "v_cvt_pk_bf16_f32 v245, v148, v149\n\t" \
    "v_cvt_pk_bf16_f32 v247, v150, v151\n\t" \
    "buffer_store_dwordx4 v[244:247], %[ov], %[srda], s45 offen sc1\n\t" \
    "s_mul_i32 s45, %[osc], 48\n\t" \
    "s_add_u32 s45, s45, %[ooff]\n\t" \
    "v_cvt_pk_bf16_f32 v248, v152, v153\n\t" \
    "v_cvt_pk_bf16_f32 v250, v154, v155\n\t" \
    "v_cvt_pk_bf16_f32 v249, v156, v157\n\t" \
    "v_cvt_pk_bf16_f32 v251, v158, v159\n\t" \
    "buffer_store_dwordx4 v[248:251], %[ov], %[srda], s45 offen sc1\n\t" \
    "s_add_u32 s45, s45, %[osi]\n\t" \
    "v_cvt_pk_bf16_f32 v252, v160, v161\n\t" \
    "v_cvt_pk_bf16_f32 v254, v162, v163\n\t" \
    "v_cvt_pk_bf16_f32 v253, v164, v165\n\t" \
    "v_cvt_pk_bf16_f32 v255, v166, v167\n\t" \
    "buffer_store_dwordx4 v[252:255], %[ov], %[srda], s45 offen sc1\n\t" \
    "s_add_u32 s45, s45, %[osi]\n\t" \
    "v_cvt_pk_bf16_f32 v232, v168, v169\n\t" \
    "v_cvt_pk_bf16_f32 v234, v170, v171\n\t" \
    "v_cvt_pk_bf16_f32 v233, v172, v173\n\t" \
    "v_cvt_pk_bf16_f32 v235, v174, v175\n\t" \
    "buffer_store_dwordx4 v[232:235], %[ov], %[srda], s45 offen sc1\n\t" \
    "s_add_u32 s45, s45, %[osi]\n\t" \
    "v_cvt_pk_bf16_f32 v236, v176, v177\n\t" \
    "v_cvt_pk_bf16_f32 v238, v178, v179\n\t" \
    "v_cvt_pk_bf16_f32 v237, v180, v181\n\t" \
    "v_cvt_pk_bf16_f32 v239, v182, v183\n\t" \
    "buffer_store_dwordx4 v[236:239], %[ov], %[srda], s45 offen sc1\n\t"
#define RK_ASM_GTH \
    "s_mov_b32 s41, %[aoff]\n\t" \
    "s_mov_b32 s42, %[boff]\n\t" \
    "s_mov_b32 s44, 0\n\t" \
    "s_mov_b32 s43, 0\n\t" \
    "s_mov_b32 s48, %[bm]\n\t" \
    "s_mov_b32 s49, 0x40000000\n\t" \
    "s_mov_b32 s40, 0\n\t" \
    "s_barrier\n\t" \
    "s_add_u32 m0, s44, %[wq]\n\t" \
    "s_cmp_lt_u32 s41, %[aend]\n\t" \
    "s_cselect_b32 s45, s41, s49\n\t" \
    "buffer_load_dwordx4 %[asrc], %[srda], s45 offen lds\n\t" \
    "buffer_load_dwordx4 %[asrc], %[srda], s45 offen offset:1024 lds\n\t" \
    "s_add_u32 s41, s41, 16384\n\t" \
    "s_add_u32 s44, s44, 16384\n\t" \
    "s_cmp_eq_u32 s44, 49152\n\t" \
    "s_cselect_b32 s44, 0, s44\n\t" \
    "s_add_u32 m0, s48, 0\n\t" \
    "s_cmp_lt_u32 s42, %[bend]\n\t" \
    "s_cselect_b32 s46, s42, s49\n\t" \
    "buffer_load_dwordx4 %[bo0], %[srdb], s46 offen lds\n\t" \
    "buffer_load_dwordx4 %[bo1], %[srdb], s46 offen offset:1024 lds\n\t" \
    "buffer_load_dwordx4 %[bo2], %[srdb], s46 offen offset:2048 lds\n\t" \
    "buffer_load_dwordx4 %[bo3], %[srdb], s46 offen offset:3072 lds\n\t" \
    "s_add_u32 m0, s48, 4096\n\t" \
    "s_add_u32 s42, s42, %[bstep]\n\t" \
    "buffer_load_dwordx4 %[bo4], %[srdb], s46 offen lds\n\t" \
    "buffer_load_dwordx4 %[bo5], %[srdb], s46 offen offset:1024 lds\n\t" \
    "s_add_u32 m0, s44, %[wq]\n\t" \
    "s_cmp_lt_u32 s41, %[aend]\n\t" \
    "s_cselect_b32 s45, s41, s49\n\t" \
    "buffer_load_dwordx4 %[asrc], %[srda], s45 offen lds\n\t" \
    "buffer_load_dwordx4 %[asrc], %[srda], s45 offen offset:1024 lds\n\t" \
    "s_add_u32 s41, s41, 16384\n\t" \
    "s_add_u32 s44, s44, 16384\n\t" \
    "s_cmp_eq_u32 s44, 49152\n\t" \
    "s_cselect_b32 s44, 0, s44\n\t" \
    "s_add_u32 m0, s48, 6144\n\t" \
    "s_cmp_lt_u32 s42, %[bend]\n\t" \
    "s_cselect_b32 s46, s42, s49\n\t" \
    "buffer_load_dwordx4 %[bo0], %[srdb], s46 offen lds\n\t" \
    "buffer_load_dwordx4 %[bo1], %[srdb], s46 offen offset:1024 lds\n\t" \
    "buffer_load_dwordx4 %[bo2], %[srdb], s46 offen offset:2048 lds\n\t" \
    "buffer_load_dwordx4 %[bo3], %[srdb], s46 offen offset:3072 lds\n\t" \
    "s_add_u32 m0, s48, 10240\n\t" \
    "s_add_u32 s42, s42, %[bstep]\n\t" \
    "buffer_load_dwordx4 %[bo4], %[srdb], s46 offen lds\n\t" \
    "buffer_load_dwordx4 %[bo5], %[srdb], s46 offen offset:1024 lds\n\t" \
    "s_add_u32 m0, s48, 12288\n\t" \
    "s_cmp_lt_u32 s42, %[bend]\n\t" \
    "s_cselect_b32 s46, s42, s49\n\t" \
    "buffer_load_dwordx4 %[bo0], %[srdb], s46 offen lds\n\t" \
    "buffer_load_dwordx4 %[bo1], %[srdb], s46 offen offset:1024 lds\n\t" \
    "buffer_load_dwordx4 %[bo2], %[srdb], s46 offen offset:2048 lds\n\t" \
    "buffer_load_dwordx4 %[bo3], %[srdb], s46 offen offset:3072 lds\n\t" \
    "s_add_u32 m0, s48, 16384\n\t" \
    "s_add_u32 s42, s42, %[bstep]\n\t" \
    "buffer_load_dwordx4 %[bo4], %[srdb], s46 offen lds\n\t" \
    "buffer_load_dwordx4 %[bo5], %[srdb], s46 offen offset:1024 lds\n\t" \
    "s_add_u32 m0, s44, %[wq]\n\t" \
    "s_cmp_lt_u32 s41, %[aend]\n\t" \
    "s_cselect_b32 s45, s41, s49\n\t" \
    "buffer_load_dwordx4 %[asrc], %[srda], s45 offen lds\n\t" \
    "buffer_load_dwordx4 %[asrc], %[srda], s45 offen offset:1024 lds\n\t" \
    "s_add_u32 s41, s41, 16384\n\t" \
    "s_add_u32 s44, s44, 16384\n\t" \
    "s_cmp_eq_u32 s44, 49152\n\t" \
    "s_cselect_b32 s44, 0, s44\n\t" \
    "s_add_u32 m0, s48, 18432\n\t" \
    "s_cmp_lt_u32 s42, %[bend]\n\t" \
    "s_cselect_b32 s46, s42, s49\n\t" \
    "buffer_load_dwordx4 %[bo0], %[srdb], s46 offen lds\n\t" \
    "buffer_load_dwordx4 %[bo1], %[srdb], s46 offen offset:1024 lds\n\t" \
    "buffer_load_dwordx4 %[bo2], %[srdb], s46 offen offset:2048 lds\n\t" \
    "buffer_load_dwordx4 %[bo3], %[srdb], s46 offen offset:3072 lds\n\t" \
    "s_add_u32 m0, s48, 22528\n\t" \
    "s_add_u32 s42, s42, %[bstep]\n\t" \
    "buffer_load_dwordx4 %[bo4], %[srdb], s46 offen lds\n\t" \
    "buffer_load_dwordx4 %[bo5], %[srdb], s46 offen offset:1024 lds\n\t" \
    "v_mov_b64 v[24:25], 0\n\t" \
    "v_mov_b64 v[26:27], 0\n\t" \
    "v_mov_b64 v[28:29], 0\n\t" \
    "v_mov_b64 v[30:31], 0\n\t" \
    "v_mov_b64 v[32:33], 0\n\t" \
    "v_mov_b64 v[34:35], 0\n\t" \
    "v_mov_b64 v[36:37], 0\n\t" \
    "v_mov_b64 v[38:39], 0\n\t" \
    "v_mov_b64 v[40:41], 0\n\t" \
    "v_mov_b64 v[42:43], 0\n\t" \
    "v_mov_b64 v[44:45], 0\n\t" \
    "v_mov_b64 v[46:47], 0\n\t" \
    "v_mov_b64 v[48:49], 0\n\t" \
    "v_mov_b64 v[50:51], 0\n\t" \
    "v_mov_b64 v[52:53], 0\n\t" \
    "v_mov_b64 v[54:55], 0\n\t" \
    "v_mov_b64 v[56:57], 0\n\t" \
    "v_mov_b64 v[58:59], 0\n\t" \
    "v_mov_b64 v[60:61], 0\n\t" \
    "v_mov_b64 v[62:63], 0\n\t" \
    "v_mov_b64 v[64:65], 0\n\t" \
    "v_mov_b64 v[66:67], 0\n\t" \
    "v_mov_b64 v[68:69], 0\n\t" \
    "v_mov_b64 v[70:71], 0\n\t" \
    "v_mov_b64 v[72:73], 0\n\t" \
    "v_mov_b64 v[74:75], 0\n\t" \
    "v_mov_b64 v[76:77], 0\n\t" \
    "v_mov_b64 v[78:79], 0\n\t" \
    "v_mov_b64 v[80:81], 0\n\t" \
    "v_mov_b64 v[82:83], 0\n\t" \
    "v_mov_b64 v[84:85], 0\n\t" \
    "v_mov_b64 v[86:87], 0\n\t" \
    "v_mov_b64 v[88:89], 0\n\t" \
    "v_mov_b64 v[90:91], 0\n\t" \
    "v_mov_b64 v[92:93], 0\n\t" \
    "v_mov_b64 v[94:95], 0\n\t" \
    "v_mov_b64 v[96:97], 0\n\t" \
    "v_mov_b64 v[98:99], 0\n\t" \
    "v_mov_b64 v[100:101], 0\n\t" \
    "v_mov_b64 v[102:103], 0\n\t" \
    "v_mov_b64 v[104:105], 0\n\t" \
    "v_mov_b64 v[106:107], 0\n\t" \
    "v_mov_b64 v[108:109], 0\n\t" \
    "v_mov_b64 v[110:111], 0\n\t" \
    "v_mov_b64 v[112:113], 0\n\t" \
    "v_mov_b64 v[114:115], 0\n\t" \
    "v_mov_b64 v[116:117], 0\n\t" \
    "v_mov_b64 v[118:119], 0\n\t" \
    "v_mov_b32 v232, %[ard]\n\t" \
    "s_waitcnt vmcnt(22)\n\t" \
    "s_barrier\n\t" \
    "ds_read_b64_tr_b16 a[64:65], %[brd] offset:0\n\t" \
    "ds_read_b64_tr_b16 a[66:67], %[brd] offset:1536\n\t" \
    "ds_read_b128 a[0:3], v232 offset:0\n\t" \
    "ds_read_b128 a[4:7], v232 offset:1024\n\t" \
    "ds_read_b128 a[8:11], v232 offset:2048\n\t" \
    "ds_read_b128 a[12:15], v232 offset:3072\n\t" \
    "ds_read_b64_tr_b16 a[68:69], %[brd] offset:256\n\t" \
    "ds_read_b64_tr_b16 a[70:71], %[brd] offset:1792\n\t" \
    "ds_read_b64_tr_b16 a[72:73], %[brd] offset:512\n\t" \
    "ds_read_b64_tr_b16 a[74:75], %[brd] offset:2048\n\t" \
    "ds_read_b64_tr_b16 a[76:77], %[brd] offset:768\n\t" \
    "ds_read_b64_tr_b16 a[78:79], %[brd] offset:2304\n\t" \
    "ds_read_b64_tr_b16 a[80:81], %[brd] offset:1024\n\t" \
    "ds_read_b64_tr_b16 a[82:83], %[brd] offset:2560\n\t" \
    "ds_read_b64_tr_b16 a[84:85], %[brd] offset:1280\n\t" \
    "ds_read_b64_tr_b16 a[86:87], %[brd] offset:2816\n\t" \
    "tl_1_%=:\n\t" \
    "s_waitcnt vmcnt(14) lgkmcnt(0)\n\t" \
    "v_mfma_f32_16x16x32_bf16 v[24:27], a[64:67], a[0:3], v[24:27]\n\t" \
    "ds_read_b64_tr_b16 a[88:89], %[brd] offset:6144\n\t" \
    "v_mfma_f32_16x16x32_bf16 v[28:31], a[64:67], a[4:7], v[28:31]\n\t" \
    "ds_read_b64_tr_b16 a[90:91], %[brd] offset:7680\n\t" \
    "v_mfma_f32_16x16x32_bf16 v[32:35], a[64:67], a[8:11], v[32:35]\n\t" \
    "ds_read_b128 a[32:35], v232 offset:4096\n\t" \
    "v_mfma_f32_16x16x32_bf16 v[36:39], a[64:67], a[12:15], v[36:39]\n\t" \
    "ds_read_b128 a[36:39], v232 offset:5120\n\t" \
    "v_mfma_f32_16x16x32_bf16 v[40:43], a[68:71], a[0:3], v[40:43]\n\t" \
    "ds_read_b128 a[40:43], v232 offset:6144\n\t" \
    "v_mfma_f32_16x16x32_bf16 v[44:47], a[68:71], a[4:7], v[44:47]\n\t" \
    "ds_read_b128 a[44:47], v232 offset:7168\n\t" \
    "v_mfma_f32_16x16x32_bf16 v[48:51], a[68:71], a[8:11], v[48:51]\n\t" \
    "ds_read_b64_tr_b16 a[92:93], %[brd] offset:6400\n\t" \
    "v_mfma_f32_16x16x32_bf16 v[52:55], a[68:71], a[12:15], v[52:55]\n\t" \
    "ds_read_b64_tr_b16 a[94:95], %[brd] offset:7936\n\t" \
    "v_mfma_f32_16x16x32_bf16 v[56:59], a[72:75], a[0:3], v[56:59]\n\t" \
    "ds_read_b64_tr_b16 a[96:97], %[brd] offset:6656\n\t" \
    "v_mfma_f32_16x16x32_bf16 v[60:63], a[72:75], a[4:7], v[60:63]\n\t" \
    "ds_read_b64_tr_b16 a[98:99], %[brd] offset:8192\n\t" \
    "v_mfma_f32_16x16x32_bf16 v[64:67], a[72:75], a[8:11], v[64:67]\n\t" \
    "ds_read_b64_tr_b16 a[100:101], %[brd] offset:6912\n\t" \
    "v_mfma_f32_16x16x32_bf16 v[68:71], a[72:75], a[12:15], v[68:71]\n\t" \
    "ds_read_b64_tr_b16 a[102:103], %[brd] offset:8448\n\t" \
    "v_mfma_f32_16x16x32_bf16 v[72:75], a[76:79], a[0:3], v[72:75]\n\t" \
    "ds_read_b64_tr_b16 a[104:105], %[brd] offset:7168\n\t" \
    "v_mfma_f32_16x16x32_bf16 v[76:79], a[76:79], a[4:7], v[76:79]\n\t" \
    "ds_read_b64_tr_b16 a[106:107], %[brd] offset:8704\n\t" \
    "v_mfma_f32_16x16x32_bf16 v[80:83], a[76:79], a[8:11], v[80:83]\n\t" \
    "ds_read_b64_tr_b16 a[108:109], %[brd] offset:7424\n\t" \
    "v_mfma_f32_16x16x32_bf16 v[84:87], a[76:79], a[12:15], v[84:87]\n\t" \
    "ds_read_b64_tr_b16 a[110:111], %[brd] offset:8960\n\t" \
    "v_mfma_f32_16x16x32_bf16 v[88:91], a[80:83], a[0:3], v[88:91]\n\t" \
    "s_add_u32 m0, s48, 0\n\t" \
    "s_cmp_lt_u32 s42, %[bend]\n\t" \
    "s_cselect_b32 s46, s42, s49\n\t" \
    "buffer_load_dwordx4 %[bo0], %[srdb], s46 offen lds\n\t" \
    "v_mfma_f32_16x16x32_bf16 v[92:95], a[80:83], a[4:7], v[92:95]\n\t" \
    "buffer_load_dwordx4 %[bo1], %[srdb], s46 offen offset:1024 lds\n\t" \
    "v_mfma_f32_16x16x32_bf16 v[96:99], a[80:83], a[8:11], v[96:99]\n\t" \
    "buffer_load_dwordx4 %[bo2], %[srdb], s46 offen offset:2048 lds\n\t" \
    "v_mfma_f32_16x16x32_bf16 v[100:103], a[80:83], a[12:15], v[100:103]\n\t" \
    "buffer_load_dwordx4 %[bo3], %[srdb], s46 offen offset:3072 lds\n\t" \
    "v_mfma_f32_16x16x32_bf16 v[104:107], a[84:87], a[0:3], v[104:107]\n\t" \
    "s_add_u32 m0, s48, 4096\n\t" \
    "s_add_u32 s42, s42, %[bstep]\n\t" \
    "buffer_load_dwordx4 %[bo4], %[srdb], s46 offen lds\n\t" \
    "v_mfma_f32_16x16x32_bf16 v[108:111], a[84:87], a[4:7], v[108:111]\n\t" \
    "buffer_load_dwordx4 %[bo5], %[srdb], s46 offen offset:1024 lds\n\t" \
    "v_mfma_f32_16x16x32_bf16 v[112:115], a[84:87], a[8:11], v[112:115]\n\t" \
    "v_mfma_f32_16x16x32_bf16 v[116:119], a[84:87], a[12:15], v[116:119]\n\t" \
    "s_waitcnt vmcnt(14) lgkmcnt(0)\n\t" \
    "s_barrier\n\t" \
    "s_add_u32 s43, s43, 16384\n\t" \
    "s_cmp_eq_u32 s43, 49152\n\t" \
    "s_cselect_b32 s43, 0, s43\n\t" \
    "v_add_u32 v232, s43, %[ard]\n\t" \
    "v_mfma_f32_16x16x32_bf16 v[24:27], a[88:91], a[32:35], v[24:27]\n\t" \
    "ds_read_b64_tr_b16 a[64:65], %[brd] offset:12288\n\t" \
    "v_mfma_f32_16x16x32_bf16 v[28:31], a[88:91], a[36:39], v[28:31]\n\t" \
    "ds_read_b64_tr_b16 a[66:67], %[brd] offset:13824\n\t" \
    "v_mfma_f32_16x16x32_bf16 v[32:35], a[88:91], a[40:43], v[32:35]\n\t" \
    "ds_read_b128 a[0:3], v232 offset:0\n\t" \
    "v_mfma_f32_16x16x32_bf16 v[36:39], a[88:91], a[44:47], v[36:39]\n\t" \
    "ds_read_b128 a[4:7], v232 offset:1024\n\t" \
    "v_mfma_f32_16x16x32_bf16 v[40:43], a[92:95], a[32:35], v[40:43]\n\t" \
    "ds_read_b128 a[8:11], v232 offset:2048\n\t" \
    "v_mfma_f32_16x16x32_bf16 v[44:47], a[92:95], a[36:39], v[44:47]\n\t" \
    "ds_read_b128 a[12:15], v232 offset:3072\n\t" \
    "v_mfma_f32_16x16x32_bf16 v[48:51], a[92:95], a[40:43], v[48:51]\n\t" \
    "ds_read_b64_tr_b16 a[68:69], %[brd] offset:12544\n\t" \
    "v_mfma_f32_16x16x32_bf16 v[52:55], a[92:95], a[44:47], v[52:55]\n\t" \
    "ds_read_b64_tr_b16 a[70:71], %[brd] offset:14080\n\t" \
    "v_mfma_f32_16x16x32_bf16 v[56:59], a[96:99], a[32:35], v[56:59]\n\t" \
    "ds_read_b64_tr_b16 a[72:73], %[brd] offset:12800\n\t" \
    "v_mfma_f32_16x16x32_bf16 v[60:63], a[96:99], a[36:39], v[60:63]\n\t" \
    "ds_read_b64_tr_b16 a[74:75], %[brd] offset:14336\n\t" \
    "v_mfma_f32_16x16x32_bf16 v[64:67], a[96:99], a[40:43], v[64:67]\n\t" \
    "ds_read_b64_tr_b16 a[76:77], %[brd] offset:13056\n\t" \
    "v_mfma_f32_16x16x32_bf16 v[68:71], a[96:99], a[44:47], v[68:71]\n\t" \
    "ds_read_b64_tr_b16 a[78:79], %[brd] offset:14592\n\t" \
    "v_mfma_f32_16x16x32_bf16 v[72:75], a[100:103], a[32:35], v[72:75]\n\t" \
    "ds_read_b64_tr_b16 a[80:81], %[brd] offset:13312\n\t" \
    "v_mfma_f32_16x16x32_bf16 v[76:79], a[100:103], a[36:39], v[76:79]\n\t" \
    "ds_read_b64_tr_b16 a[82:83], %[brd] offset:14848\n\t" \
    "v_mfma_f32_16x16x32_bf16 v[80:83], a[100:103], a[40:43], v[80:83]\n\t" \
    "ds_read_b64_tr_b16 a[84:85], %[brd] offset:13568\n\t" \
    "v_mfma_f32_16x16x32_bf16 v[84:87], a[100:103], a[44:47], v[84:87]\n\t" \
    "ds_read_b64_tr_b16 a[86:87], %[brd] offset:15104\n\t" \
    "v_mfma_f32_16x16x32_bf16 v[88:91], a[104:107], a[32:35], v[88:91]\n\t" \
    "s_add_u32 m0, s44, %[wq]\n\t" \
    "s_cmp_lt_u32 s41, %[aend]\n\t" \
    "s_cselect_b32 s45, s41, s49\n\t" \
    "buffer_load_dwordx4 %[asrc], %[srda], s45 offen lds\n\t" \
    "v_mfma_f32_16x16x32_bf16 v[92:95], a[104:107], a[36:39], v[92:95]\n\t" \
    "buffer_load_dwordx4 %[asrc], %[srda], s45 offen offset:1024 lds\n\t" \
    "s_add_u32 s41, s41, 16384\n\t" \
    "s_add_u32 s44, s44, 16384\n\t" \
    "s_cmp_eq_u32 s44, 49152\n\t" \
    "s_cselect_b32 s44, 0, s44\n\t" \
    "v_mfma_f32_16x16x32_bf16 v[96:99], a[104:107], a[40:43], v[96:99]\n\t" \
    "s_add_u32 m0, s48, 6144\n\t" \
    "s_cmp_lt_u32 s42, %[bend]\n\t" \
    "s_cselect_b32 s46, s42, s49\n\t" \
    "buffer_load_dwordx4 %[bo0], %[srdb], s46 offen lds\n\t" \
    "v_mfma_f32_16x16x32_bf16 v[100:103], a[104:107], a[44:47], v[100:103]\n\t" \
    "buffer_load_dwordx4 %[bo1], %[srdb], s46 offen offset:1024 lds\n\t" \
    "v_mfma_f32_16x16x32_bf16 v[104:107], a[108:111], a[32:35], v[104:107]\n\t" \
    "buffer_load_dwordx4 %[bo2], %[srdb], s46 offen offset:2048 lds\n\t" \
    "v_mfma_f32_16x16x32_bf16 v[108:111], a[108:111], a[36:39], v[108:111]\n\t" \
    "buffer_load_dwordx4 %[bo3], %[srdb], s46 offen offset:3072 lds\n\t" \
    "v_mfma_f32_16x16x32_bf16 v[112:115], a[108:111], a[40:43], v[112:115]\n\t" \
    "s_add_u32 m0, s48, 10240\n\t" \
    "s_add_u32 s42, s42, %[bstep]\n\t" \
    "buffer_load_dwordx4 %[bo4], %[srdb], s46 offen lds\n\t" \
    "v_mfma_f32_16x16x32_bf16 v[116:119], a[108:111], a[44:47], v[116:119]\n\t" \
    "buffer_load_dwordx4 %[bo5], %[srdb], s46 offen offset:1024 lds\n\t" \
    "s_waitcnt vmcnt(14) lgkmcnt(0)\n\t" \
    "v_mfma_f32_16x16x32_bf16 v[24:27], a[64:67], a[0:3], v[24:27]\n\t" \
    "ds_read_b64_tr_b16 a[88:89], %[brd] offset:18432\n\t" \
    "v_mfma_f32_16x16x32_bf16 v[28:31], a[64:67], a[4:7], v[28:31]\n\t" \
    "ds_read_b64_tr_b16 a[90:91], %[brd] offset:19968\n\t" \
    "v_mfma_f32_16x16x32_bf16 v[32:35], a[64:67], a[8:11], v[32:35]\n\t" \
    "ds_read_b128 a[32:35], v232 offset:4096\n\t" \
    "v_mfma_f32_16x16x32_bf16 v[36:39], a[64:67], a[12:15], v[36:39]\n\t" \
    "ds_read_b128 a[36:39], v232 offset:5120\n\t" \
    "v_mfma_f32_16x16x32_bf16 v[40:43], a[68:71], a[0:3], v[40:43]\n\t" \
    "ds_read_b128 a[40:43], v232 offset:6144\n\t" \
    "v_mfma_f32_16x16x32_bf16 v[44:47], a[68:71], a[4:7], v[44:47]\n\t" \
    "ds_read_b128 a[44:47], v232 offset:7168\n\t" \
    "v_mfma_f32_16x16x32_bf16 v[48:51], a[68:71], a[8:11], v[48:51]\n\t" \
    "ds_read_b64_tr_b16 a[92:93], %[brd] offset:18688\n\t" \
    "v_mfma_f32_16x16x32_bf16 v[52:55], a[68:71], a[12:15], v[52:55]\n\t" \
    "ds_read_b64_tr_b16 a[94:95], %[brd] offset:20224\n\t" \
    "v_mfma_f32_16x16x32_bf16 v[56:59], a[72:75], a[0:3], v[56:59]\n\t" \
    "ds_read_b64_tr_b16 a[96:97], %[brd] offset:18944\n\t" \
    "v_mfma_f32_16x16x32_bf16 v[60:63], a[72:75], a[4:7], v[60:63]\n\t" \
    "ds_read_b64_tr_b16 a[98:99], %[brd] offset:20480\n\t" \
    "v_mfma_f32_16x16x32_bf16 v[64:67], a[72:75], a[8:11], v[64:67]\n\t" \
    "ds_read_b64_tr_b16 a[100:101], %[brd] offset:19200\n\t" \
    "v_mfma_f32_16x16x32_bf16 v[68:71], a[72:75], a[12:15], v[68:71]\n\t" \
    "ds_read_b64_tr_b16 a[102:103], %[brd] offset:20736\n\t" \
    "v_mfma_f32_16x16x32_bf16 v[72:75], a[76:79], a[0:3], v[72:75]\n\t" \
    "ds_read_b64_tr_b16 a[104:105], %[brd] offset:19456\n\t" \
    "v_mfma_f32_16x16x32_bf16 v[76:79], a[76:79], a[4:7], v[76:79]\n\t" \
    "ds_read_b64_tr_b16 a[106:107], %[brd] offset:20992\n\t" \
    "v_mfma_f32_16x16x32_bf16 v[80:83], a[76:79], a[8:11], v[80:83]\n\t" \
    "ds_read_b64_tr_b16 a[108:109], %[brd] offset:19712\n\t" \
    "v_mfma_f32_16x16x32_bf16 v[84:87], a[76:79], a[12:15], v[84:87]\n\t" \
    "ds_read_b64_tr_b16 a[110:111], %[brd] offset:21248\n\t" \
    "v_mfma_f32_16x16x32_bf16 v[88:91], a[80:83], a[0:3], v[88:91]\n\t" \
    "s_add_u32 m0, s48, 12288\n\t" \
    "s_cmp_lt_u32 s42, %[bend]\n\t" \
    "s_cselect_b32 s46, s42, s49\n\t" \
    "buffer_load_dwordx4 %[bo0], %[srdb], s46 offen lds\n\t" \
    "v_mfma_f32_16x16x32_bf16 v[92:95], a[80:83], a[4:7], v[92:95]\n\t" \
    "buffer_load_dwordx4 %[bo1], %[srdb], s46 offen offset:1024 lds\n\t" \
    "v_mfma_f32_16x16x32_bf16 v[96:99], a[80:83], a[8:11], v[96:99]\n\t" \
    "buffer_load_dwordx4 %[bo2], %[srdb], s46 offen offset:2048 lds\n\t" \
    "v_mfma_f32_16x16x32_bf16 v[100:103], a[80:83], a[12:15], v[100:103]\n\t" \
    "buffer_load_dwordx4 %[bo3], %[srdb], s46 offen offset:3072 lds\n\t" \
    "v_mfma_f32_16x16x32_bf16 v[104:107], a[84:87], a[0:3], v[104:107]\n\t" \
    "s_add_u32 m0, s48, 16384\n\t" \
    "s_add_u32 s42, s42, %[bstep]\n\t" \
    "buffer_load_dwordx4 %[bo4], %[srdb], s46 offen lds\n\t" \
    "v_mfma_f32_16x16x32_bf16 v[108:111], a[84:87], a[4:7], v[108:111]\n\t" \
    "buffer_load_dwordx4 %[bo5], %[srdb], s46 offen offset:1024 lds\n\t" \
    "v_mfma_f32_16x16x32_bf16 v[112:115], a[84:87], a[8:11], v[112:115]\n\t" \
    "v_mfma_f32_16x16x32_bf16 v[116:119], a[84:87], a[12:15], v[116:119]\n\t" \
    "s_waitcnt vmcnt(14) lgkmcnt(0)\n\t" \
    "s_barrier\n\t" \
    "s_add_u32 s43, s43, 16384\n\t" \
    "s_cmp_eq_u32 s43, 49152\n\t" \
    "s_cselect_b32 s43, 0, s43\n\t" \
    "v_add_u32 v232, s43, %[ard]\n\t" \
    "v_mfma_f32_16x16x32_bf16 v[24:27], a[88:91], a[32:35], v[24:27]\n\t" \
    "ds_read_b64_tr_b16 a[64:65], %[brd] offset:0\n\t" \
    "v_mfma_f32_16x16x32_bf16 v[28:31], a[88:91], a[36:39], v[28:31]\n\t" \
    "ds_read_b64_tr_b16 a[66:67], %[brd] offset:1536\n\t" \
    "v_mfma_f32_16x16x32_bf16 v[32:35], a[88:91], a[40:43], v[32:35]\n\t" \
    "ds_read_b128 a[0:3], v232 offset:0\n\t" \
    "v_mfma_f32_16x16x32_bf16 v[36:39], a[88:91], a[44:47], v[36:39]\n\t" \
    "ds_read_b128 a[4:7], v232 offset:1024\n\t" \
    "v_mfma_f32_16x16x32_bf16 v[40:43], a[92:95], a[32:35], v[40:43]\n\t" \
    "ds_read_b128 a[8:11], v232 offset:2048\n\t" \
    "v_mfma_f32_16x16x32_bf16 v[44:47], a[92:95], a[36:39], v[44:47]\n\t" \
    "ds_read_b128 a[12:15], v232 offset:3072\n\t" \
    "v_mfma_f32_16x16x32_bf16 v[48:51], a[92:95], a[40:43], v[48:51]\n\t" \
    "ds_read_b64_tr_b16 a[68:69], %[brd] offset:256\n\t" \
    "v_mfma_f32_16x16x32_bf16 v[52:55], a[92:95], a[44:47], v[52:55]\n\t" \
    "ds_read_b64_tr_b16 a[70:71], %[brd] offset:1792\n\t" \
    "v_mfma_f32_16x16x32_bf16 v[56:59], a[96:99], a[32:35], v[56:59]\n\t" \
    "ds_read_b64_tr_b16 a[72:73], %[brd] offset:512\n\t" \
    "v_mfma_f32_16x16x32_bf16 v[60:63], a[96:99], a[36:39], v[60:63]\n\t" \
    "ds_read_b64_tr_b16 a[74:75], %[brd] offset:2048\n\t" \
    "v_mfma_f32_16x16x32_bf16 v[64:67], a[96:99], a[40:43], v[64:67]\n\t" \
    "ds_read_b64_tr_b16 a[76:77], %[brd] offset:768\n\t" \
    "v_mfma_f32_16x16x32_bf16 v[68:71], a[96:99], a[44:47], v[68:71]\n\t" \
    "ds_read_b64_tr_b16 a[78:79], %[brd] offset:2304\n\t" \
    "v_mfma_f32_16x16x32_bf16 v[72:75], a[100:103], a[32:35], v[72:75]\n\t" \
    "ds_read_b64_tr_b16 a[80:81], %[brd] offset:1024\n\t" \
    "v_mfma_f32_16x16x32_bf16 v[76:79], a[100:103], a[36:39], v[76:79]\n\t" \
    "ds_read_b64_tr_b16 a[82:83], %[brd] offset:2560\n\t" \
    "v_mfma_f32_16x16x32_bf16 v[80:83], a[100:103], a[40:43], v[80:83]\n\t" \
    "ds_read_b64_tr_b16 a[84:85], %[brd] offset:1280\n\t" \
    "v_mfma_f32_16x16x32_bf16 v[84:87], a[100:103], a[44:47], v[84:87]\n\t" \
    "ds_read_b64_tr_b16 a[86:87], %[brd] offset:2816\n\t" \
    "v_mfma_f32_16x16x32_bf16 v[88:91], a[104:107], a[32:35], v[88:91]\n\t" \
    "s_add_u32 m0, s44, %[wq]\n\t" \
    "s_cmp_lt_u32 s41, %[aend]\n\t" \
    "s_cselect_b32 s45, s41, s49\n\t" \
    "buffer_load_dwordx4 %[asrc], %[srda], s45 offen lds\n\t" \
    "v_mfma_f32_16x16x32_bf16 v[92:95], a[104:107], a[36:39], v[92:95]\n\t" \
    "buffer_load_dwordx4 %[asrc], %[srda], s45 offen offset:1024 lds\n\t" \
    "s_add_u32 s41, s41, 16384\n\t" \
    "s_add_u32 s44, s44, 16384\n\t" \
    "s_cmp_eq_u32 s44, 49152\n\t" \
    "s_cselect_b32 s44, 0, s44\n\t" \
    "v_mfma_f32_16x16x32_bf16 v[96:99], a[104:107], a[40:43], v[96:99]\n\t" \
    "s_add_u32 m0, s48, 18432\n\t" \
    "s_cmp_lt_u32 s42, %[bend]\n\t" \
    "s_cselect_b32 s46, s42, s49\n\t" \
    "buffer_load_dwordx4 %[bo0], %[srdb], s46 offen lds\n\t" \
    "v_mfma_f32_16x16x32_bf16 v[100:103], a[104:107], a[44:47], v[100:103]\n\t" \
    "buffer_load_dwordx4 %[bo1], %[srdb], s46 offen offset:1024 lds\n\t" \
    "v_mfma_f32_16x16x32_bf16 v[104:107], a[108:111], a[32:35], v[104:107]\n\t" \
    "buffer_load_dwordx4 %[bo2], %[srdb], s46 offen offset:2048 lds\n\t" \
    "v_mfma_f32_16x16x32_bf16 v[108:111], a[108:111], a[36:39], v[108:111]\n\t" \
    "buffer_load_dwordx4 %[bo3], %[srdb], s46 offen offset:3072 lds\n\t" \
    "v_mfma_f32_16x16x32_bf16 v[112:115], a[108:111], a[40:43], v[112:115]\n\t" \
    "s_add_u32 m0, s48, 22528\n\t" \
    "s_add_u32 s42, s42, %[bstep]\n\t" \
    "buffer_load_dwordx4 %[bo4], %[srdb], s46 offen lds\n\t" \
    "v_mfma_f32_16x16x32_bf16 v[116:119], a[108:111], a[44:47], v[116:119]\n\t" \
    "buffer_load_dwordx4 %[bo5], %[srdb], s46 offen offset:1024 lds\n\t" \
    "s_add_u32 s40, s40, 4\n\t" \
    "s_cmp_lt_u32 s40, %[nk]\n\t" \
    "s_cbranch_scc1 tl_1_%=\n\t" \
    "s_waitcnt vmcnt(0) lgkmcnt(0)\n\t" \
    "s_barrier\n\t" \
    "s_cmp_lg_u32 %[tolds], 0\n\t" \
    "s_cbranch_scc1 el_2_%=\n\t" \
    "v_cvt_pk_bf16_f32 v236, v24, v25\n\t" \
    "v_cvt_pk_bf16_f32 v237, v26, v27\n\t" \
    "v_cvt_pk_bf16_f32 v238, v40, v41\n\t" \
    "v_cvt_pk_bf16_f32 v239, v42, v43\n\t" \
    "s_mul_i32 s45, %[os1], 0\n\t" \
    "s_add_u32 s45, s45, %[ooff]\n\t" \
    "v_permlane32_swap_b32 v236, v238\n\t" \
    "v_permlane32_swap_b32 v237, v239\n\t" \
    "s_nop 1\n\t" \
    "v_permlane16_swap_b32 v236, v238\n\t" \
    "v_permlane16_swap_b32 v237, v239\n\t" \
    "buffer_store_dwordx4 v[236:239], %[ov], %[srda], s45 offen sc1\n\t" \
    "v_cvt_pk_bf16_f32 v240, v56, v57\n\t" \
    "v_cvt_pk_bf16_f32 v241, v58, v59\n\t" \
    "v_cvt_pk_bf16_f32 v242, v72, v73\n\t" \
    "v_cvt_pk_bf16_f32 v243, v74, v75\n\t" \
    "s_add_u32 s45, s45, %[os3]\n\t" \
    "s_nop 0\n\t" \
    "v_permlane32_swap_b32 v240, v242\n\t" \
    "v_permlane32_swap_b32 v241, v243\n\t" \
    "s_nop 1\n\t" \
    "v_permlane16_swap_b32 v240, v242\n\t" \
    "v_permlane16_swap_b32 v241, v243\n\t" \
    "buffer_store_dwordx4 v[240:243], %[ov], %[srda], s45 offen sc1\n\t" \
    "v_cvt_pk_bf16_f32 v244, v88, v89\n\t" \
    "v_cvt_pk_bf16_f32 v245, v90, v91\n\t" \
    "v_cvt_pk_bf16_f32 v246, v104, v105\n\t" \
    "v_cvt_pk_bf16_f32 v247, v106, v107\n\t" \
    "s_add_u32 s45, s45, %[os3]\n\t" \
    "s_nop 0\n\t" \
    "v_permlane32_swap_b32 v244, v246\n\t" \
    "v_permlane32_swap_b32 v245, v247\n\t" \
    "s_nop 1\n\t" \
    "v_permlane16_swap_b32 v244, v246\n\t" \
    "v_permlane16_swap_b32 v245, v247\n\t" \
    "buffer_store_dwordx4 v[244:247], %[ov], %[srda], s45 offen sc1\n\t" \
    "v_cvt_pk_bf16_f32 v248, v28, v29\n\t" \
    "v_cvt_pk_bf16_f32 v249, v30, v31\n\t" \
    "v_cvt_pk_bf16_f32 v250, v44, v45\n\t" \
    "v_cvt_pk_bf16_f32 v251, v46, v47\n\t" \
    "s_mul_i32 s45, %[os1], 0\n\t" \
    "s_add_u32 s45, s45, %[ooff]\n\t" \
    "s_add_u32 s45, s45, %[os2]\n\t" \
    "v_permlane32_swap_b32 v248, v250\n\t" \
    "v_permlane32_swap_b32 v249, v251\n\t" \
    "s_nop 1\n\t" \
    "v_permlane16_swap_b32 v248, v250\n\t" \
    "v_permlane16_swap_b32 v249, v251\n\t" \
    "buffer_store_dwordx4 v[248:251], %[ov], %[srda], s45 offen sc1\n\t" \
    "v_cvt_pk_bf16_f32 v236, v60, v61\n\t" \
    "v_cvt_pk_bf16_f32 v237, v62, v63\n\t" \
    "v_cvt_pk_bf16_f32 v238, v76, v77\n\t" \
    "v_cvt_pk_bf16_f32 v239, v78, v79\n\t" \
    "s_add_u32 s45, s45, %[os3]\n\t" \
    "s_nop 0\n\t" \
    "v_permlane32_swap_b32 v236, v238\n\t" \
    "v_permlane32_swap_b32 v237, v239\n\t" \
    "s_nop 1\n\t" \
    "v_permlane16_swap_b32 v236, v238\n\t" \
    "v_permlane16_swap_b32 v237, v239\n\t" \
    "buffer_store_dwordx4 v[236:239], %[ov], %[srda], s45 offen sc1\n\t" \
    "v_cvt_pk_bf16_f32 v240, v92, v93\n\t" \
    "v_cvt_pk_bf16_f32 v241, v94, v95\n\t" \
    "v_cvt_pk_bf16_f32 v242, v108, v109\n\t" \
    "v_cvt_pk_bf16_f32 v243, v110, v111\n\t" \
    "s_add_u32 s45, s45, %[os3]\n\t" \
    "s_nop 0\n\t" \
    "v_permlane32_swap_b32 v240, v242\n\t" \
    "v_permlane32_swap_b32 v241, v243\n\t" \
    "s_nop 1\n\t" \
    "v_permlane16_swap_b32 v240, v242\n\t" \
    "v_permlane16_swap_b32 v241, v243\n\t" \
    "buffer_store_dwordx4 v[240:243], %[ov], %[srda], s45 offen sc1\n\t" \
    "v_cvt_pk_bf16_f32 v244, v32, v33\n\t" \
    "v_cvt_pk_bf16_f32 v245, v34, v35\n\t" \
    "v_cvt_pk_bf16_f32 v246, v48, v49\n\t" \
    "v_cvt_pk_bf16_f32 v247, v50, v51\n\t" \
    "s_mul_i32 s45, %[os1], 1\n\t" \
    "s_add_u32 s45, s45, %[ooff]\n\t" \
    "v_permlane32_swap_b32 v244, v246\n\t" \
    "v_permlane32_swap_b32 v245, v247\n\t" \
    "s_nop 1\n\t" \
    "v_permlane16_swap_b32 v244, v246\n\t" \
    "v_permlane16_swap_b32 v245, v247\n\t" \
    "buffer_store_dwordx4 v[244:247], %[ov], %[srda], s45 offen sc1\n\t" \
    "v_cvt_pk_bf16_f32 v248, v64, v65\n\t" \
    "v_cvt_pk_bf16_f32 v249, v66, v67\n\t" \
    "v_cvt_pk_bf16_f32 v250, v80, v81\n\t" \
    "v_cvt_pk_bf16_f32 v251, v82, v83\n\t" \
    "s_add_u32 s45, s45, %[os3]\n\t" \
    "s_nop 0\n\t" \
    "v_permlane32_swap_b32 v248, v250\n\t" \
    "v_permlane32_swap_b32 v249, v251\n\t" \
    "s_nop 1\n\t" \
    "v_permlane16_swap_b32 v248, v250\n\t" \
    "v_permlane16_swap_b32 v249, v251\n\t" \
    "buffer_store_dwordx4 v[248:251], %[ov], %[srda], s45 offen sc1\n\t" \
    "v_cvt_pk_bf16_f32 v236, v96, v97\n\t" \
    "v_cvt_pk_bf16_f32 v237, v98, v99\n\t" \
    "v_cvt_pk_bf16_f32 v238, v112, v113\n\t" \
    "v_cvt_pk_bf16_f32 v239, v114, v115\n\t" \
    "s_add_u32 s45, s45, %[os3]\n\t" \
    "s_nop 0\n\t" \
    "v_permlane32_swap_b32 v236, v238\n\t" \
    "v_permlane32_swap_b32 v237, v239\n\t" \
    "s_nop 1\n\t" \
    "v_permlane16_swap_b32 v236, v238\n\t" \
    "v_permlane16_swap_b32 v237, v239\n\t" \
    "buffer_store_dwordx4 v[236:239], %[ov], %[srda], s45 offen sc1\n\t" \
    "v_cvt_pk_bf16_f32 v240, v36, v37\n\t" \
    "v_cvt_pk_bf16_f32 v241, v38, v39\n\t" \
    "v_cvt_pk_bf16_f32 v242, v52, v53\n\t" \
    "v_cvt_pk_bf16_f32 v243, v54, v55\n\t" \
    "s_mul_i32 s45, %[os1], 1\n\t" \
    "s_add_u32 s45, s45, %[ooff]\n\t" \
    "s_add_u32 s45, s45, %[os2]\n\t" \
    "v_permlane32_swap_b32 v240, v242\n\t" \
    "v_permlane32_swap_b32 v241, v243\n\t" \
    "s_nop 1\n\t" \
    "v_permlane16_swap_b32 v240, v242\n\t" \
    "v_permlane16_swap_b32 v241, v243\n\t" \
    "buffer_store_dwordx4 v[240:243], %[ov], %[srda], s45 offen sc1\n\t" \
    "v_cvt_pk_bf16_f32 v244, v68, v69\n\t" \
    "v_cvt_pk_bf16_f32 v245, v70, v71\n\t" \
    "v_cvt_pk_bf16_f32 v246, v84, v85\n\t" \
    "v_cvt_pk_bf16_f32 v247, v86, v87\n\t" \
    "s_add_u32 s45, s45, %[os3]\n\t" \
    "s_nop 0\n\t" \
    "v_permlane32_swap_b32 v244, v246\n\t" \
    "v_permlane32_swap_b32 v245, v247\n\t" \
    "s_nop 1\n\t" \
    "v_permlane16_swap_b32 v244, v246\n\t" \
    "v_permlane16_swap_b32 v245, v247\n\t" \
    "buffer_store_dwordx4 v[244:247], %[ov], %[srda], s45 offen sc1\n\t" \
    "v_cvt_pk_bf16_f32 v248, v100, v101\n\t" \
    "v_cvt_pk_bf16_f32 v249, v102, v103\n\t" \
    "v_cvt_pk_bf16_f32 v250, v116, v117\n\t" \
    "v_cvt_pk_bf16_f32 v251, v118, v119\n\t" \
    "s_add_u32 s45, s45, %[os3]\n\t" \
    "s_nop 0\n\t" \
    "v_permlane32_swap_b32 v248, v250\n\t" \
    "v_permlane32_swap_b32 v249, v251\n\t" \
    "s_nop 1\n\t" \
    "v_permlane16_swap_b32 v248, v250\n\t" \
    "v_permlane16_swap_b32 v249, v251\n\t" \
    "buffer_store_dwordx4 v[248:251], %[ov], %[srda], s45 offen sc1\n\t" \
    "s_branch ed_3_%=\n\t" \
    "el_2_%=:\n\t" \
    "v_cvt_pk_bf16_f32 v236, v24, v25\n\t" \
    "v_cvt_pk_bf16_f32 v237, v26, v27\n\t" \
    "v_cvt_pk_bf16_f32 v238, v40, v41\n\t" \
    "v_cvt_pk_bf16_f32 v239, v42, v43\n\t" \
    "s_nop 1\n\t" \
    "v_permlane32_swap_b32 v236, v238\n\t" \
    "v_permlane32_swap_b32 v237, v239\n\t" \
    "s_nop 1\n\t" \
    "v_permlane16_swap_b32 v236, v238\n\t" \
    "v_permlane16_swap_b32 v237, v239\n\t" \
    "ds_write_b128 %[ov], v[236:239] offset:0\n\t" \
    "v_cvt_pk_bf16_f32 v240, v56, v57\n\t" \
    "v_cvt_pk_bf16_f32 v241, v58, v59\n\t" \
    "v_cvt_pk_bf16_f32 v242, v72, v73\n\t" \
    "v_cvt_pk_bf16_f32 v243, v74, v75\n\t" \
    "s_nop 1\n\t" \
    "v_permlane32_swap_b32 v240, v242\n\t" \
    "v_permlane32_swap_b32 v241, v243\n\t" \
    "s_nop 1\n\t" \
    "v_permlane16_swap_b32 v240, v242\n\t" \
    "v_permlane16_swap_b32 v241, v243\n\t" \
    "ds_write_b128 %[ov], v[240:243] offset:8192\n\t" \
    "v_cvt_pk_bf16_f32 v244, v88, v89\n\t" \
    "v_cvt_pk_bf16_f32 v245, v90, v91\n\t" \
    "v_cvt_pk_bf16_f32 v246, v104, v105\n\t" \
    "v_cvt_pk_bf16_f32 v247, v106, v107\n\t" \
    "s_nop 1\n\t" \
    "v_permlane32_swap_b32 v244, v246\n\t" \
    "v_permlane32_swap_b32 v245, v247\n\t" \
    "s_nop 1\n\t" \
    "v_permlane16_swap_b32 v244, v246\n\t" \
    "v_permlane16_swap_b32 v245, v247\n\t" \
    "ds_write_b128 %[ov], v[244:247] offset:16384\n\t" \
    "v_cvt_pk_bf16_f32 v248, v28, v29\n\t" \
    "v_cvt_pk_bf16_f32 v249, v30, v31\n\t" \
    "v_cvt_pk_bf16_f32 v250, v44, v45\n\t" \
    "v_cvt_pk_bf16_f32 v251, v46, v47\n\t" \
    "s_nop 1\n\t" \
    "v_permlane32_swap_b32 v248, v250\n\t" \
    "v_permlane32_swap_b32 v249, v251\n\t" \
    "s_nop 1\n\t" \
    "v_permlane16_swap_b32 v248, v250\n\t" \
    "v_permlane16_swap_b32 v249, v251\n\t" \
    "ds_write_b128 %[ov], v[248:251] offset:128\n\t" \
    "v_cvt_pk_bf16_f32 v236, v60, v61\n\t" \
    "v_cvt_pk_bf16_f32 v237, v62, v63\n\t" \
    "v_cvt_pk_bf16_f32 v238, v76, v77\n\t" \
    "v_cvt_pk_bf16_f32 v239, v78, v79\n\t" \
    "s_nop 1\n\t" \
    "v_permlane32_swap_b32 v236, v238\n\t" \
    "v_permlane32_swap_b32 v237, v239\n\t" \
    "s_nop 1\n\t" \
    "v_permlane16_swap_b32 v236, v238\n\t" \
    "v_permlane16_swap_b32 v237, v239\n\t" \
    "ds_write_b128 %[ov], v[236:239] offset:8320\n\t" \
    "v_cvt_pk_bf16_f32 v240, v92, v93\n\t" \
    "v_cvt_pk_bf16_f32 v241, v94, v95\n\t" \
    "v_cvt_pk_bf16_f32 v242, v108, v109\n\t" \
    "v_cvt_pk_bf16_f32 v243, v110, v111\n\t" \
    "s_nop 1\n\t" \
    "v_permlane32_swap_b32 v240, v242\n\t" \
    "v_permlane32_swap_b32 v241, v243\n\t" \
    "s_nop 1\n\t" \
    "v_permlane16_swap_b32 v240, v242\n\t" \
    "v_permlane16_swap_b32 v241, v243\n\t" \
    "ds_write_b128 %[ov], v[240:243] offset:16512\n\t" \
    "v_cvt_pk_bf16_f32 v244, v32, v33\n\t" \
    "v_cvt_pk_bf16_f32 v245, v34, v35\n\t" \
    "v_cvt_pk_bf16_f32 v246, v48, v49\n\t" \
    "v_cvt_pk_bf16_f32 v247, v50, v51\n\t" \
    "s_nop 1\n\t" \
    "v_permlane32_swap_b32 v244, v246\n\t" \
    "v_permlane32_swap_b32 v245, v247\n\t" \
    "s_nop 1\n\t" \
    "v_permlane16_swap_b32 v244, v246\n\t" \
    "v_permlane16_swap_b32 v245, v247\n\t" \
    "ds_write_b128 %[ov], v[244:247] offset:2048\n\t" \
    "v_cvt_pk_bf16_f32 v248, v64, v65\n\t" \
    "v_cvt_pk_bf16_f32 v249, v66, v67\n\t" \
    "v_cvt_pk_bf16_f32 v250, v80, v81\n\t" \
    "v_cvt_pk_bf16_f32 v251, v82, v83\n\t" \
    "s_nop 1\n\t" \
    "v_permlane32_swap_b32 v248, v250\n\t" \
    "v_permlane32_swap_b32 v249, v251\n\t" \
    "s_nop 1\n\t" \
    "v_permlane16_swap_b32 v248, v250\n\t" \
    "v_permlane16_swap_b32 v249, v251\n\t" \
    "ds_write_b128 %[ov], v[248:251] offset:10240\n\t" \
    "v_cvt_pk_bf16_f32 v236, v96, v97\n\t" \
    "v_cvt_pk_bf16_f32 v237, v98, v99\n\t" \
    "v_cvt_pk_bf16_f32 v238, v112, v113\n\t" \
    "v_cvt_pk_bf16_f32 v239, v114, v115\n\t" \
    "s_nop 1\n\t" \
    "v_permlane32_swap_b32 v236, v238\n\t" \
    "v_permlane32_swap_b32 v237, v239\n\t" \
    "s_nop 1\n\t" \
    "v_permlane16_swap_b32 v236, v238\n\t" \
    "v_permlane16_swap_b32 v237, v239\n\t" \
    "ds_write_b128 %[ov], v[236:239] offset:18432\n\t" \
    "v_cvt_pk_bf16_f32 v240, v36, v37\n\t" \
    "v_cvt_pk_bf16_f32 v241, v38, v39\n\t" \
    "v_cvt_pk_bf16_f32 v242, v52, v53\n\t" \
    "v_cvt_pk_bf16_f32 v243, v54, v55\n\t" \
    "s_nop 1\n\t" \
    "v_permlane32_swap_b32 v240, v242\n\t" \
    "v_permlane32_swap_b32 v241, v243\n\t" \
    "s_nop 1\n\t" \
    "v_permlane16_swap_b32 v240, v242\n\t" \
    "v_permlane16_swap_b32 v241, v243\n\t" \
    "ds_write_b128 %[ov], v[240:243] offset:2176\n\t" \
    "v_cvt_pk_bf16_f32 v244, v68, v69\n\t" \
    "v_cvt_pk_bf16_f32 v245, v70, v71\n\t" \
    "v_cvt_pk_bf16_f32 v246, v84, v85\n\t" \
    "v_cvt_pk_bf16_f32 v247, v86, v87\n\t" \
    "s_nop 1\n\t" \
    "v_permlane32_swap_b32 v244, v246\n\t" \
    "v_permlane32_swap_b32 v245, v247\n\t" \
    "s_nop 1\n\t" \
    "v_permlane16_swap_b32 v244, v246\n\t" \
    "v_permlane16_swap_b32 v245, v247\n\t" \
    "ds_write_b128 %[ov], v[244:247] offset:10368\n\t" \
    "v_cvt_pk_bf16_f32 v248, v100, v101\n\t" \
    "v_cvt_pk_bf16_f32 v249, v102, v103\n\t" \
    "v_cvt_pk_bf16_f32 v250, v116, v117\n\t" \
    "v_cvt_pk_bf16_f32 v251, v118, v119\n\t" \
    "s_nop 1\n\t" \
    "v_permlane32_swap_b32 v248, v250\n\t" \
    "v_permlane32_swap_b32 v249, v251\n\t" \
    "s_nop 1\n\t" \
    "v_permlane16_swap_b32 v248, v250\n\t" \
    "v_permlane16_swap_b32 v249, v251\n\t" \
    "ds_write_b128 %[ov], v[248:251] offset:18560\n\t" \
    "ed_3_%=:\n\t" \
    "s_waitcnt lgkmcnt(0)\n\t"
#define RK_ASM_GUH \
    "s_nop 4\n\t" \
    "s_mov_b32 s48, %[bm]\n\t" \
    "s_add_u32 m0, s48, 0\n\t" \
    "s_add_u32 s46, %[boff], 0\n\t" \
    "buffer_load_dwordx4 %[bo0], %[srdb], s46 offen lds\n\t" \
    "buffer_load_dwordx4 %[bo1], %[srdb], s46 offen offset:1024 lds\n\t" \
    "buffer_load_dwordx4 %[bo2], %[srdb], s46 offen offset:2048 lds\n\t" \
    "buffer_load_dwordx4 %[bo3], %[srdb], s46 offen offset:3072 lds\n\t" \
    "s_add_u32 m0, s48, 4096\n\t" \
    "s_nop 0\n\t" \
    "buffer_load_dwordx4 %[bo4], %[srdb], s46 offen lds\n\t" \
    "s_add_u32 m0, s48, 5120\n\t" \
    "s_add_u32 s46, %[boff], 40960\n\t" \
    "buffer_load_dwordx4 %[bo0], %[srdb], s46 offen lds\n\t" \
    "buffer_load_dwordx4 %[bo1], %[srdb], s46 offen offset:1024 lds\n\t" \
    "buffer_load_dwordx4 %[bo2], %[srdb], s46 offen offset:2048 lds\n\t" \
    "buffer_load_dwordx4 %[bo3], %[srdb], s46 offen offset:3072 lds\n\t" \
    "s_add_u32 m0, s48, 9216\n\t" \
    "s_nop 0\n\t" \
    "buffer_load_dwordx4 %[bo4], %[srdb], s46 offen lds\n\t" \
    "s_add_u32 m0, s48, 10240\n\t" \
    "s_add_u32 s46, %[boff], 81920\n\t" \
    "buffer_load_dwordx4 %[bo0], %[srdb], s46 offen lds\n\t" \
    "buffer_load_dwordx4 %[bo1], %[srdb], s46 offen offset:1024 lds\n\t" \
    "buffer_load_dwordx4 %[bo2], %[srdb], s46 offen offset:2048 lds\n\t" \
    "buffer_load_dwordx4 %[bo3], %[srdb], s46 offen offset:3072 lds\n\t" \
    "s_add_u32 m0, s48, 14336\n\t" \
    "s_nop 0\n\t" \
    "buffer_load_dwordx4 %[bo4], %[srdb], s46 offen lds\n\t" \
    "s_add_u32 m0, s48, 15360\n\t" \
    "s_add_u32 s46, %[boff], 122880\n\t" \
    "buffer_load_dwordx4 %[bo0], %[srdb], s46 offen lds\n\t" \
    "buffer_load_dwordx4 %[bo1], %[srdb], s46 offen offset:1024 lds\n\t" \
    "buffer_load_dwordx4 %[bo2], %[srdb], s46 offen offset:2048 lds\n\t" \
    "buffer_load_dwordx4 %[bo3], %[srdb], s46 offen offset:3072 lds\n\t" \
    "s_add_u32 m0, s48, 19456\n\t" \
    "s_nop 0\n\t" \
    "buffer_load_dwordx4 %[bo4], %[srdb], s46 offen lds\n\t" \
    "s_waitcnt vmcnt(15) lgkmcnt(0)\n\t" \
    "s_barrier\n\t" \
    "ds_read_b64_tr_b16 a[64:65], %[brd] offset:0\n\t" \
    "ds_read_b64_tr_b16 a[66:67], %[brd] offset:1280\n\t" \
    "ds_read_b128 a[0:3], %[ard] offset:0\n\t" \
    "ds_read_b128 a[4:7], %[ard] offset:1024\n\t" \
    "ds_read_b128 a[8:11], %[ard] offset:2048\n\t" \
    "ds_read_b128 a[12:15], %[ard] offset:3072\n\t" \
    "ds_read_b64_tr_b16 a[68:69], %[brd] offset:256\n\t" \
    "ds_read_b64_tr_b16 a[70:71], %[brd] offset:1536\n\t" \
    "ds_read_b64_tr_b16 a[72:73], %[brd] offset:512\n\t" \
    "ds_read_b64_tr_b16 a[74:75], %[brd] offset:1792\n\t" \
    "ds_read_b64_tr_b16 a[76:77], %[brd] offset:768\n\t" \
    "ds_read_b64_tr_b16 a[78:79], %[brd] offset:2048\n\t" \
    "ds_read_b64_tr_b16 a[80:81], %[brd] offset:1024\n\t" \
    "ds_read_b64_tr_b16 a[82:83], %[brd] offset:2304\n\t" \
    "s_waitcnt lgkmcnt(0) vmcnt(10)\n\t" \
    "v_mfma_f32_16x16x32_bf16 v[24:27], a[0:3], a[64:67], 0\n\t" \
    "ds_read_b64_tr_b16 a[88:89], %[brd] offset:5120\n\t" \
    "v_mfma_f32_16x16x32_bf16 v[28:31], a[4:7], a[64:67], 0\n\t" \
    "ds_read_b64_tr_b16 a[90:91], %[brd] offset:6400\n\t" \
    "v_mfma_f32_16x16x32_bf16 v[32:35], a[8:11], a[64:67], 0\n\t" \
    "ds_read_b128 a[32:35], %[ard] offset:8192\n\t" \
    "v_mfma_f32_16x16x32_bf16 v[36:39], a[12:15], a[64:67], 0\n\t" \
    "ds_read_b128 a[36:39], %[ard] offset:9216\n\t" \
    "v_mfma_f32_16x16x32_bf16 v[40:43], a[0:3], a[68:71], 0\n\t" \
    "ds_read_b128 a[40:43], %[ard] offset:10240\n\t" \
    "v_mfma_f32_16x16x32_bf16 v[44:47], a[4:7], a[68:71], 0\n\t" \
    "ds_read_b128 a[44:47], %[ard] offset:11264\n\t" \
    "v_mfma_f32_16x16x32_bf16 v[48:51], a[8:11], a[68:71], 0\n\t" \
    "ds_read_b64_tr_b16 a[92:93], %[brd] offset:5376\n\t" \
    "v_mfma_f32_16x16x32_bf16 v[52:55], a[12:15], a[68:71], 0\n\t" \
    "ds_read_b64_tr_b16 a[94:95], %[brd] offset:6656\n\t" \
    "v_mfma_f32_16x16x32_bf16 v[56:59], a[0:3], a[72:75], 0\n\t" \
    "ds_read_b64_tr_b16 a[96:97], %[brd] offset:5632\n\t" \
    "v_mfma_f32_16x16x32_bf16 v[60:63], a[4:7], a[72:75], 0\n\t" \
    "ds_read_b64_tr_b16 a[98:99], %[brd] offset:6912\n\t" \
    "v_mfma_f32_16x16x32_bf16 v[64:67], a[8:11], a[72:75], 0\n\t" \
    "ds_read_b64_tr_b16 a[100:101], %[brd] offset:5888\n\t" \
    "v_mfma_f32_16x16x32_bf16 v[68:71], a[12:15], a[72:75], 0\n\t" \
    "ds_read_b64_tr_b16 a[102:103], %[brd] offset:7168\n\t" \
    "v_mfma_f32_16x16x32_bf16 v[72:75], a[0:3], a[76:79], 0\n\t" \
    "ds_read_b64_tr_b16 a[104:105], %[brd] offset:6144\n\t" \
    "v_mfma_f32_16x16x32_bf16 v[76:79], a[4:7], a[76:79], 0\n\t" \
    "ds_read_b64_tr_b16 a[106:107], %[brd] offset:7424\n\t" \
    "v_mfma_f32_16x16x32_bf16 v[80:83], a[8:11], a[76:79], 0\n\t" \
    "s_add_u32 m0, s48, 0\n\t" \
    "s_add_u32 s46, %[boff], 163840\n\t" \
    "buffer_load_dwordx4 %[bo0], %[srdb], s46 offen lds\n\t" \
    "v_mfma_f32_16x16x32_bf16 v[84:87], a[12:15], a[76:79], 0\n\t" \
    "buffer_load_dwordx4 %[bo1], %[srdb], s46 offen offset:1024 lds\n\t" \
    "v_mfma_f32_16x16x32_bf16 v[88:91], a[0:3], a[80:83], 0\n\t" \
    "buffer_load_dwordx4 %[bo2], %[srdb], s46 offen offset:2048 lds\n\t" \
    "v_mfma_f32_16x16x32_bf16 v[92:95], a[4:7], a[80:83], 0\n\t" \
    "buffer_load_dwordx4 %[bo3], %[srdb], s46 offen offset:3072 lds\n\t" \
    "v_mfma_f32_16x16x32_bf16 v[96:99], a[8:11], a[80:83], 0\n\t" \
    "s_add_u32 m0, s48, 4096\n\t" \
    "s_nop 0\n\t" \
    "buffer_load_dwordx4 %[bo4], %[srdb], s46 offen lds\n\t" \
    "v_mfma_f32_16x16x32_bf16 v[100:103], a[12:15], a[80:83], 0\n\t" \
    "s_waitcnt lgkmcnt(0) vmcnt(10)\n\t" \
    "v_mfma_f32_16x16x32_bf16 v[24:27], a[32:35], a[88:91], v[24:27]\n\t" \
    "ds_read_b64_tr_b16 a[64:65], %[brd] offset:10240\n\t" \
    "v_mfma_f32_16x16x32_bf16 v[28:31], a[36:39], a[88:91], v[28:31]\n\t" \
    "ds_read_b64_tr_b16 a[66:67], %[brd] offset:11520\n\t" \
    "v_mfma_f32_16x16x32_bf16 v[32:35], a[40:43], a[88:91], v[32:35]\n\t" \
    "ds_read_b128 a[0:3], %[ard] offset:16384\n\t" \
    "v_mfma_f32_16x16x32_bf16 v[36:39], a[44:47], a[88:91], v[36:39]\n\t" \
    "ds_read_b128 a[4:7], %[ard] offset:17408\n\t" \
    "v_mfma_f32_16x16x32_bf16 v[40:43], a[32:35], a[92:95], v[40:43]\n\t" \
    "ds_read_b128 a[8:11], %[ard] offset:18432\n\t" \
    "v_mfma_f32_16x16x32_bf16 v[44:47], a[36:39], a[92:95], v[44:47]\n\t" \
    "ds_read_b128 a[12:15], %[ard] offset:19456\n\t" \
    "v_mfma_f32_16x16x32_bf16 v[48:51], a[40:43], a[92:95], v[48:51]\n\t" \
    "ds_read_b64_tr_b16 a[68:69], %[brd] offset:10496\n\t" \
    "v_mfma_f32_16x16x32_bf16 v[52:55], a[44:47], a[92:95], v[52:55]\n\t" \
    "ds_read_b64_tr_b16 a[70:71], %[brd] offset:11776\n\t" \
    "v_mfma_f32_16x16x32_bf16 v[56:59], a[32:35], a[96:99], v[56:59]\n\t" \
    "ds_read_b64_tr_b16 a[72:73], %[brd] offset:10752\n\t" \
    "v_mfma_f32_16x16x32_bf16 v[60:63], a[36:39], a[96:99], v[60:63]\n\t" \
    "ds_read_b64_tr_b16 a[74:75], %[brd] offset:12032\n\t" \
    "v_mfma_f32_16x16x32_bf16 v[64:67], a[40:43], a[96:99], v[64:67]\n\t" \
    "ds_read_b64_tr_b16 a[76:77], %[brd] offset:11008\n\t" \
    "v_mfma_f32_16x16x32_bf16 v[68:71], a[44:47], a[96:99], v[68:71]\n\t" \
    "ds_read_b64_tr_b16 a[78:79], %[brd] offset:12288\n\t" \
    "v_mfma_f32_16x16x32_bf16 v[72:75], a[32:35], a[100:103], v[72:75]\n\t" \
    "ds_read_b64_tr_b16 a[80:81], %[brd] offset:11264\n\t" \
    "v_mfma_f32_16x16x32_bf16 v[76:79], a[36:39], a[100:103], v[76:79]\n\t" \
    "ds_read_b64_tr_b16 a[82:83], %[brd] offset:12544\n\t" \
    "v_mfma_f32_16x16x32_bf16 v[80:83], a[40:43], a[100:103], v[80:83]\n\t" \
    "s_add_u32 m0, s48, 5120\n\t" \
    "s_add_u32 s46, %[boff], 204800\n\t" \
    "buffer_load_dwordx4 %[bo0], %[srdb], s46 offen lds\n\t" \
    "v_mfma_f32_16x16x32_bf16 v[84:87], a[44:47], a[100:103], v[84:87]\n\t" \
    "buffer_load_dwordx4 %[bo1], %[srdb], s46 offen offset:1024 lds\n\t" \
    "v_mfma_f32_16x16x32_bf16 v[88:91], a[32:35], a[104:107], v[88:91]\n\t" \
    "buffer_load_dwordx4 %[bo2], %[srdb], s46 offen offset:2048 lds\n\t" \
    "v_mfma_f32_16x16x32_bf16 v[92:95], a[36:39], a[104:107], v[92:95]\n\t" \
    "buffer_load_dwordx4 %[bo3], %[srdb], s46 offen offset:3072 lds\n\t" \
    "v_mfma_f32_16x16x32_bf16 v[96:99], a[40:43], a[104:107], v[96:99]\n\t" \
    "s_add_u32 m0, s48, 9216\n\t" \
    "s_nop 0\n\t" \
    "buffer_load_dwordx4 %[bo4], %[srdb], s46 offen lds\n\t" \
    "v_mfma_f32_16x16x32_bf16 v[100:103], a[44:47], a[104:107], v[100:103]\n\t" \
    "s_waitcnt lgkmcnt(0) vmcnt(10)\n\t" \
    "v_mfma_f32_16x16x32_bf16 v[24:27], a[0:3], a[64:67], v[24:27]\n\t" \
    "ds_read_b64_tr_b16 a[88:89], %[brd] offset:15360\n\t" \
    "v_mfma_f32_16x16x32_bf16 v[28:31], a[4:7], a[64:67], v[28:31]\n\t" \
    "ds_read_b64_tr_b16 a[90:91], %[brd] offset:16640\n\t" \
    "v_mfma_f32_16x16x32_bf16 v[32:35], a[8:11], a[64:67], v[32:35]\n\t" \
    "ds_read_b128 a[32:35], %[ard] offset:24576\n\t" \
    "v_mfma_f32_16x16x32_bf16 v[36:39], a[12:15], a[64:67], v[36:39]\n\t" \
    "ds_read_b128 a[36:39], %[ard] offset:25600\n\t" \
    "v_mfma_f32_16x16x32_bf16 v[40:43], a[0:3], a[68:71], v[40:43]\n\t" \
    "ds_read_b128 a[40:43], %[ard] offset:26624\n\t" \
    "v_mfma_f32_16x16x32_bf16 v[44:47], a[4:7], a[68:71], v[44:47]\n\t" \
    "ds_read_b128 a[44:47], %[ard] offset:27648\n\t" \
    "v_mfma_f32_16x16x32_bf16 v[48:51], a[8:11], a[68:71], v[48:51]\n\t" \
    "ds_read_b64_tr_b16 a[92:93], %[brd] offset:15616\n\t" \
    "v_mfma_f32_16x16x32_bf16 v[52:55], a[12:15], a[68:71], v[52:55]\n\t" \
    "ds_read_b64_tr_b16 a[94:95], %[brd] offset:16896\n\t" \
    "v_mfma_f32_16x16x32_bf16 v[56:59], a[0:3], a[72:75], v[56:59]\n\t" \
    "ds_read_b64_tr_b16 a[96:97], %[brd] offset:15872\n\t" \
    "v_mfma_f32_16x16x32_bf16 v[60:63], a[4:7], a[72:75], v[60:63]\n\t" \
    "ds_read_b64_tr_b16 a[98:99], %[brd] offset:17152\n\t" \
    "v_mfma_f32_16x16x32_bf16 v[64:67], a[8:11], a[72:75], v[64:67]\n\t" \
    "ds_read_b64_tr_b16 a[100:101], %[brd] offset:16128\n\t" \
    "v_mfma_f32_16x16x32_bf16 v[68:71], a[12:15], a[72:75], v[68:71]\n\t" \
    "ds_read_b64_tr_b16 a[102:103], %[brd] offset:17408\n\t" \
    "v_mfma_f32_16x16x32_bf16 v[72:75], a[0:3], a[76:79], v[72:75]\n\t" \
    "ds_read_b64_tr_b16 a[104:105], %[brd] offset:16384\n\t" \
    "v_mfma_f32_16x16x32_bf16 v[76:79], a[4:7], a[76:79], v[76:79]\n\t" \
    "ds_read_b64_tr_b16 a[106:107], %[brd] offset:17664\n\t" \
    "v_mfma_f32_16x16x32_bf16 v[80:83], a[8:11], a[76:79], v[80:83]\n\t" \
    "s_add_u32 m0, s48, 10240\n\t" \
    "s_add_u32 s46, %[boff], 640\n\t" \
    "buffer_load_dwordx4 %[bo0], %[srdb], s46 offen lds\n\t" \
    "v_mfma_f32_16x16x32_bf16 v[84:87], a[12:15], a[76:79], v[84:87]\n\t" \
    "buffer_load_dwordx4 %[bo1], %[srdb], s46 offen offset:1024 lds\n\t" \
    "v_mfma_f32_16x16x32_bf16 v[88:91], a[0:3], a[80:83], v[88:91]\n\t" \
    "buffer_load_dwordx4 %[bo2], %[srdb], s46 offen offset:2048 lds\n\t" \
    "v_mfma_f32_16x16x32_bf16 v[92:95], a[4:7], a[80:83], v[92:95]\n\t" \
    "buffer_load_dwordx4 %[bo3], %[srdb], s46 offen offset:3072 lds\n\t" \
    "v_mfma_f32_16x16x32_bf16 v[96:99], a[8:11], a[80:83], v[96:99]\n\t" \
    "s_add_u32 m0, s48, 14336\n\t" \
    "s_nop 0\n\t" \
    "buffer_load_dwordx4 %[bo4], %[srdb], s46 offen lds\n\t" \
    "v_mfma_f32_16x16x32_bf16 v[100:103], a[12:15], a[80:83], v[100:103]\n\t" \
    "s_waitcnt lgkmcnt(0) vmcnt(10)\n\t" \
    "v_mfma_f32_16x16x32_bf16 v[24:27], a[32:35], a[88:91], v[24:27]\n\t" \
    "ds_read_b64_tr_b16 a[64:65], %[brd] offset:0\n\t" \
    "v_mfma_f32_16x16x32_bf16 v[28:31], a[36:39], a[88:91], v[28:31]\n\t" \
    "ds_read_b64_tr_b16 a[66:67], %[brd] offset:1280\n\t" \
    "v_mfma_f32_16x16x32_bf16 v[32:35], a[40:43], a[88:91], v[32:35]\n\t" \
    "ds_read_b128 a[0:3], %[ard] offset:32768\n\t" \
    "v_mfma_f32_16x16x32_bf16 v[36:39], a[44:47], a[88:91], v[36:39]\n\t" \
    "ds_read_b128 a[4:7], %[ard] offset:33792\n\t" \
    "v_mfma_f32_16x16x32_bf16 v[40:43], a[32:35], a[92:95], v[40:43]\n\t" \
    "ds_read_b128 a[8:11], %[ard] offset:34816\n\t" \
    "v_mfma_f32_16x16x32_bf16 v[44:47], a[36:39], a[92:95], v[44:47]\n\t" \
    "ds_read_b128 a[12:15], %[ard] offset:35840\n\t" \
    "v_mfma_f32_16x16x32_bf16 v[48:51], a[40:43], a[92:95], v[48:51]\n\t" \
    "ds_read_b64_tr_b16 a[68:69], %[brd] offset:256\n\t" \
    "v_mfma_f32_16x16x32_bf16 v[52:55], a[44:47], a[92:95], v[52:55]\n\t" \
    "ds_read_b64_tr_b16 a[70:71], %[brd] offset:1536\n\t" \
    "v_mfma_f32_16x16x32_bf16 v[56:59], a[32:35], a[96:99], v[56:59]\n\t" \
    "ds_read_b64_tr_b16 a[72:73], %[brd] offset:512\n\t" \
    "v_mfma_f32_16x16x32_bf16 v[60:63], a[36:39], a[96:99], v[60:63]\n\t" \
    "ds_read_b64_tr_b16 a[74:75], %[brd] offset:1792\n\t" \
    "v_mfma_f32_16x16x32_bf16 v[64:67], a[40:43], a[96:99], v[64:67]\n\t" \
    "ds_read_b64_tr_b16 a[76:77], %[brd] offset:768\n\t" \
    "v_mfma_f32_16x16x32_bf16 v[68:71], a[44:47], a[96:99], v[68:71]\n\t" \
    "ds_read_b64_tr_b16 a[78:79], %[brd] offset:2048\n\t" \
    "v_mfma_f32_16x16x32_bf16 v[72:75], a[32:35], a[100:103], v[72:75]\n\t" \
    "ds_read_b64_tr_b16 a[80:81], %[brd] offset:1024\n\t" \
    "v_mfma_f32_16x16x32_bf16 v[76:79], a[36:39], a[100:103], v[76:79]\n\t" \
    "ds_read_b64_tr_b16 a[82:83], %[brd] offset:2304\n\t" \
    "v_mfma_f32_16x16x32_bf16 v[80:83], a[40:43], a[100:103], v[80:83]\n\t" \
    "s_add_u32 m0, s48, 15360\n\t" \
    "s_add_u32 s46, %[boff], 41600\n\t" \
    "buffer_load_dwordx4 %[bo0], %[srdb], s46 offen lds\n\t" \
    "v_mfma_f32_16x16x32_bf16 v[84:87], a[44:47], a[100:103], v[84:87]\n\t" \
    "buffer_load_dwordx4 %[bo1], %[srdb], s46 offen offset:1024 lds\n\t" \
    "v_mfma_f32_16x16x32_bf16 v[88:91], a[32:35], a[104:107], v[88:91]\n\t" \
    "buffer_load_dwordx4 %[bo2], %[srdb], s46 offen offset:2048 lds\n\t" \
    "v_mfma_f32_16x16x32_bf16 v[92:95], a[36:39], a[104:107], v[92:95]\n\t" \
    "buffer_load_dwordx4 %[bo3], %[srdb], s46 offen offset:3072 lds\n\t" \
    "v_mfma_f32_16x16x32_bf16 v[96:99], a[40:43], a[104:107], v[96:99]\n\t" \
    "s_add_u32 m0, s48, 19456\n\t" \
    "s_nop 0\n\t" \
    "buffer_load_dwordx4 %[bo4], %[srdb], s46 offen lds\n\t" \
    "v_mfma_f32_16x16x32_bf16 v[100:103], a[44:47], a[104:107], v[100:103]\n\t" \
    "s_waitcnt lgkmcnt(0) vmcnt(10)\n\t" \
    "v_mfma_f32_16x16x32_bf16 v[24:27], a[0:3], a[64:67], v[24:27]\n\t" \
    "ds_read_b64_tr_b16 a[88:89], %[brd] offset:5120\n\t" \
    "v_mfma_f32_16x16x32_bf16 v[28:31], a[4:7], a[64:67], v[28:31]\n\t" \
    "ds_read_b64_tr_b16 a[90:91], %[brd] offset:6400\n\t" \
    "v_mfma_f32_16x16x32_bf16 v[32:35], a[8:11], a[64:67], v[32:35]\n\t" \
    "ds_read_b128 a[32:35], %[ard] offset:40960\n\t" \
    "v_mfma_f32_16x16x32_bf16 v[36:39], a[12:15], a[64:67], v[36:39]\n\t" \
    "ds_read_b128 a[36:39], %[ard] offset:41984\n\t" \
    "v_mfma_f32_16x16x32_bf16 v[40:43], a[0:3], a[68:71], v[40:43]\n\t" \
    "ds_read_b128 a[40:43], %[ard] offset:43008\n\t" \
    "v_mfma_f32_16x16x32_bf16 v[44:47], a[4:7], a[68:71], v[44:47]\n\t" \
    "ds_read_b128 a[44:47], %[ard] offset:44032\n\t" \
    "v_mfma_f32_16x16x32_bf16 v[48:51], a[8:11], a[68:71], v[48:51]\n\t" \
    "ds_read_b64_tr_b16 a[92:93], %[brd] offset:5376\n\t" \
    "v_mfma_f32_16x16x32_bf16 v[52:55], a[12:15], a[68:71], v[52:55]\n\t" \
    "ds_read_b64_tr_b16 a[94:95], %[brd] offset:6656\n\t" \
    "v_mfma_f32_16x16x32_bf16 v[56:59], a[0:3], a[72:75], v[56:59]\n\t" \
    "ds_read_b64_tr_b16 a[96:97], %[brd] offset:5632\n\t" \
    "v_mfma_f32_16x16x32_bf16 v[60:63], a[4:7], a[72:75], v[60:63]\n\t" \
    "ds_read_b64_tr_b16 a[98:99], %[brd] offset:6912\n\t" \
    "v_mfma_f32_16x16x32_bf16 v[64:67], a[8:11], a[72:75], v[64:67]\n\t" \
    "ds_read_b64_tr_b16 a[100:101], %[brd] offset:5888\n\t" \
    "v_mfma_f32_16x16x32_bf16 v[68:71], a[12:15], a[72:75], v[68:71]\n\t" \
    "ds_read_b64_tr_b16 a[102:103], %[brd] offset:7168\n\t" \
    "v_mfma_f32_16x16x32_bf16 v[72:75], a[0:3], a[76:79], v[72:75]\n\t" \
    "ds_read_b64_tr_b16 a[104:105], %[brd] offset:6144\n\t" \
    "v_mfma_f32_16x16x32_bf16 v[76:79], a[4:7], a[76:79], v[76:79]\n\t" \
    "ds_read_b64_tr_b16 a[106:107], %[brd] offset:7424\n\t" \
    "v_mfma_f32_16x16x32_bf16 v[80:83], a[8:11], a[76:79], v[80:83]\n\t" \
    "s_add_u32 m0, s48, 0\n\t" \
    "s_add_u32 s46, %[boff], 82560\n\t" \
    "buffer_load_dwordx4 %[bo0], %[srdb], s46 offen lds\n\t" \
    "v_mfma_f32_16x16x32_bf16 v[84:87], a[12:15], a[76:79], v[84:87]\n\t" \
    "buffer_load_dwordx4 %[bo1], %[srdb], s46 offen offset:1024 lds\n\t" \
    "v_mfma_f32_16x16x32_bf16 v[88:91], a[0:3], a[80:83], v[88:91]\n\t" \
    "buffer_load_dwordx4 %[bo2], %[srdb], s46 offen offset:2048 lds\n\t" \
    "v_mfma_f32_16x16x32_bf16 v[92:95], a[4:7], a[80:83], v[92:95]\n\t" \
    "buffer_load_dwordx4 %[bo3], %[srdb], s46 offen offset:3072 lds\n\t" \
    "v_mfma_f32_16x16x32_bf16 v[96:99], a[8:11], a[80:83], v[96:99]\n\t" \
    "s_add_u32 m0, s48, 4096\n\t" \
    "s_nop 0\n\t" \
    "buffer_load_dwordx4 %[bo4], %[srdb], s46 offen lds\n\t" \
    "v_mfma_f32_16x16x32_bf16 v[100:103], a[12:15], a[80:83], v[100:103]\n\t" \
    "s_waitcnt lgkmcnt(0) vmcnt(10)\n\t" \
    "v_mfma_f32_16x16x32_bf16 v[24:27], a[32:35], a[88:91], v[24:27]\n\t" \
    "ds_read_b64_tr_b16 a[64:65], %[brd] offset:10240\n\t" \
    "v_mfma_f32_16x16x32_bf16 v[28:31], a[36:39], a[88:91], v[28:31]\n\t" \
    "ds_read_b64_tr_b16 a[66:67], %[brd] offset:11520\n\t" \
    "v_mfma_f32_16x16x32_bf16 v[32:35], a[40:43], a[88:91], v[32:35]\n\t" \
    "ds_read_b128 a[0:3], %[ard] offset:0\n\t" \
    "v_mfma_f32_16x16x32_bf16 v[36:39], a[44:47], a[88:91], v[36:39]\n\t" \
    "ds_read_b128 a[4:7], %[ard] offset:1024\n\t" \
    "v_mfma_f32_16x16x32_bf16 v[40:43], a[32:35], a[92:95], v[40:43]\n\t" \
    "ds_read_b128 a[8:11], %[ard] offset:2048\n\t" \
    "v_mfma_f32_16x16x32_bf16 v[44:47], a[36:39], a[92:95], v[44:47]\n\t" \
    "ds_read_b128 a[12:15], %[ard] offset:3072\n\t" \
    "v_mfma_f32_16x16x32_bf16 v[48:51], a[40:43], a[92:95], v[48:51]\n\t" \
    "ds_read_b64_tr_b16 a[68:69], %[brd] offset:10496\n\t" \
    "v_mfma_f32_16x16x32_bf16 v[52:55], a[44:47], a[92:95], v[52:55]\n\t" \
    "ds_read_b64_tr_b16 a[70:71], %[brd] offset:11776\n\t" \
    "v_mfma_f32_16x16x32_bf16 v[56:59], a[32:35], a[96:99], v[56:59]\n\t" \
    "ds_read_b64_tr_b16 a[72:73], %[brd] offset:10752\n\t" \
    "v_mfma_f32_16x16x32_bf16 v[60:63], a[36:39], a[96:99], v[60:63]\n\t" \
    "ds_read_b64_tr_b16 a[74:75], %[brd] offset:12032\n\t" \
    "v_mfma_f32_16x16x32_bf16 v[64:67], a[40:43], a[96:99], v[64:67]\n\t" \
    "ds_read_b64_tr_b16 a[76:77], %[brd] offset:11008\n\t" \
    "v_mfma_f32_16x16x32_bf16 v[68:71], a[44:47], a[96:99], v[68:71]\n\t" \
    "ds_read_b64_tr_b16 a[78:79], %[brd] offset:12288\n\t" \
    "v_mfma_f32_16x16x32_bf16 v[72:75], a[32:35], a[100:103], v[72:75]\n\t" \
    "ds_read_b64_tr_b16 a[80:81], %[brd] offset:11264\n\t" \
    "v_mfma_f32_16x16x32_bf16 v[76:79], a[36:39], a[100:103], v[76:79]\n\t" \
    "ds_read_b64_tr_b16 a[82:83], %[brd] offset:12544\n\t" \
    "v_mfma_f32_16x16x32_bf16 v[80:83], a[40:43], a[100:103], v[80:83]\n\t" \
    "s_add_u32 m0, s48, 5120\n\t" \
    "s_add_u32 s46, %[boff], 123520\n\t" \
    "buffer_load_dwordx4 %[bo0], %[srdb], s46 offen lds\n\t" \
    "v_mfma_f32_16x16x32_bf16 v[84:87], a[44:47], a[100:103], v[84:87]\n\t" \
    "buffer_load_dwordx4 %[bo1], %[srdb], s46 offen offset:1024 lds\n\t" \
    "v_mfma_f32_16x16x32_bf16 v[88:91], a[32:35], a[104:107], v[88:91]\n\t" \
    "buffer_load_dwordx4 %[bo2], %[srdb], s46 offen offset:2048 lds\n\t" \
    "v_mfma_f32_16x16x32_bf16 v[92:95], a[36:39], a[104:107], v[92:95]\n\t" \
    "buffer_load_dwordx4 %[bo3], %[srdb], s46 offen offset:3072 lds\n\t" \
    "v_mfma_f32_16x16x32_bf16 v[96:99], a[40:43], a[104:107], v[96:99]\n\t" \
    "s_add_u32 m0, s48, 9216\n\t" \
    "s_nop 0\n\t" \
    "buffer_load_dwordx4 %[bo4], %[srdb], s46 offen lds\n\t" \
    "v_mfma_f32_16x16x32_bf16 v[100:103], a[44:47], a[104:107], v[100:103]\n\t" \
    "s_nop 7\n\t" \
    "s_mul_i32 s45, %[osc], 0\n\t" \
    "s_add_u32 s45, s45, %[ooff]\n\t" \
    "v_cvt_pk_bf16_f32 v232, v24, v25\n\t" \
    "v_cvt_pk_bf16_f32 v233, v26, v27\n\t" \
    "v_cvt_pk_bf16_f32 v234, v28, v29\n\t" \
    "v_cvt_pk_bf16_f32 v235, v30, v31\n\t" \
    "buffer_store_dwordx4 v[232:235], %[ov], %[srda], s45 offen sc1\n\t" \
    "s_add_u32 s45, s45, %[osi]\n\t" \
    "v_cvt_pk_bf16_f32 v236, v32, v33\n\t" \
    "v_cvt_pk_bf16_f32 v237, v34, v35\n\t" \
    "v_cvt_pk_bf16_f32 v238, v36, v37\n\t" \
    "v_cvt_pk_bf16_f32 v239, v38, v39\n\t" \
    "buffer_store_dwordx4 v[236:239], %[ov], %[srda], s45 offen sc1\n\t" \
    "s_mul_i32 s45, %[osc], 2\n\t" \
    "s_add_u32 s45, s45, %[ooff]\n\t" \
    "v_cvt_pk_bf16_f32 v240, v40, v41\n\t" \
    "v_cvt_pk_bf16_f32 v241, v42, v43\n\t" \
    "v_cvt_pk_bf16_f32 v242, v44, v45\n\t" \
    "v_cvt_pk_bf16_f32 v243, v46, v47\n\t" \
    "buffer_store_dwordx4 v[240:243], %[ov], %[srda], s45 offen sc1\n\t" \
    "s_add_u32 s45, s45, %[osi]\n\t" \
    "v_cvt_pk_bf16_f32 v244, v48, v49\n\t" \
    "v_cvt_pk_bf16_f32 v245, v50, v51\n\t" \
    "v_cvt_pk_bf16_f32 v246, v52, v53\n\t" \
    "v_cvt_pk_bf16_f32 v247, v54, v55\n\t" \
    "buffer_store_dwordx4 v[244:247], %[ov], %[srda], s45 offen sc1\n\t" \
    "s_mul_i32 s45, %[osc], 4\n\t" \
    "s_add_u32 s45, s45, %[ooff]\n\t" \
    "v_cvt_pk_bf16_f32 v248, v56, v57\n\t" \
    "v_cvt_pk_bf16_f32 v249, v58, v59\n\t" \
    "v_cvt_pk_bf16_f32 v250, v60, v61\n\t" \
    "v_cvt_pk_bf16_f32 v251, v62, v63\n\t" \
    "buffer_store_dwordx4 v[248:251], %[ov], %[srda], s45 offen sc1\n\t" \
    "s_add_u32 s45, s45, %[osi]\n\t" \
    "v_cvt_pk_bf16_f32 v252, v64, v65\n\t" \
    "v_cvt_pk_bf16_f32 v253, v66, v67\n\t" \
    "v_cvt_pk_bf16_f32 v254, v68, v69\n\t" \
    "v_cvt_pk_bf16_f32 v255, v70, v71\n\t" \
    "buffer_store_dwordx4 v[252:255], %[ov], %[srda], s45 offen sc1\n\t" \
    "s_mul_i32 s45, %[osc], 6\n\t" \
    "s_add_u32 s45, s45, %[ooff]\n\t" \
    "v_cvt_pk_bf16_f32 v232, v72, v73\n\t" \
    "v_cvt_pk_bf16_f32 v233, v74, v75\n\t" \
    "v_cvt_pk_bf16_f32 v234, v76, v77\n\t" \
    "v_cvt_pk_bf16_f32 v235, v78, v79\n\t" \
    "buffer_store_dwordx4 v[232:235], %[ov], %[srda], s45 offen sc1\n\t" \
    "s_add_u32 s45, s45, %[osi]\n\t" \
    "v_cvt_pk_bf16_f32 v236, v80, v81\n\t" \
    "v_cvt_pk_bf16_f32 v237, v82, v83\n\t" \
    "v_cvt_pk_bf16_f32 v238, v84, v85\n\t" \
    "v_cvt_pk_bf16_f32 v239, v86, v87\n\t" \
    "buffer_store_dwordx4 v[236:239], %[ov], %[srda], s45 offen sc1\n\t" \
    "s_mul_i32 s45, %[osc], 8\n\t" \
    "s_add_u32 s45, s45, %[ooff]\n\t" \
    "v_cvt_pk_bf16_f32 v240, v88, v89\n\t" \
    "v_cvt_pk_bf16_f32 v241, v90, v91\n\t" \
    "v_cvt_pk_bf16_f32 v242, v92, v93\n\t" \
    "v_cvt_pk_bf16_f32 v243, v94, v95\n\t" \
    "buffer_store_dwordx4 v[240:243], %[ov], %[srda], s45 offen sc1\n\t" \
    "s_add_u32 s45, s45, %[osi]\n\t" \
    "v_cvt_pk_bf16_f32 v244, v96, v97\n\t" \
    "v_cvt_pk_bf16_f32 v245, v98, v99\n\t" \
    "v_cvt_pk_bf16_f32 v246, v100, v101\n\t" \
    "v_cvt_pk_bf16_f32 v247, v102, v103\n\t" \
    "buffer_store_dwordx4 v[244:247], %[ov], %[srda], s45 offen sc1\n\t" \
    "s_waitcnt lgkmcnt(0) vmcnt(20)\n\t" \
    "v_mfma_f32_16x16x32_bf16 v[24:27], a[0:3], a[64:67], 0\n\t" \
    "ds_read_b64_tr_b16 a[88:89], %[brd] offset:15360\n\t" \
    "v_mfma_f32_16x16x32_bf16 v[28:31], a[4:7], a[64:67], 0\n\t" \
    "ds_read_b64_tr_b16 a[90:91], %[brd] offset:16640\n\t" \
    "v_mfma_f32_16x16x32_bf16 v[32:35], a[8:11], a[64:67], 0\n\t" \
    "ds_read_b128 a[32:35], %[ard] offset:8192\n\t" \
    "v_mfma_f32_16x16x32_bf16 v[36:39], a[12:15], a[64:67], 0\n\t" \
    "ds_read_b128 a[36:39], %[ard] offset:9216\n\t" \
    "v_mfma_f32_16x16x32_bf16 v[40:43], a[0:3], a[68:71], 0\n\t" \
    "ds_read_b128 a[40:43], %[ard] offset:10240\n\t" \
    "v_mfma_f32_16x16x32_bf16 v[44:47], a[4:7], a[68:71], 0\n\t" \
    "ds_read_b128 a[44:47], %[ard] offset:11264\n\t" \
    "v_mfma_f32_16x16x32_bf16 v[48:51], a[8:11], a[68:71], 0\n\t" \
    "ds_read_b64_tr_b16 a[92:93], %[brd] offset:15616\n\t" \
    "v_mfma_f32_16x16x32_bf16 v[52:55], a[12:15], a[68:71], 0\n\t" \
    "ds_read_b64_tr_b16 a[94:95], %[brd] offset:16896\n\t" \
    "v_mfma_f32_16x16x32_bf16 v[56:59], a[0:3], a[72:75], 0\n\t" \
    "ds_read_b64_tr_b16 a[96:97], %[brd] offset:15872\n\t" \
    "v_mfma_f32_16x16x32_bf16 v[60:63], a[4:7], a[72:75], 0\n\t" \
    "ds_read_b64_tr_b16 a[98:99], %[brd] offset:17152\n\t" \
    "v_mfma_f32_16x16x32_bf16 v[64:67], a[8:11], a[72:75], 0\n\t" \
    "ds_read_b64_tr_b16 a[100:101], %[brd] offset:16128\n\t" \
    "v_mfma_f32_16x16x32_bf16 v[68:71], a[12:15], a[72:75], 0\n\t" \
    "ds_read_b64_tr_b16 a[102:103], %[brd] offset:17408\n\t" \
    "v_mfma_f32_16x16x32_bf16 v[72:75], a[0:3], a[76:79], 0\n\t" \
    "ds_read_b64_tr_b16 a[104:105], %[brd] offset:16384\n\t" \
    "v_mfma_f32_16x16x32_bf16 v[76:79], a[4:7], a[76:79], 0\n\t" \
    "ds_read_b64_tr_b16 a[106:107], %[brd] offset:17664\n\t" \
    "v_mfma_f32_16x16x32_bf16 v[80:83], a[8:11], a[76:79], 0\n\t" \
    "s_add_u32 m0, s48, 10240\n\t" \
    "s_add_u32 s46, %[boff], 164480\n\t" \
    "buffer_load_dwordx4 %[bo0], %[srdb], s46 offen lds\n\t" \
    "v_mfma_f32_16x16x32_bf16 v[84:87], a[12:15], a[76:79], 0\n\t" \
    "buffer_load_dwordx4 %[bo1], %[srdb], s46 offen offset:1024 lds\n\t" \
    "v_mfma_f32_16x16x32_bf16 v[88:91], a[0:3], a[80:83], 0\n\t" \
    "buffer_load_dwordx4 %[bo2], %[srdb], s46 offen offset:2048 lds\n\t" \
    "v_mfma_f32_16x16x32_bf16 v[92:95], a[4:7], a[80:83], 0\n\t" \
    "buffer_load_dwordx4 %[bo3], %[srdb], s46 offen offset:3072 lds\n\t" \
    "v_mfma_f32_16x16x32_bf16 v[96:99], a[8:11], a[80:83], 0\n\t" \
    "s_add_u32 m0, s48, 14336\n\t" \
    "s_nop 0\n\t" \
    "buffer_load_dwordx4 %[bo4], %[srdb], s46 offen lds\n\t" \
    "v_mfma_f32_16x16x32_bf16 v[100:103], a[12:15], a[80:83], 0\n\t" \
    "s_waitcnt lgkmcnt(0) vmcnt(20)\n\t" \
    "v_mfma_f32_16x16x32_bf16 v[24:27], a[32:35], a[88:91], v[24:27]\n\t" \
    "ds_read_b64_tr_b16 a[64:65], %[brd] offset:0\n\t" \
    "v_mfma_f32_16x16x32_bf16 v[28:31], a[36:39], a[88:91], v[28:31]\n\t" \
    "ds_read_b64_tr_b16 a[66:67], %[brd] offset:1280\n\t" \
    "v_mfma_f32_16x16x32_bf16 v[32:35], a[40:43], a[88:91], v[32:35]\n\t" \
    "ds_read_b128 a[0:3], %[ard] offset:16384\n\t" \
    "v_mfma_f32_16x16x32_bf16 v[36:39], a[44:47], a[88:91], v[36:39]\n\t" \
    "ds_read_b128 a[4:7], %[ard] offset:17408\n\t" \
    "v_mfma_f32_16x16x32_bf16 v[40:43], a[32:35], a[92:95], v[40:43]\n\t" \
    "ds_read_b128 a[8:11], %[ard] offset:18432\n\t" \
    "v_mfma_f32_16x16x32_bf16 v[44:47], a[36:39], a[92:95], v[44:47]\n\t" \
    "ds_read_b128 a[12:15], %[ard] offset:19456\n\t" \
    "v_mfma_f32_16x16x32_bf16 v[48:51], a[40:43], a[92:95], v[48:51]\n\t" \
    "ds_read_b64_tr_b16 a[68:69], %[brd] offset:256\n\t" \
    "v_mfma_f32_16x16x32_bf16 v[52:55], a[44:47], a[92:95], v[52:55]\n\t" \
    "ds_read_b64_tr_b16 a[70:71], %[brd] offset:1536\n\t" \
    "v_mfma_f32_16x16x32_bf16 v[56:59], a[32:35], a[96:99], v[56:59]\n\t" \
    "ds_read_b64_tr_b16 a[72:73], %[brd] offset:512\n\t" \
    "v_mfma_f32_16x16x32_bf16 v[60:63], a[36:39], a[96:99], v[60:63]\n\t" \
    "ds_read_b64_tr_b16 a[74:75], %[brd] offset:1792\n\t" \
    "v_mfma_f32_16x16x32_bf16 v[64:67], a[40:43], a[96:99], v[64:67]\n\t" \
    "ds_read_b64_tr_b16 a[76:77], %[brd] offset:768\n\t" \
    "v_mfma_f32_16x16x32_bf16 v[68:71], a[44:47], a[96:99], v[68:71]\n\t" \
    "ds_read_b64_tr_b16 a[78:79], %[brd] offset:2048\n\t" \
    "v_mfma_f32_16x16x32_bf16 v[72:75], a[32:35], a[100:103], v[72:75]\n\t" \
    "ds_read_b64_tr_b16 a[80:81], %[brd] offset:1024\n\t" \
    "v_mfma_f32_16x16x32_bf16 v[76:79], a[36:39], a[100:103], v[76:79]\n\t" \
    "ds_read_b64_tr_b16 a[82:83], %[brd] offset:2304\n\t" \
    "v_mfma_f32_16x16x32_bf16 v[80:83], a[40:43], a[100:103], v[80:83]\n\t" \
    "s_add_u32 m0, s48, 15360\n\t" \
    "s_add_u32 s46, %[boff], 205440\n\t" \
    "buffer_load_dwordx4 %[bo0], %[srdb], s46 offen lds\n\t" \
    "v_mfma_f32_16x16x32_bf16 v[84:87], a[44:47], a[100:103], v[84:87]\n\t" \
    "buffer_load_dwordx4 %[bo1], %[srdb], s46 offen offset:1024 lds\n\t" \
    "v_mfma_f32_16x16x32_bf16 v[88:91], a[32:35], a[104:107], v[88:91]\n\t" \
    "buffer_load_dwordx4 %[bo2], %[srdb], s46 offen offset:2048 lds\n\t" \
    "v_mfma_f32_16x16x32_bf16 v[92:95], a[36:39], a[104:107], v[92:95]\n\t" \
    "buffer_load_dwordx4 %[bo3], %[srdb], s46 offen offset:3072 lds\n\t" \
    "v_mfma_f32_16x16x32_bf16 v[96:99], a[40:43], a[104:107], v[96:99]\n\t" \
    "s_add_u32 m0, s48, 19456\n\t" \
    "s_nop 0\n\t" \
    "buffer_load_dwordx4 %[bo4], %[srdb], s46 offen lds\n\t" \
    "v_mfma_f32_16x16x32_bf16 v[100:103], a[44:47], a[104:107], v[100:103]\n\t" \
    "s_waitcnt lgkmcnt(0) vmcnt(20)\n\t" \
    "v_mfma_f32_16x16x32_bf16 v[24:27], a[0:3], a[64:67], v[24:27]\n\t" \
    "ds_read_b64_tr_b16 a[88:89], %[brd] offset:5120\n\t" \
    "v_mfma_f32_16x16x32_bf16 v[28:31], a[4:7], a[64:67], v[28:31]\n\t" \
    "ds_read_b64_tr_b16 a[90:91], %[brd] offset:6400\n\t" \
    "v_mfma_f32_16x16x32_bf16 v[32:35], a[8:11], a[64:67], v[32:35]\n\t" \
    "ds_read_b128 a[32:35], %[ard] offset:24576\n\t" \
    "v_mfma_f32_16x16x32_bf16 v[36:39], a[12:15], a[64:67], v[36:39]\n\t" \
    "ds_read_b128 a[36:39], %[ard] offset:25600\n\t" \
    "v_mfma_f32_16x16x32_bf16 v[40:43], a[0:3], a[68:71], v[40:43]\n\t" \
    "ds_read_b128 a[40:43], %[ard] offset:26624\n\t" \
    "v_mfma_f32_16x16x32_bf16 v[44:47], a[4:7], a[68:71], v[44:47]\n\t" \
    "ds_read_b128 a[44:47], %[ard] offset:27648\n\t" \
    "v_mfma_f32_16x16x32_bf16 v[48:51], a[8:11], a[68:71], v[48:51]\n\t" \
    "ds_read_b64_tr_b16 a[92:93], %[brd] offset:5376\n\t" \
    "v_mfma_f32_16x16x32_bf16 v[52:55], a[12:15], a[68:71], v[52:55]\n\t" \
    "ds_read_b64_tr_b16 a[94:95], %[brd] offset:6656\n\t" \
    "v_mfma_f32_16x16x32_bf16 v[56:59], a[0:3], a[72:75], v[56:59]\n\t" \
    "ds_read_b64_tr_b16 a[96:97], %[brd] offset:5632\n\t" \
    "v_mfma_f32_16x16x32_bf16 v[60:63], a[4:7], a[72:75], v[60:63]\n\t" \
    "ds_read_b64_tr_b16 a[98:99], %[brd] offset:6912\n\t" \
    "v_mfma_f32_16x16x32_bf16 v[64:67], a[8:11], a[72:75], v[64:67]\n\t" \
    "ds_read_b64_tr_b16 a[100:101], %[brd] offset:5888\n\t" \
    "v_mfma_f32_16x16x32_bf16 v[68:71], a[12:15], a[72:75], v[68:71]\n\t" \
    "ds_read_b64_tr_b16 a[102:103], %[brd] offset:7168\n\t" \
    "v_mfma_f32_16x16x32_bf16 v[72:75], a[0:3], a[76:79], v[72:75]\n\t" \
    "ds_read_b64_tr_b16 a[104:105], %[brd] offset:6144\n\t" \
    "v_mfma_f32_16x16x32_bf16 v[76:79], a[4:7], a[76:79], v[76:79]\n\t" \
    "ds_read_b64_tr_b16 a[106:107], %[brd] offset:7424\n\t" \
    "v_mfma_f32_16x16x32_bf16 v[80:83], a[8:11], a[76:79], v[80:83]\n\t" \
    "v_mfma_f32_16x16x32_bf16 v[84:87], a[12:15], a[76:79], v[84:87]\n\t" \
    "v_mfma_f32_16x16x32_bf16 v[88:91], a[0:3], a[80:83], v[88:91]\n\t" \
    "v_mfma_f32_16x16x32_bf16 v[92:95], a[4:7], a[80:83], v[92:95]\n\t" \
    "v_mfma_f32_16x16x32_bf16 v[96:99], a[8:11], a[80:83], v[96:99]\n\t" \
    "v_mfma_f32_16x16x32_bf16 v[100:103], a[12:15], a[80:83], v[100:103]\n\t" \
    "s_waitcnt lgkmcnt(0) vmcnt(5)\n\t" \
    "v_mfma_f32_16x16x32_bf16 v[24:27], a[32:35], a[88:91], v[24:27]\n\t" \
    "ds_read_b64_tr_b16 a[64:65], %[brd] offset:10240\n\t" \
    "v_mfma_f32_16x16x32_bf16 v[28:31], a[36:39], a[88:91], v[28:31]\n\t" \
    "ds_read_b64_tr_b16 a[66:67], %[brd] offset:11520\n\t" \
    "v_mfma_f32_16x16x32_bf16 v[32:35], a[40:43], a[88:91], v[32:35]\n\t" \
    "ds_read_b128 a[0:3], %[ard] offset:32768\n\t" \
    "v_mfma_f32_16x16x32_bf16 v[36:39], a[44:47], a[88:91], v[36:39]\n\t" \
    "ds_read_b128 a[4:7], %[ard] offset:33792\n\t" \
    "v_mfma_f32_16x16x32_bf16 v[40:43], a[32:35], a[92:95], v[40:43]\n\t" \
    "ds_read_b128 a[8:11], %[ard] offset:34816\n\t" \
    "v_mfma_f32_16x16x32_bf16 v[44:47], a[36:39], a[92:95], v[44:47]\n\t" \
    "ds_read_b128 a[12:15], %[ard] offset:35840\n\t" \
    "v_mfma_f32_16x16x32_bf16 v[48:51], a[40:43], a[92:95], v[48:51]\n\t" \
    "ds_read_b64_tr_b16 a[68:69], %[brd] offset:10496\n\t" \
    "v_mfma_f32_16x16x32_bf16 v[52:55], a[44:47], a[92:95], v[52:55]\n\t" \
    "ds_read_b64_tr_b16 a[70:71], %[brd] offset:11776\n\t" \
    "v_mfma_f32_16x16x32_bf16 v[56:59], a[32:35], a[96:99], v[56:59]\n\t" \
    "ds_read_b64_tr_b16 a[72:73], %[brd] offset:10752\n\t" \
    "v_mfma_f32_16x16x32_bf16 v[60:63], a[36:39], a[96:99], v[60:63]\n\t" \
    "ds_read_b64_tr_b16 a[74:75], %[brd] offset:12032\n\t" \
    "v_mfma_f32_16x16x32_bf16 v[64:67], a[40:43], a[96:99], v[64:67]\n\t" \
    "ds_read_b64_tr_b16 a[76:77], %[brd] offset:11008\n\t" \
    "v_mfma_f32_16x16x32_bf16 v[68:71], a[44:47], a[96:99], v[68:71]\n\t" \
    "ds_read_b64_tr_b16 a[78:79], %[brd] offset:12288\n\t" \
    "v_mfma_f32_16x16x32_bf16 v[72:75], a[32:35], a[100:103], v[72:75]\n\t" \
    "ds_read_b64_tr_b16 a[80:81], %[brd] offset:11264\n\t" \
    "v_mfma_f32_16x16x32_bf16 v[76:79], a[36:39], a[100:103], v[76:79]\n\t" \
    "ds_read_b64_tr_b16 a[82:83], %[brd] offset:12544\n\t" \
    "v_mfma_f32_16x16x32_bf16 v[80:83], a[40:43], a[100:103], v[80:83]\n\t" \
    "v_mfma_f32_16x16x32_bf16 v[84:87], a[44:47], a[100:103], v[84:87]\n\t" \
    "v_mfma_f32_16x16x32_bf16 v[88:91], a[32:35], a[104:107], v[88:91]\n\t" \
    "v_mfma_f32_16x16x32_bf16 v[92:95], a[36:39], a[104:107], v[92:95]\n\t" \
    "v_mfma_f32_16x16x32_bf16 v[96:99], a[40:43], a[104:107], v[96:99]\n\t" \
    "v_mfma_f32_16x16x32_bf16 v[100:103], a[44:47], a[104:107], v[100:103]\n\t" \
    "s_waitcnt lgkmcnt(0) vmcnt(0)\n\t" \
    "v_mfma_f32_16x16x32_bf16 v[24:27], a[0:3], a[64:67], v[24:27]\n\t" \
    "ds_read_b64_tr_b16 a[88:89], %[brd] offset:15360\n\t" \
    "v_mfma_f32_16x16x32_bf16 v[28:31], a[4:7], a[64:67], v[28:31]\n\t" \
    "ds_read_b64_tr_b16 a[90:91], %[brd] offset:16640\n\t" \
    "v_mfma_f32_16x16x32_bf16 v[32:35], a[8:11], a[64:67], v[32:35]\n\t" \
    "ds_read_b128 a[32:35], %[ard] offset:40960\n\t" \
    "v_mfma_f32_16x16x32_bf16 v[36:39], a[12:15], a[64:67], v[36:39]\n\t" \
    "ds_read_b128 a[36:39], %[ard] offset:41984\n\t" \
    "v_mfma_f32_16x16x32_bf16 v[40:43], a[0:3], a[68:71], v[40:43]\n\t" \
    "ds_read_b128 a[40:43], %[ard] offset:43008\n\t" \
    "v_mfma_f32_16x16x32_bf16 v[44:47], a[4:7], a[68:71], v[44:47]\n\t" \
    "ds_read_b128 a[44:47], %[ard] offset:44032\n\t" \
    "v_mfma_f32_16x16x32_bf16 v[48:51], a[8:11], a[68:71], v[48:51]\n\t" \
    "ds_read_b64_tr_b16 a[92:93], %[brd] offset:15616\n\t" \
    "v_mfma_f32_16x16x32_bf16 v[52:55], a[12:15], a[68:71], v[52:55]\n\t" \
    "ds_read_b64_tr_b16 a[94:95], %[brd] offset:16896\n\t" \
    "v_mfma_f32_16x16x32_bf16 v[56:59], a[0:3], a[72:75], v[56:59]\n\t" \
    "ds_read_b64_tr_b16 a[96:97], %[brd] offset:15872\n\t" \
    "v_mfma_f32_16x16x32_bf16 v[60:63], a[4:7], a[72:75], v[60:63]\n\t" \
    "ds_read_b64_tr_b16 a[98:99], %[brd] offset:17152\n\t" \
    "v_mfma_f32_16x16x32_bf16 v[64:67], a[8:11], a[72:75], v[64:67]\n\t" \
    "ds_read_b64_tr_b16 a[100:101], %[brd] offset:16128\n\t" \
    "v_mfma_f32_16x16x32_bf16 v[68:71], a[12:15], a[72:75], v[68:71]\n\t" \
    "ds_read_b64_tr_b16 a[102:103], %[brd] offset:17408\n\t" \
    "v_mfma_f32_16x16x32_bf16 v[72:75], a[0:3], a[76:79], v[72:75]\n\t" \
    "ds_read_b64_tr_b16 a[104:105], %[brd] offset:16384\n\t" \
    "v_mfma_f32_16x16x32_bf16 v[76:79], a[4:7], a[76:79], v[76:79]\n\t" \
    "ds_read_b64_tr_b16 a[106:107], %[brd] offset:17664\n\t" \
    "v_mfma_f32_16x16x32_bf16 v[80:83], a[8:11], a[76:79], v[80:83]\n\t" \
    "v_mfma_f32_16x16x32_bf16 v[84:87], a[12:15], a[76:79], v[84:87]\n\t" \
    "v_mfma_f32_16x16x32_bf16 v[88:91], a[0:3], a[80:83], v[88:91]\n\t" \
    "v_mfma_f32_16x16x32_bf16 v[92:95], a[4:7], a[80:83], v[92:95]\n\t" \
    "v_mfma_f32_16x16x32_bf16 v[96:99], a[8:11], a[80:83], v[96:99]\n\t" \
    "v_mfma_f32_16x16x32_bf16 v[100:103], a[12:15], a[80:83], v[100:103]\n\t" \
    "s_waitcnt lgkmcnt(0)\n\t" \
    "v_mfma_f32_16x16x32_bf16 v[24:27], a[32:35], a[88:91], v[24:27]\n\t" \
    "v_mfma_f32_16x16x32_bf16 v[28:31], a[36:39], a[88:91], v[28:31]\n\t" \
    "v_mfma_f32_16x16x32_bf16 v[32:35], a[40:43], a[88:91], v[32:35]\n\t" \
    "v_mfma_f32_16x16x32_bf16 v[36:39], a[44:47], a[88:91], v[36:39]\n\t" \
    "v_mfma_f32_16x16x32_bf16 v[40:43], a[32:35], a[92:95], v[40:43]\n\t" \
    "v_mfma_f32_16x16x32_bf16 v[44:47], a[36:39], a[92:95], v[44:47]\n\t" \
    "v_mfma_f32_16x16x32_bf16 v[48:51], a[40:43], a[92:95], v[48:51]\n\t" \
    "v_mfma_f32_16x16x32_bf16 v[52:55], a[44:47], a[92:95], v[52:55]\n\t" \
    "v_mfma_f32_16x16x32_bf16 v[56:59], a[32:35], a[96:99], v[56:59]\n\t" \
    "v_mfma_f32_16x16x32_bf16 v[60:63], a[36:39], a[96:99], v[60:63]\n\t" \
    "v_mfma_f32_16x16x32_bf16 v[64:67], a[40:43], a[96:99], v[64:67]\n\t" \
    "v_mfma_f32_16x16x32_bf16 v[68:71], a[44:47], a[96:99], v[68:71]\n\t" \
    "v_mfma_f32_16x16x32_bf16 v[72:75], a[32:35], a[100:103], v[72:75]\n\t" \
    "v_mfma_f32_16x16x32_bf16 v[76:79], a[36:39], a[100:103], v[76:79]\n\t" \
    "v_mfma_f32_16x16x32_bf16 v[80:83], a[40:43], a[100:103], v[80:83]\n\t" \
    "v_mfma_f32_16x16x32_bf16 v[84:87], a[44:47], a[100:103], v[84:87]\n\t" \
    "v_mfma_f32_16x16x32_bf16 v[88:91], a[32:35], a[104:107], v[88:91]\n\t" \
    "v_mfma_f32_16x16x32_bf16 v[92:95], a[36:39], a[104:107], v[92:95]\n\t" \
    "v_mfma_f32_16x16x32_bf16 v[96:99], a[40:43], a[104:107], v[96:99]\n\t" \
    "v_mfma_f32_16x16x32_bf16 v[100:103], a[44:47], a[104:107], v[100:103]\n\t" \
    "s_nop 7\n\t" \
    "s_mul_i32 s45, %[osc], 40\n\t" \
    "s_add_u32 s45, s45, %[ooff]\n\t" \
    "v_cvt_pk_bf16_f32 v232, v24, v25\n\t" \
    "v_cvt_pk_bf16_f32 v233, v26, v27\n\t" \
    "v_cvt_pk_bf16_f32 v234, v28, v29\n\t" \
    "v_cvt_pk_bf16_f32 v235, v30, v31\n\t" \
    "buffer_store_dwordx4 v[232:235], %[ov], %[srda], s45 offen sc1\n\t" \
    "s_add_u32 s45, s45, %[osi]\n\t" \
    "v_cvt_pk_bf16_f32 v236, v32, v33\n\t" \
    "v_cvt_pk_bf16_f32 v237, v34, v35\n\t" \
    "v_cvt_pk_bf16_f32 v238, v36, v37\n\t" \
    "v_cvt_pk_bf16_f32 v239, v38, v39\n\t" \
    "buffer_store_dwordx4 v[236:239], %[ov], %[srda], s45 offen sc1\n\t" \
    "s_mul_i32 s45, %[osc], 42\n\t" \
    "s_add_u32 s45, s45, %[ooff]\n\t" \
    "v_cvt_pk_bf16_f32 v240, v40, v41\n\t" \
    "v_cvt_pk_bf16_f32 v241, v42, v43\n\t" \
    "v_cvt_pk_bf16_f32 v242, v44, v45\n\t" \
    "v_cvt_pk_bf16_f32 v243, v46, v47\n\t" \
    "buffer_store_dwordx4 v[240:243], %[ov], %[srda], s45 offen sc1\n\t" \
    "s_add_u32 s45, s45, %[osi]\n\t" \
    "v_cvt_pk_bf16_f32 v244, v48, v49\n\t" \
    "v_cvt_pk_bf16_f32 v245, v50, v51\n\t" \
    "v_cvt_pk_bf16_f32 v246, v52, v53\n\t" \
    "v_cvt_pk_bf16_f32 v247, v54, v55\n\t" \
    "buffer_store_dwordx4 v[244:247], %[ov], %[srda], s45 offen sc1\n\t" \
    "s_mul_i32 s45, %[osc], 44\n\t" \
    "s_add_u32 s45, s45, %[ooff]\n\t" \
    "v_cvt_pk_bf16_f32 v248, v56, v57\n\t" \
    "v_cvt_pk_bf16_f32 v249, v58, v59\n\t" \
    "v_cvt_pk_bf16_f32 v250, v60, v61\n\t" \
    "v_cvt_pk_bf16_f32 v251, v62, v63\n\t" \
    "buffer_store_dwordx4 v[248:251], %[ov], %[srda], s45 offen sc1\n\t" \
    "s_add_u32 s45, s45, %[osi]\n\t" \
    "v_cvt_pk_bf16_f32 v252, v64, v65\n\t" \
    "v_cvt_pk_bf16_f32 v253, v66, v67\n\t" \
    "v_cvt_pk_bf16_f32 v254, v68, v69\n\t" \
    "v_cvt_pk_bf16_f32 v255, v70, v71\n\t" \
    "buffer_store_dwordx4 v[252:255], %[ov], %[srda], s45 offen sc1\n\t" \
    "s_mul_i32 s45, %[osc], 46\n\t" \
    "s_add_u32 s45, s45, %[ooff]\n\t" \
    "v_cvt_pk_bf16_f32 v232, v72, v73\n\t" \
    "v_cvt_pk_bf16_f32 v233, v74, v75\n\t" \
    "v_cvt_pk_bf16_f32 v234, v76, v77\n\t" \
    "v_cvt_pk_bf16_f32 v235, v78, v79\n\t" \
    "buffer_store_dwordx4 v[232:235], %[ov], %[srda], s45 offen sc1\n\t" \
    "s_add_u32 s45, s45, %[osi]\n\t" \
    "v_cvt_pk_bf16_f32 v236, v80, v81\n\t" \
    "v_cvt_pk_bf16_f32 v237, v82, v83\n\t" \
    "v_cvt_pk_bf16_f32 v238, v84, v85\n\t" \
    "v_cvt_pk_bf16_f32 v239, v86, v87\n\t" \
    "buffer_store_dwordx4 v[236:239], %[ov], %[srda], s45 offen sc1\n\t" \
    "s_mul_i32 s45, %[osc], 48\n\t" \
    "s_add_u32 s45, s45, %[ooff]\n\t" \
    "v_cvt_pk_bf16_f32 v240, v88, v89\n\t" \
    "v_cvt_pk_bf16_f32 v241, v90, v91\n\t" \
    "v_cvt_pk_bf16_f32 v242, v92, v93\n\t" \
    "v_cvt_pk_bf16_f32 v243, v94, v95\n\t" \
    "buffer_store_dwordx4 v[240:243], %[ov], %[srda], s45 offen sc1\n\t" \
    "s_add_u32 s45, s45, %[osi]\n\t" \
    "v_cvt_pk_bf16_f32 v244, v96, v97\n\t" \
    "v_cvt_pk_bf16_f32 v245, v98, v99\n\t" \
    "v_cvt_pk_bf16_f32 v246, v100, v101\n\t" \
    "v_cvt_pk_bf16_f32 v247, v102, v103\n\t" \
    "buffer_store_dwordx4 v[244:247], %[ov], %[srda], s45 offen sc1\n\t"
#define RK_CLOB_G "v24", "v25", "v26", "v27", "v28", "v29", "v30", "v31", "v32", "v33", "v34", "v35", "v36", "v37", "v38", "v39", "v40", "v41", "v42", "v43", "v44", "v45", "v46", "v47", "v48", "v49", "v50", "v51", "v52", "v53", "v54", "v55", "v56", "v57", "v58", "v59", "v60", "v61", "v62", "v63", "v64", "v65", "v66", "v67", "v68", "v69", "v70", "v71", "v72", "v73", "v74", "v75", "v76", "v77", "v78", "v79", "v80", "v81", "v82", "v83", "v84", "v85", "v86", "v87", "v88", "v89", "v90", "v91", "v92", "v93", "v94", "v95", "v96", "v97", "v98", "v99", "v100", "v101", "v102", "v103", "v104", "v105", "v106", "v107", "v108", "v109", "v110", "v111", "v112", "v113", "v114", "v115", "v116", "v117", "v118", "v119", "v120", "v121", "v122", "v123", "v124", "v125", "v126", "v127", "v128", "v129", "v130", "v131", "v132", "v133", "v134", "v135", "v136", "v137", "v138", "v139", "v140", "v141", "v142", "v143", "v144", "v145", "v146", "v147", "v148", "v149", "v150", "v151", "v152", "v153", "v154", "v155", "v156", "v157", "v158", "v159", "v160", "v161", "v162", "v163", "v164", "v165", "v166", "v167", "v168", "v169", "v170", "v171", "v172", "v173", "v174", "v175", "v176", "v177", "v178", "v179", "v180", "v181", "v182", "v183", "v184", "v185", "v186", "v187", "v188", "v189", "v190", "v191", "v192", "v193", "v194", "v195", "v196", "v197", "v198", "v199", "v200", "v201", "v202", "v203", "v204", "v205", "v206", "v207", "v208", "v209", "v210", "v211", "v212", "v213", "v214", "v215", "v232", "v233", "v234", "v235", "v236", "v237", "v238", "v239", "v240", "v241", "v242", "v243", "v244", "v245", "v246", "v247", "v248", "v249", "v250", "v251", "v252", "v253", "v254", "v255", "a0", "a1", "a2", "a3", "a4", "a5", "a6", "a7", "a8", "a9", "a10", "a11", "a12", "a13", "a14", "a15", "a16", "a17", "a18", "a19", "a20", "a21", "a22", "a23", "a24", "a25", "a26", "a27", "a28", "a29", "a30", "a31", "a32", "a33", "a34", "a35", "a36", "a37", "a38", "a39", "a40", "a41", "a42", "a43", "a44", "a45", "a46", "a47", "a48", "a49", "a50", "a51", "a52", "a53", "a54", "a55", "a56", "a57", "a58", "a59", "a60", "a61", "a62", "a63", "a64", "a65", "a66", "a67", "a68", "a69", "a70", "a71", "a72", "a73", "a74", "a75", "a76", "a77", "a78", "a79", "a80", "a81", "a82", "a83", "a84", "a85", "a86", "a87", "a88", "a89", "a90", "a91", "a92", "a93", "a94", "a95", "a96", "a97", "a98", "a99", "a100", "a101", "a102", "a103", "a104", "a105", "a106", "a107", "a108", "a109", "a110", "a111", "s40", "s41", "s42", "s43", "s44", "s45", "s46", "s47", "s48", "s49", "vcc", "scc", "memory"
#define RK_CLOB "v24", "v25", "v26", "v27", "v28", "v29", "v30", "v31", "v32", "v33", "v34", "v35", "v36", "v37", "v38", "v39", "v40", "v41", "v42", "v43", "v44", "v45", "v46", "v47", "v48", "v49", "v50", "v51", "v52", "v53", "v54", "v55", "v56", "v57", "v58", "v59", "v60", "v61", "v62", "v63", "v64", "v65", "v66", "v67", "v68", "v69", "v70", "v71", "v72", "v73", "v74", "v75", "v76", "v77", "v78", "v79", "v80", "v81", "v82", "v83", "v84", "v85", "v86", "v87", "v88", "v89", "v90", "v91", "v92", "v93", "v94", "v95", "v96", "v97", "v98", "v99", "v100", "v101", "v102", "v103", "v104", "v105", "v106", "v107", "v108", "v109", "v110", "v111", "v112", "v113", "v114", "v115", "v116", "v117", "v118", "v119", "v120", "v121", "v122", "v123", "v124", "v125", "v126", "v127", "v128", "v129", "v130", "v131", "v132", "v133", "v134", "v135", "v136", "v137", "v138", "v139", "v140", "v141", "v142", "v143", "v144", "v145", "v146", "v147", "v148", "v149", "v150", "v151", "v152", "v153", "v154", "v155", "v156", "v157", "v158", "v159", "v160", "v161", "v162", "v163", "v164", "v165", "v166", "v167", "v168", "v169", "v170", "v171", "v172", "v173", "v174", "v175", "v176", "v177", "v178", "v179", "v180", "v181", "v182", "v183", "v184", "v185", "v186", "v187", "v188", "v189", "v190", "v191", "v192", "v193", "v194", "v195", "v196", "v197", "v198", "v199", "v200", "v201", "v202", "v203", "v204", "v205", "v206", "v207", "v208", "v209", "v210", "v211", "v212", "v213", "v214", "v215", "v216", "v217", "v218", "v219", "v220", "v221", "v222", "v223", "v224", "v225", "v226", "v227", "v228", "v229", "v230", "v231", "v232", "v233", "v234", "v235", "v236", "v237", "v238", "v239", "v240", "v241", "v242", "v243", "v244", "v245", "v246", "v247", "v248", "v249", "v250", "v251", "v252", "v253", "v254", "v255", "a0", "a1", "a2", "a3", "a4", "a5", "a6", "a7", "a8", "a9", "a10", "a11", "a12", "a13", "a14", "a15", "a16", "a17", "a18", "a19", "a20", "a21", "a22", "a23", "a24", "a25", "a26", "a27", "a28", "a29", "a30", "a31", "a32", "a33", "a34", "a35", "a36", "a37", "a38", "a39", "a40", "a41", "a42", "a43", "a44", "a45", "a46", "a47", "a48", "a49", "a50", "a51", "a52", "a53", "a54", "a55", "a56", "a57", "a58", "a59", "a60", "a61", "a62", "a63", "a64", "a65", "a66", "a67", "a68", "a69", "a70", "a71", "a72", "a73", "a74", "a75", "a76", "a77", "a78", "a79", "a80", "a81", "a82", "a83", "a84", "a85", "a86", "a87", "a88", "a89", "a90", "a91", "a92", "a93", "a94", "a95", "a96", "a97", "a98", "a99", "a100", "a101", "a102", "a103", "a104", "a105", "a106", "a107", "a108", "a109", "a110", "a111", "a112", "a113", "a114", "a115", "a116", "a117", "a118", "a119", "a120", "a121", "a122", "a123", "a124", "a125", "a126", "a127", "a128", "a129", "a130", "a131", "a132", "a133", "a134", "a135", "a136", "a137", "a138", "a139", "a140", "a141", "a142", "a143", "a144", "a145", "a146", "a147", "a148", "a149", "a150", "a151", "a152", "a153", "a154", "a155", "a156", "a157", "a158", "a159", "a160", "a161", "a162", "a163", "a164", "a165", "a166", "a167", "a168", "a169", "a170", "a171", "a172", "a173", "a174", "a175", "a176", "a177", "a178", "a179", "a180", "a181", "a182", "a183", "a184", "a185", "a186", "a187", "a188", "a189", "a190", "a191", "a192", "a193", "a194", "a195", "a196", "a197", "a198", "a199", "a200", "a201", "a202", "a203", "a204", "a205", "a206", "a207", "a208", "a209", "a210", "a211", "a212", "a213", "a214", "a215", "a216", "a217", "a218", "a219", "a220", "a221", "a222", "a223", "a224", "a225", "a226", "a227", "a228", "a229", "a230", "a231", "a232", "a233", "a234", "a235", "a236", "a237", "a238", "a239", "a240", "a241", "a242", "a243", "a244", "a245", "a246", "a247", "a248", "a249", "a250", "a251", "a252", "a253", "a254", "a255", "s40", "s41", "s42", "s43", "s44", "s45", "s46", "s47", "s48", "s49", "s50", "s51", "s52", "s53", "s54", "s55", "s56", "s57", "s58", "s59", "s60", "s61", "s62", "s63", "s64", "s65", "s66", "s67", "s68", "s69", "s70", "s71", "s72", "s73", "s74", "s75", "s76", "s77", "s78", "s79", "s80", "s81", "s82", "s83", "s84", "s85", "s86", "s87", "s88", "s89", "s90", "s91", "s92", "s93", "s94", "s95", "vcc", "scc", "memory"

__device__ __forceinline__ v4i mk_srd(const void* p, long long bias, unsigned bytes) {
  const unsigned long long q = (unsigned long long)p + bias;
  v4i r; r.x = (int)(unsigned)q; r.y = (int)((unsigned)(q >> 32) & 0xFFFFu); r.z = (int)bytes; r.w = 0x00020000;
  return r;
}
__device__ __forceinline__ float gelu_tanh(float x) {
  const float u = 0.7978845608028654f * (x + 0.044715f * x * x * x);
  const float e = __expf(2.f * u);
  const float t = 1.f - 2.f / (e + 1.f);
  return 0.5f * x * (1.f + t);
}
#define RFL(x) __builtin_amdgcn_readfirstlane(x)
__device__ __forceinline__ int get_lane() { int l; asm volatile("v_mbcnt_lo_u32_b32 %0, -1, 0\n\tv_mbcnt_hi_u32_b32 %0, -1, %0" : "=v"(l)); return l; }
#define TID0 (w == 0 && get_lane() == 0)
__device__ __forceinline__ int vblock() {
  const int b = blockIdx.x;
  const int x = b & 7, r = b >> 3;
  if (x < 3) return x * 32 + r;
  if (x == 3) return r < 16 ? 96 + r : 112 + (16 + ((r - 16) >> 3)) * 8 + (r & 7);
  return 112 + (r >> 1) * 8 + 2 * (x - 4) + (r & 1);
}
__device__ __forceinline__ unsigned rt() { unsigned long long t; asm volatile("s_memrealtime %0\n\ts_waitcnt lgkmcnt(0)" : "=s"(t) :: "memory"); return (unsigned)t; }
#define TL(slot)

template <int HALF>
__device__ __forceinline__ void gemm_T(int Lb, int lane, int w, v4i srda, const u16* B, unsigned bbytes, int ldb, int col0, int aoff, int nk,
                                       int ooff, int os1, int os2, int os3, int ov, int tolds) {
  const v4i srdb = mk_srd(B, -4096, bbytes + 4096);
  int bo[6];
  const int ri = (lane >> 1) & 7, hf = lane & 1;
#pragma unroll
  for (int i = 0; i < 6; ++i) {
    const int u = 4 * i + (lane >> 4), kx = u / 6, ct = u % 6;
    const int row = 16 * (kx >> 1) + (ri & 3) + 8 * (ri >> 2) + 4 * (kx & 1);
    bo[i] = (row * ldb + 16 * ct + 8 * hf) * 2 + 4096 - (i & 3) * 1024;
  }
  const int g = lane >> 4;
  const int bm = Lb + 49152 + w * 24576;
  const int brd = bm + (g >> 1) * 3072 + (((lane & 15) >> 2) + 4 * (g & 1)) * 32 + (lane & 3) * 8;
  const int ard = Lb + lane * 16, asrc = (HALF ? (w >> 1) * 8192 + (w & 1) * 2048 : w * 4096) + lane * 16, wq = Lb + w * (HALF ? 2048 : 4096);
  const int boff = col0 * 2, bstep = 64 * ldb;
  if (HALF) asm volatile(RK_ASM_GTH
               :
               : [bo0] "v"(bo[0]), [bo1] "v"(bo[1]), [bo2] "v"(bo[2]), [bo3] "v"(bo[3]), [bo4] "v"(bo[4]), [bo5] "v"(bo[5]), [brd] "v"(brd),
                 [ard] "v"(ard), [asrc] "v"(asrc), [ov] "v"(ov), [srda] "s"(srda), [srdb] "s"(srdb), [wq] "s"(RFL(wq)), [bm] "s"(RFL(bm)),
                 [aoff] "s"(RFL(aoff)), [aend] "s"(RFL(aoff + nk * 8192)), [boff] "s"(RFL(boff)), [bend] "s"(RFL(boff + nk * bstep)),
                 [bstep] "s"(RFL(bstep)), [nk] "s"(RFL(nk)), [ooff] "s"(RFL(ooff)), [os1] "s"(RFL(os1)), [os2] "s"(RFL(os2)),
                 [os3] "s"(RFL(os3)), [tolds] "s"(RFL(tolds))
               : RK_CLOB_G);
  else asm volatile(RK_ASM_GT
               :
               : [bo0] "v"(bo[0]), [bo1] "v"(bo[1]), [bo2] "v"(bo[2]), [bo3] "v"(bo[3]), [bo4] "v"(bo[4]), [bo5] "v"(bo[5]), [brd] "v"(brd),
                 [ard] "v"(ard), [asrc] "v"(asrc), [ov] "v"(ov), [srda] "s"(srda), [srdb] "s"(srdb), [wq] "s"(RFL(wq)), [bm] "s"(RFL(bm)),
                 [aoff] "s"(RFL(aoff)), [aend] "s"(RFL(aoff + nk * 8192)), [boff] "s"(RFL(boff)), [bend] "s"(RFL(boff + nk * bstep)),
                 [bstep] "s"(RFL(bstep)), [nk] "s"(RFL(nk)), [ooff] "s"(RFL(ooff)), [os1] "s"(RFL(os1)), [os2] "s"(RFL(os2)),
                 [os3] "s"(RFL(os3)), [tolds] "s"(RFL(tolds))
               : RK_CLOB_G);
}
template <int ORDER>
__device__ __forceinline__ void gemm_U(int Lb, int lane, int w, v4i srda, const u16* Wo, int h, int ooff, int osi, int osc, int ov) {
  const v4i srdb = mk_srd(Wo, -4096, HE * D_ * 2 + 4096);
  int bo[5];
  const int ri = (lane >> 1) & 7, hf = lane & 1;
#pragma unroll
  for (int i = 0; i < 5; ++i) {
    const int u = 4 * i + (lane >> 4), kx = u / 5, ct = u % 5;
    const int row = 16 * (kx >> 1) + (ri & 3) + 8 * (ri >> 2) + 4 * (kx & 1);
    bo[i] = (row * D_ + 80 * w + 16 * ct + 8 * hf) * 2 + 4096 - (i & 3) * 1024;
  }
  const int g = lane >> 4;
  const int bm = Lb + 49152 + w * 24576;
  const int brd = bm + (g >> 1) * 2560 + (((lane & 15) >> 2) + 4 * (g & 1)) * 32 + (lane & 3) * 8;
  const int ard = Lb + lane * 16;
  const int boff = h * E_ * D_ * 2;
#define RK_U_OPS                                                                                                                           \
  : [bo0] "v"(bo[0]), [bo1] "v"(bo[1]), [bo2] "v"(bo[2]), [bo3] "v"(bo[3]), [bo4] "v"(bo[4]), [brd] "v"(brd), [ard] "v"(ard), [ov] "v"(ov), \
    [srda] "s"(srda), [srdb] "s"(srdb), [bm] "s"(RFL(bm)), [boff] "s"(RFL(boff)), [ooff] "s"(RFL(ooff)), [osi] "s"(RFL(osi)),              \
    [osc] "s"(RFL(osc))                                                                                                                    \
  : RK_CLOB_G
  if (ORDER == 0) asm volatile(RK_ASM_GU0 : RK_U_OPS);
  else if (ORDER == 1) asm volatile(RK_ASM_GU1 : RK_U_OPS);
  else asm volatile(RK_ASM_GUH : RK_U_OPS);
}

struct MegaArgs {
  unsigned char* ws;
  const u16 *x, *mem0, *Wq, *Wk, *Wv, *Wo, *w1, *b1, *w2, *b2;
  u16* out;
  int tva, tvb, pena, zlag, decay, pad;
};
typedef GAS unsigned gu32;
__device__ __forceinline__ unsigned ld_flag(unsigned char* ws, int off) { return __hip_atomic_load((gu32*)(ws + off), RLX_AGENT); }
__device__ __forceinline__ void signal(unsigned char* ws, int off, int w, unsigned inc = 1u) {
  asm volatile("s_waitcnt vmcnt(0)" ::: "memory");
  __syncthreads();
  if (TID0) __hip_atomic_fetch_add((gu32*)(ws + off), inc, RLX_AGENT);
}
__device__ __forceinline__ bool wait_ge(unsigned char* ws, int off, unsigned want, LAS unsigned* lf, int w) {
  __syncthreads();
  if (TID0) {
    unsigned ok = 1;
    for (unsigned spins = 0; ld_flag(ws, off) < want; ++spins) {
      __builtin_amdgcn_s_sleep(16);
      if (spins > (1u << 20)) { __hip_atomic_store((gu32*)(ws + C_ABORT), 1u, RLX_AGENT); ok = 0; break; }
      if ((spins & 63u) == 63u && ld_flag(ws, C_ABORT)) { ok = 0; break; }
    }
    *lf = ok;
  }
  __syncthreads();
  return *lf != 0;
}
__device__ __forceinline__ bool wait_rec(unsigned char* ws, int seg, int off2, unsigned want2, LAS unsigned* lf, int w) {
  if (TID0) {
    unsigned ok = 1;
    for (unsigned spins = 0;; ++spins) {
      const unsigned prog = ld_flag(ws, C_PROG), fin = ld_flag(ws, C_FINAL), c2 = off2 ? ld_flag(ws, off2) : want2;
      if ((((int)prog > seg) || fin >= (unsigned)(NS + NZ)) && c2 >= want2) break;
      __builtin_amdgcn_s_sleep(8);
      if (spins > (1u << 20)) { __hip_atomic_store((gu32*)(ws + C_ABORT), 1u, RLX_AGENT); ok = 0; break; }
      if ((spins & 63u) == 63u && ld_flag(ws, C_ABORT)) { ok = 0; break; }
    }
    *lf = ok;
  }
  __syncthreads();
  return *lf != 0;
}
#define LD16(off) __builtin_amdgcn_raw_buffer_load_b128(rs, (off), 0, 16)

template <int HALF>
__device__ __forceinline__ void unit_kvu(const MegaArgs& a, int tb, int hf, int h, int Lb, int lane, int w, v4i srda) {
  lane = get_lane();
  const int g = lane >> 4, col = lane & 15;
  const int isv = w >> 1, seg0 = tb * 4 + 2 * hf;
  const int ovk = (w & 1) * 3072 + lane * 16;
  const int ovv = Lb + (w & 1) * 24576 + ((col >> 2) & 1) * 1024 + g * 256 + (4 * (col >> 3) + (col & 3)) * 16;
  gemm_T<HALF>(Lb, lane, w, srda, isv ? a.Wv : a.Wk, D_ * HE * 2, HE, h * E_ + (w & 1) * 96, (int)OFF_XN + tb * 163840 + hf * 4096, 20,
               (int)OFF_KX + (seg0 * 32 + h) * 12288, 32 * 12288, 6144, 1024, isv ? ovv : ovk, isv);
  const int ov = (10 * w + (col >> 3)) * 20480 + g * 128 + (col & 7) * 16;
  gemm_U<HALF ? 2 : 0>(Lb, lane, w, srda, a.Wo, h, (int)OFF_G + seg0 * NZ * 20480 + h * 512, NZ * 20480, 20480, ov);
  signal(a.ws, C_KV + tb * 128, w, HALF ? 1u : 2u);
}
__device__ __forceinline__ void unit_wvo(const MegaArgs& a, int h, int db, LAS unsigned char* L, int Lb, int lane, int w, v4i srda) {
  lane = get_lane();
  const int g = lane >> 4, col = lane & 15;
  __syncthreads();
  const int dl = 16 * (col >> 3) + 4 * ((col >> 2) & 1) + (col & 3);
#pragma unroll
  for (int j = 0; j < 12; ++j) {
    const int pc = w * 12 + j, ks = pc >> 3, rt = pc & 7;
    const u16* src = a.Wv + (size_t)(128 * db + 32 * (rt >> 1) + 8 * (rt & 1) + dl) * HE + h * E_ + 32 * ks + 8 * g;
    __builtin_amdgcn_global_load_lds((const GAS unsigned*)src, (LAS unsigned*)(L + pc * 1024), 16, 0, 0);
  }
  const int ov = (10 * w + (col >> 3)) * 327680 + g * 256 + (col & 7) * 16;
  gemm_U<1>(Lb, lane, w, srda, a.Wo, h, (int)OFF_WVO + ((h >> 3) * 40 + ((h >> 1) & 3) * 10) * 2048 + (h & 1) * 128 + 4 * db * 1024, 1024, 327680, ov);
  signal(a.ws, C_W, w);
}
__device__ __forceinline__ void unit_t(const MegaArgs& a, int kind, int tb, int idx, int Lb, int lane, int w, v4i srda) {
  lane = get_lane();
  const int g = lane >> 4;
  const u16* B; unsigned bb; int ldb, col0, aoff, nk, ooff, os1, os2, os3, ov;
  if (kind == 0) {
    const int h = idx + 8 * (w >> 1);
    B = a.Wq; bb = D_ * HE * 2; ldb = HE; col0 = h * E_ + (w & 1) * 96; aoff = (int)OFF_XN + tb * 163840; nk = 20;
    ooff = (int)OFF_QX + (tb * 4 * 32 + h) * 12288; os1 = 32 * 12288; os2 = 6144; os3 = 1024; ov = (w & 1) * 3072 + lane * 16;
  } else if (kind == 1) {
    B = a.w1; bb = D_ * F_ * 2; ldb = F_; col0 = idx * 384 + w * 96; aoff = (int)OFF_XN + tb * 163840; nk = 20;
    ooff = (int)OFF_HID + tb * (48 * 8192) + (idx * 12 + w * 3) * 8192; os1 = 2048; os2 = 1024; os3 = 8192; ov = lane * 16;
  } else {
    B = a.w2; bb = F_ * D_ * 2; ldb = D_; col0 = idx * 256 + w * 96; aoff = (int)OFF_HID + tb * (48 * 8192); nk = 48;
    ooff = (int)OFF_Y + tb * 163840 + (col0 >> 5) * 8192; os1 = 2048; os2 = 1024; os3 = 8192; ov = lane * 16;
  }
  gemm_T<0>(Lb, lane, w, srda, B, bb, ldb, col0, aoff, nk, ooff, os1, os2, os3, ov, 0);
  if (kind == 1) {
    asm volatile("s_waitcnt vmcnt(0)" ::: "memory");
    const __amdgpu_buffer_rsrc_t rs = __builtin_amdgcn_make_buffer_rsrc(a.ws, 0, (int)WS_END, 0x00020000);
    v4u xv[24];
#pragma unroll
    for (int i = 0; i < 24; ++i) xv[i] = LD16(ooff + i * 1024 + lane * 16);
    __builtin_amdgcn_sched_barrier(0);
#pragma unroll
    for (int pr = 0; pr < 3; ++pr) {
      const v4u bv = *(const v4u*)(a.b1 + (idx * 12 + w * 3 + pr) * 32 + 8 * g);
      float bf[8];
#pragma unroll
      for (int i = 0; i < 4; ++i) { bf[2 * i] = bf2f(bv[i] & 0xFFFF); bf[2 * i + 1] = bf2f(bv[i] >> 16); }
#pragma unroll
      for (int rt = 0; rt < 8; ++rt) {
        const v4u x = xv[pr * 8 + rt];
        v4u o;
#pragma unroll
        for (int i = 0; i < 4; ++i) o[i] = pk2(gelu_tanh(bf2f(x[i] & 0xFFFF) + bf[2 * i]), gelu_tanh(bf2f(x[i] >> 16) + bf[2 * i + 1]));
        __builtin_amdgcn_raw_buffer_store_b128(o, rs, ooff + (pr * 8 + rt) * 1024 + lane * 16, 0, 16);
      }
    }
  }
  signal(a.ws, (kind == 0 ? C_Q : kind == 1 ? C_F1 : C_F2) + tb * 128, w);
}

__device__ __forceinline__ bool unit_px(const MegaArgs& a, int seg, int hq, int lane, int w, bool ready, LAS unsigned* lf) {
  lane = get_lane();
  const __amdgpu_buffer_rsrc_t rs = __builtin_amdgcn_make_buffer_rsrc(a.ws, 0, (int)WS_END, 0x00020000);
  const int h = 4 * hq + w, r16 = lane & 15, kq = lane >> 4;
  const int qo = (int)OFF_QX + (seg * 32 + h) * 12288 + lane * 16, ko = (int)OFF_KX + (seg * 32 + h) * 12288 + lane * 16;
  const int mo = (int)OFF_KM + (((seg * H_ + h) * 8 + (r16 & 7)) * E_ + 8 * kq) * 2;
  v4u q0[6], q1[6], k0[6], k1[6], k2[6];
#pragma unroll
  for (int ks = 0; ks < 6; ++ks) {
    q0[ks] = LD16(qo + ks * 1024); q1[ks] = LD16(qo + 6144 + ks * 1024);
    k0[ks] = LD16(ko + ks * 1024); k1[ks] = LD16(ko + 6144 + ks * 1024);
  }
  __builtin_amdgcn_sched_barrier(0);
  if (!ready && !wait_rec(a.ws, seg, 0, 0u, lf, w)) return false;
#pragma unroll
  for (int ks = 0; ks < 6; ++ks) k2[ks] = LD16(mo + ks * 64);
  __builtin_amdgcn_sched_barrier(0);
  f32x4 s[2][3];
#pragma unroll
  for (int qt = 0; qt < 2; ++qt)
#pragma unroll
    for (int kt = 0; kt < 3; ++kt) s[qt][kt] = f32x4{0.f, 0.f, 0.f, 0.f};
#define BC(x) __builtin_bit_cast(bf16x8, x)
#pragma unroll
  for (int ks = 0; ks < 6; ++ks) {
    s[0][0] = __builtin_amdgcn_mfma_f32_16x16x32_bf16(BC(k0[ks]), BC(q0[ks]), s[0][0], 0, 0, 0);
    s[1][0] = __builtin_amdgcn_mfma_f32_16x16x32_bf16(BC(k0[ks]), BC(q1[ks]), s[1][0], 0, 0, 0);
    s[1][1] = __builtin_amdgcn_mfma_f32_16x16x32_bf16(BC(k1[ks]), BC(q1[ks]), s[1][1], 0, 0, 0);
  }
#pragma unroll
  for (int ks = 0; ks < 6; ++ks) {
    s[0][2] = __builtin_amdgcn_mfma_f32_16x16x32_bf16(BC(k2[ks]), BC(q0[ks]), s[0][2], 0, 0, 0);
    s[1][2] = __builtin_amdgcn_mfma_f32_16x16x32_bf16(BC(k2[ks]), BC(q1[ks]), s[1][2], 0, 0, 0);
  }
  const float sc2 = 0.07216878364870322f * 1.4426950408889634f;
  const float NEG = -__builtin_inff();
#pragma unroll
  for (int qt = 0; qt < 2; ++qt) {
    const int qi = qt * 16 + r16;
    float v[12];
#pragma unroll
    for (int kt = 0; kt < 3; ++kt)
#pragma unroll
      for (int r = 0; r < 4; ++r) {
        const int key = kt * 16 + 4 * kq + r;
        const bool valid = (kt < 2) ? (key <= qi) : (kq < 2);
        v[kt * 4 + r] = valid ? s[qt][kt][r] * sc2 : NEG;
      }
    float mx = v[0];
#pragma unroll
    for (int i = 1; i < 12; ++i) mx = fmaxf(mx, v[i]);
    mx = fmaxf(mx, __shfl_xor(mx, 16)); mx = fmaxf(mx, __shfl_xor(mx, 32));
    float sum = 0.f;
#pragma unroll
    for (int i = 0; i < 12; ++i) { v[i] = __builtin_amdgcn_exp2f(v[i] - mx); sum += v[i]; }
    sum += __shfl_xor(sum, 16); sum += __shfl_xor(sum, 32);
    const float inv = 1.f / sum;
    unsigned a0 = pk2(v[0] * inv, v[1] * inv), a1 = pk2(v[2] * inv, v[3] * inv), b0 = pk2(v[4] * inv, v[5] * inv), b1 = pk2(v[6] * inv, v[7] * inv);
    { auto r = __builtin_amdgcn_permlane32_swap(a0, b0, false, false); auto r2 = __builtin_amdgcn_permlane16_swap(r[0], r[1], false, false); a0 = r2[0]; b0 = r2[1]; }
    { auto r = __builtin_amdgcn_permlane32_swap(a1, b1, false, false); auto r2 = __builtin_amdgcn_permlane16_swap(r[0], r[1], false, false); a1 = r2[0]; b1 = r2[1]; }
    const int pb = (int)OFF_P + (seg * 2 + qt) * 40960;
    __builtin_amdgcn_raw_buffer_store_b128(v4u{a0, a1, b0, b1}, rs, pb + h * 1024 + lane * 16, 0, 16);
    if (kq < 2)
      __builtin_amdgcn_raw_buffer_store_b64(v2u{pk2(v[8] * inv, v[9] * inv), pk2(v[10] * inv, v[11] * inv)}, rs,
                                            pb + (32 + (h >> 2)) * 1024 + ((h & 3) * 16 + r16) * 16 + kq * 8, 0, 16);
  }
  signal(a.ws, C_P + seg * 128, w);
  return true;
}

__device__ __forceinline__ bool unit_fin(const MegaArgs& a, int seg, int cq, LAS unsigned char* L, int lane, int w, bool ready, LAS unsigned* lf) {
  lane = get_lane();
  const __amdgpu_buffer_rsrc_t rs = __builtin_amdgcn_make_buffer_rsrc(a.ws, 0, (int)WS_END, 0x00020000);
  const int r16 = lane & 15, kq = lane >> 4;
  const int po = (int)OFF_P + seg * 2 * 40960 + lane * 16;
  const int go = (int)OFF_G + (seg * NZ + cq * 8 + (r16 >> 3)) * 20480 + kq * 128 + (r16 & 7) * 16;
  v4u pf[2][10], gf[4][10];
#pragma unroll
  for (int ks = 0; ks < 8; ++ks)
#pragma unroll
    for (int ct = 0; ct < 4; ++ct) gf[ct][ks] = LD16(go + ct * 40960 + (8 * w + ks) * 512);
  v2u yv[2], xv[2];
  const int c = cq * 64 + w * 16 + 4 * kq;
#pragma unroll
  for (int rt = 0; rt < 2; ++rt) {
    const int row = seg * 32 + rt * 16 + r16;
    yv[rt] = __builtin_amdgcn_raw_buffer_load_b64(rs, (int)OFF_Y + (row >> 7) * 163840 + (c >> 5) * 8192 + ((row >> 4) & 7) * 1024 + (((c >> 3) & 3) * 16 + (row & 15)) * 16 + (c & 7) * 2, 0, 16);
    xv[rt] = *(const v2u*)(a.x + (size_t)row * D_ + c);
  }
  const v2u bb = *(const v2u*)(a.b2 + c);
  __builtin_amdgcn_sched_barrier(0);
  if (!ready && !wait_rec(a.ws, seg, C_P + seg * 128, 8u, lf, w)) return false;
#pragma unroll
  for (int ks = 0; ks < 10; ++ks) {
    const int kk = ks < 8 ? 8 * w + ks : 32 + 2 * w + (ks - 8);
    pf[0][ks] = LD16(po + kk * 1024); pf[1][ks] = LD16(po + 40960 + kk * 1024);
    if (ks >= 8) {
#pragma unroll
      for (int ct = 0; ct < 4; ++ct) gf[ct][ks] = LD16(go + ct * 40960 + kk * 512);
    }
  }
  __builtin_amdgcn_sched_barrier(0);
  f32x4 acc[2][4];
#pragma unroll
  for (int i = 0; i < 2; ++i)
#pragma unroll
    for (int j = 0; j < 4; ++j) acc[i][j] = f32x4{0.f, 0.f, 0.f, 0.f};
#pragma unroll
  for (int ks = 0; ks < 10; ++ks)
#pragma unroll
    for (int rt = 0; rt < 2; ++rt)
#pragma unroll
      for (int ct = 0; ct < 4; ++ct)
        acc[rt][ct] = __builtin_amdgcn_mfma_f32_16x16x32_bf16(BC(gf[ct][ks]), BC(pf[rt][ks]), acc[rt][ct], 0, 0, 0);
  __syncthreads();
#pragma unroll
  for (int rt = 0; rt < 2; ++rt)
#pragma unroll
    for (int ct = 0; ct < 4; ++ct) *(LAS f32x4*)(L + ((w * 8 + rt * 4 + ct) * 64 + lane) * 16) = acc[rt][ct];
  __syncthreads();
#pragma unroll
  for (int rt = 0; rt < 2; ++rt) {
    f32x4 sv = *(LAS f32x4*)(L + ((rt * 4 + w) * 64 + lane) * 16);
#pragma unroll
    for (int ww = 1; ww < 4; ++ww) sv += *(LAS f32x4*)(L + ((ww * 8 + rt * 4 + w) * 64 + lane) * 16);
    const int row = seg * 32 + rt * 16 + r16;
    f32x4 gt = {bf2f(yv[rt].x & 0xFFFF) + bf2f(bb.x & 0xFFFF), bf2f(yv[rt].x >> 16) + bf2f(bb.x >> 16), bf2f(yv[rt].y & 0xFFFF) + bf2f(bb.y & 0xFFFF), bf2f(yv[rt].y >> 16) + bf2f(bb.y >> 16)};
#pragma unroll
    for (int i = 0; i < 4; ++i) gt[i] = gt[i] / (1.f + __expf(-gt[i]));
    const float o0 = bf2f(xv[rt].x & 0xFFFF) + sv[0] * gt[0], o1 = bf2f(xv[rt].x >> 16) + sv[1] * gt[1];
    const float o2 = bf2f(xv[rt].y & 0xFFFF) + sv[2] * gt[2], o3 = bf2f(xv[rt].y >> 16) + sv[3] * gt[3];
    *(v2u*)(a.out + (size_t)row * D_ + c) = v2u{pk2(o0, o1), pk2(o2, o3)};
  }
  return true;
}

constexpr int NBULK = 126;
struct BulkList { unsigned short e[NBULK]; };
constexpr BulkList make_bulk() {
  BulkList l{}; int n = 0;
  auto put = [&](int t, int x, int y) { l.e[n++] = (unsigned short)(t << 8 | x << 3 | y); };
  for (int q = 0; q < 8; ++q) put(5, 0, q);
  for (int i = 0; i < 10; ++i) put(2, i, 0);
  for (int q = 0; q < 8; ++q) put(5, 1, q);
  for (int tb = 2; tb < 4; ++tb) for (int q = 0; q < 4; ++q) put(0, tb, q);
  for (int c = 0; c < 4; ++c) put(3, 0, c);
  for (int k = 0; k < 16; ++k) {
    if (k + 4 < 16) for (int q = 0; q < 4; ++q) put(0, k + 4, q);
    put(1, k, 0); put(1, k, 1);
    if (k == 3) { put(4, 0, 0); put(4, 0, 1); }
    if (k == 4) for (int c = 0; c < 4; ++c) put(3, 1, c);
    if (k == 8) { put(4, 1, 0); put(4, 1, 1); }
  }
  return l;
}
__device__ const BulkList kBulk = make_bulk();

typedef const __attribute__((address_space(4))) MegaArgs* KargPtr;
__device__ __forceinline__ MegaArgs kargs() {
  KargPtr p = (KargPtr)__builtin_amdgcn_kernarg_segment_ptr();
  asm volatile("" : "+s"(p));
  MegaArgs r;
  r.ws = p->ws; r.x = p->x; r.mem0 = p->mem0; r.Wq = p->Wq; r.Wk = p->Wk; r.Wv = p->Wv; r.Wo = p->Wo; r.w1 = p->w1; r.b1 = p->b1; r.w2 = p->w2;
  r.b2 = p->b2; r.out = p->out; r.tva = p->tva; r.tvb = p->tvb; r.pena = p->pena; r.zlag = p->zlag; r.decay = p->decay; r.pad = 0;
  return r;
}
__device__ __forceinline__ void worker(LAS unsigned char* L, int Lb, int lane, int w) {
  const int b = vblock(), j = b - (NS + NZ);
  int x = b & 7;
  LAS unsigned* lf = (LAS unsigned*)(L + 147456);
  int ti = j % 18;
  int tseg = j / 18;
  bool bulk_left = true;
  for (;;) {
    const MegaArgs a = kargs();
    asm volatile("" : "+s"(w), "+s"(x), "+s"(ti), "+s"(Lb));
    unsigned char* ws = a.ws;
    const v4i srda = mk_srd(ws, 0, (unsigned)WS_END);
    __syncthreads();
    if (TID0) {
      unsigned act = 2, code = 0;
      for (unsigned spins = 0;; ++spins) {
        if (tseg < NSEG) {
          const unsigned prog = ld_flag(ws, C_PROG), fin = ld_flag(ws, C_FINAL);
          const unsigned c1 = ld_flag(ws, (ti < 8 ? C_Q : C_F2) + (tseg >> 2) * 128);
          const unsigned c2 = ld_flag(ws, C_P + tseg * 128);
          const bool rec = ((int)prog > tseg) || fin >= (unsigned)(NS + NZ);
          const bool stat = ti < 8 ? c1 >= 16u : c1 >= 2u;
          if (rec && stat && (ti < 8 || c2 >= 8u)) { act = 1; break; }
          if (!bulk_left && stat && ld_flag(ws, C_KV + (tseg >> 2) * 128) >= 64u) { act = 3; break; }
        }
        if (bulk_left) {
          const unsigned n = __hip_atomic_fetch_add((gu32*)(ws + C_QUEUE + x * 128), 1u, RLX_AGENT);
          if (n < (unsigned)NBULK) { act = 0; code = kBulk.e[n]; break; }
          bulk_left = false;
        }
        if (tseg >= NSEG) break;
        __builtin_amdgcn_s_sleep(32);
        if (spins > (1u << 19)) { __hip_atomic_store((gu32*)(ws + C_ABORT), 1u, RLX_AGENT); break; }
        if ((spins & 15u) == 15u && ld_flag(ws, C_ABORT)) break;
      }
      lf[0] = act; lf[1] = code;
    }
    __syncthreads();
    const int act = RFL(lf[0]), code = RFL(lf[1]);
    if (act == 2) return;
    if (act & 1) {
      const bool ok = (ti < 8) ? unit_px(a, tseg, ti, lane, w, act == 1, lf + 2) : unit_fin(a, tseg, ti - 8, L, lane, w, act == 1, lf + 2);
      if (!ok) return;
      tseg += 8;
    } else {
      const int type = code >> 8, p = (code >> 3) & 31, q = code & 7;
      if (type == 0) unit_kvu<0>(a, p, 0, x + 8 * q, Lb, lane, w, srda);
      else if (type == 5) unit_kvu<1>(a, p, q >> 2, x + 8 * (q & 3), Lb, lane, w, srda);
      else if (type == 2) unit_wvo(a, x + 8 * (2 + p / 5), p % 5, L, Lb, lane, w, srda);
      else {
        if (type == 4) {
          if (!wait_ge(ws, C_F1 + (x + 8 * p) * 128, 4u, lf + 2, w)) return;
          if (TID0) { __builtin_amdgcn_fence(__ATOMIC_ACQUIRE, "agent"); asm volatile("s_waitcnt vmcnt(0)" ::: "memory"); }
          __syncthreads();
        }
        unit_t(a, type == 1 ? 0 : type - 2, type == 1 ? p : x + 8 * p, type == 1 ? x + 16 * q : q, Lb, lane, w, srda);
      }
    }
  }
}

constexpr int L_QK = 0;
constexpr int L_FLAG = 7680;
constexpr int L_PART = 8192;
constexpr int L_W = L_PART + 12288;
constexpr int L_BYTES = L_W + 4 * 16 * 2048;
static_assert(L_BYTES <= 160 * 1024, "lds");

#define RK_ASM_S \
    "s_mov_b32 s40, 0\n\t" \
    "s_mov_b32 s41, 0\n\t" \
    "s_mov_b32 s42, 0\n\t" \
    "s_mov_b32 s43, 0\n\t" \
    "s_mov_b32 s44, 124\n\t" \
    "s_mov_b32 s45, 88\n\t" \
    "s_mov_b32 s63, 0\n\t" \
    "s_mov_b32 s64, 0\n\t" \
    "s_mov_b32 s65, 0\n\t" \
    "s_mov_b32 s46, %[offm]\n\t" \
    "s_add_u32 s47, s46, 4096\n\t" \
    "s_add_u32 s48, s46, 8192\n\t" \
    "s_mov_b32 s49, 0\n\t" \
    "s_mov_b32 s61, 1\n\t" \
    "s_nop 4\n\t" \
    "s_add_u32 m0, %[lwm], 0\n\t" \
    "s_mov_b32 s60, 0\n\t" \
    "buffer_load_dwordx4 %[wsrc], %[srdk], s60 offen lds\n\t" \
    "buffer_load_dwordx4 %[wsrc2], %[srdk], s60 offen offset:1024 lds\n\t" \
    "s_add_u32 m0, %[lwm], 2048\n\t" \
    "s_mov_b32 s60, 128\n\t" \
    "buffer_load_dwordx4 %[wsrc], %[srdk], s60 offen lds\n\t" \
    "buffer_load_dwordx4 %[wsrc2], %[srdk], s60 offen offset:1024 lds\n\t" \
    "s_add_u32 m0, %[lwm], 4096\n\t" \
    "s_mov_b32 s60, 256\n\t" \
    "buffer_load_dwordx4 %[wsrc], %[srdq], s60 offen lds\n\t" \
    "buffer_load_dwordx4 %[wsrc2], %[srdq], s60 offen offset:1024 lds\n\t" \
    "s_add_u32 m0, %[lwm], 6144\n\t" \
    "s_mov_b32 s60, 786432\n\t" \
    "buffer_load_dwordx4 %[wsrc], %[srdq], s60 offen lds\n\t" \
    "buffer_load_dwordx4 %[wsrc2], %[srdq], s60 offen offset:1024 lds\n\t" \
    "s_add_u32 m0, %[lwm], 8192\n\t" \
    "s_mov_b32 s60, 786432\n\t" \
    "buffer_load_dwordx4 %[wsrc], %[srdk], s60 offen lds\n\t" \
    "buffer_load_dwordx4 %[wsrc2], %[srdk], s60 offen offset:1024 lds\n\t" \
    "s_add_u32 m0, %[lwm], 10240\n\t" \
    "s_mov_b32 s60, 786560\n\t" \
    "buffer_load_dwordx4 %[wsrc], %[srdk], s60 offen lds\n\t" \
    "buffer_load_dwordx4 %[wsrc2], %[srdk], s60 offen offset:1024 lds\n\t" \
    "s_add_u32 m0, %[lwm], 12288\n\t" \
    "s_mov_b32 s60, 786688\n\t" \
    "buffer_load_dwordx4 %[wsrc], %[srdq], s60 offen lds\n\t" \
    "buffer_load_dwordx4 %[wsrc2], %[srdq], s60 offen offset:1024 lds\n\t" \
    "s_add_u32 m0, %[lwm], 14336\n\t" \
    "s_mov_b32 s60, 1572864\n\t" \
    "buffer_load_dwordx4 %[wsrc], %[srdq], s60 offen lds\n\t" \
    "buffer_load_dwordx4 %[wsrc2], %[srdq], s60 offen offset:1024 lds\n\t" \
    "s_add_u32 m0, %[lwm], 16384\n\t" \
    "s_mov_b32 s60, 1572992\n\t" \
    "buffer_load_dwordx4 %[wsrc], %[srdq], s60 offen lds\n\t" \
    "buffer_load_dwordx4 %[wsrc2], %[srdq], s60 offen offset:1024 lds\n\t" \
    "s_add_u32 m0, %[lwm], 18432\n\t" \
    "s_mov_b32 s60, 1572992\n\t" \
    "buffer_load_dwordx4 %[wsrc], %[srdk], s60 offen lds\n\t" \
    "buffer_load_dwordx4 %[wsrc2], %[srdk], s60 offen offset:1024 lds\n\t" \
    "s_add_u32 m0, %[lwm], 20480\n\t" \
    "s_mov_b32 s60, 1573120\n\t" \
    "buffer_load_dwordx4 %[wsrc], %[srdk], s60 offen lds\n\t" \
    "buffer_load_dwordx4 %[wsrc2], %[srdk], s60 offen offset:1024 lds\n\t" \
    "s_add_u32 m0, %[lwm], 22528\n\t" \
    "s_mov_b32 s60, 2359296\n\t" \
    "buffer_load_dwordx4 %[wsrc], %[srdq], s60 offen lds\n\t" \
    "buffer_load_dwordx4 %[wsrc2], %[srdq], s60 offen offset:1024 lds\n\t" \
    "s_add_u32 m0, %[lwm], 24576\n\t" \
    "s_mov_b32 s60, 2359424\n\t" \
    "buffer_load_dwordx4 %[wsrc], %[srdq], s60 offen lds\n\t" \
    "buffer_load_dwordx4 %[wsrc2], %[srdq], s60 offen offset:1024 lds\n\t" \
    "s_add_u32 m0, %[lwm], 26624\n\t" \
    "s_mov_b32 s60, 2359424\n\t" \
    "buffer_load_dwordx4 %[wsrc], %[srdk], s60 offen lds\n\t" \
    "buffer_load_dwordx4 %[wsrc2], %[srdk], s60 offen offset:1024 lds\n\t" \
    "s_add_u32 m0, %[lwm], 28672\n\t" \
    "s_mov_b32 s60, 2359552\n\t" \
    "buffer_load_dwordx4 %[wsrc], %[srdk], s60 offen lds\n\t" \
    "buffer_load_dwordx4 %[wsrc2], %[srdk], s60 offen offset:1024 lds\n\t" \
    "s_add_u32 m0, %[lwm], 30720\n\t" \
    "s_mov_b32 s60, 3145728\n\t" \
    "buffer_load_dwordx4 %[wsrc], %[srdk], s60 offen lds\n\t" \
    "buffer_load_dwordx4 %[wsrc2], %[srdk], s60 offen offset:1024 lds\n\t" \
    "s_waitcnt vmcnt(16)\n\t" \
    "ds_read_b64_tr_b16 a[0:1], %[trd] offset:0\n\t" \
    "ds_read_b64_tr_b16 a[2:3], %[trd] offset:64\n\t" \
    "ds_read_b64_tr_b16 a[4:5], %[trd] offset:1024\n\t" \
    "ds_read_b64_tr_b16 a[6:7], %[trd] offset:1088\n\t" \
    "ds_read_b64_tr_b16 a[8:9], %[trd] offset:2048\n\t" \
    "ds_read_b64_tr_b16 a[10:11], %[trd] offset:2112\n\t" \
    "ds_read_b64_tr_b16 a[12:13], %[trd] offset:3072\n\t" \
    "ds_read_b64_tr_b16 a[14:15], %[trd] offset:3136\n\t" \
    "ds_read_b64_tr_b16 a[16:17], %[trd] offset:4096\n\t" \
    "ds_read_b64_tr_b16 a[18:19], %[trd] offset:4160\n\t" \
    "ds_read_b64_tr_b16 a[20:21], %[trd] offset:5120\n\t" \
    "ds_read_b64_tr_b16 a[22:23], %[trd] offset:5184\n\t" \
    "ds_read_b64_tr_b16 a[24:25], %[trd] offset:6144\n\t" \
    "ds_read_b64_tr_b16 a[26:27], %[trd] offset:6208\n\t" \
    "ds_read_b64_tr_b16 a[28:29], %[trd] offset:7168\n\t" \
    "ds_read_b64_tr_b16 a[30:31], %[trd] offset:7232\n\t" \
    "ds_read_b64_tr_b16 a[32:33], %[trd] offset:8192\n\t" \
    "ds_read_b64_tr_b16 a[34:35], %[trd] offset:8256\n\t" \
    "ds_read_b64_tr_b16 a[36:37], %[trd] offset:9216\n\t" \
    "ds_read_b64_tr_b16 a[38:39], %[trd] offset:9280\n\t" \
    "ds_read_b64_tr_b16 a[40:41], %[trd] offset:10240\n\t" \
    "ds_read_b64_tr_b16 a[42:43], %[trd] offset:10304\n\t" \
    "ds_read_b64_tr_b16 a[44:45], %[trd] offset:11264\n\t" \
    "ds_read_b64_tr_b16 a[46:47], %[trd] offset:11328\n\t" \
    "ds_read_b64_tr_b16 a[48:49], %[trd] offset:12288\n\t" \
    "ds_read_b64_tr_b16 a[50:51], %[trd] offset:12352\n\t" \
    "ds_read_b64_tr_b16 a[52:53], %[trd] offset:13312\n\t" \
    "ds_read_b64_tr_b16 a[54:55], %[trd] offset:13376\n\t" \
    "ds_read_b64_tr_b16 a[56:57], %[trd] offset:14336\n\t" \
    "ds_read_b64_tr_b16 a[58:59], %[trd] offset:14400\n\t" \
    "ds_read_b64_tr_b16 a[60:61], %[trd] offset:15360\n\t" \
    "ds_read_b64_tr_b16 a[62:63], %[trd] offset:15424\n\t" \
    "s_waitcnt lgkmcnt(0)\n\t" \
    "s_add_u32 m0, %[lwm], 0\n\t" \
    "s_mov_b32 s60, 3145856\n\t" \
    "buffer_load_dwordx4 %[wsrc], %[srdq], s60 offen lds\n\t" \
    "buffer_load_dwordx4 %[wsrc2], %[srdq], s60 offen offset:1024 lds\n\t" \
    "s_add_u32 m0, %[lwm], 2048\n\t" \
    "s_mov_b32 s60, 3145984\n\t" \
    "buffer_load_dwordx4 %[wsrc], %[srdq], s60 offen lds\n\t" \
    "buffer_load_dwordx4 %[wsrc2], %[srdq], s60 offen offset:1024 lds\n\t" \
    "s_add_u32 m0, %[lwm], 4096\n\t" \
    "s_mov_b32 s60, 3145984\n\t" \
    "buffer_load_dwordx4 %[wsrc], %[srdk], s60 offen lds\n\t" \
    "buffer_load_dwordx4 %[wsrc2], %[srdk], s60 offen offset:1024 lds\n\t" \
    "s_add_u32 m0, %[lwm], 6144\n\t" \
    "s_mov_b32 s60, 4718592\n\t" \
    "buffer_load_dwordx4 %[wsrc], %[srdq], s60 offen lds\n\t" \
    "buffer_load_dwordx4 %[wsrc2], %[srdq], s60 offen offset:1024 lds\n\t" \
    "s_add_u32 m0, %[lwm], 8192\n\t" \
    "s_mov_b32 s60, 5505024\n\t" \
    "buffer_load_dwordx4 %[wsrc], %[srdq], s60 offen lds\n\t" \
    "buffer_load_dwordx4 %[wsrc2], %[srdq], s60 offen offset:1024 lds\n\t" \
    "s_add_u32 m0, %[lwm], 10240\n\t" \
    "s_mov_b32 s60, 7077888\n\t" \
    "buffer_load_dwordx4 %[wsrc], %[srdq], s60 offen lds\n\t" \
    "buffer_load_dwordx4 %[wsrc2], %[srdq], s60 offen offset:1024 lds\n\t" \
    "s_add_u32 m0, %[lwm], 12288\n\t" \
    "s_mov_b32 s60, 4718592\n\t" \
    "buffer_load_dwordx4 %[wsrc], %[srdk], s60 offen lds\n\t" \
    "buffer_load_dwordx4 %[wsrc2], %[srdk], s60 offen offset:1024 lds\n\t" \
    "s_add_u32 m0, %[lwm], 14336\n\t" \
    "s_mov_b32 s60, 5505024\n\t" \
    "buffer_load_dwordx4 %[wsrc], %[srdk], s60 offen lds\n\t" \
    "buffer_load_dwordx4 %[wsrc2], %[srdk], s60 offen offset:1024 lds\n\t" \
    "s_waitcnt vmcnt(16)\n\t" \
    "ds_read_b64_tr_b16 a[64:65], %[trd] offset:16384\n\t" \
    "ds_read_b64_tr_b16 a[66:67], %[trd] offset:16448\n\t" \
    "ds_read_b64_tr_b16 a[68:69], %[trd] offset:17408\n\t" \
    "ds_read_b64_tr_b16 a[70:71], %[trd] offset:17472\n\t" \
    "ds_read_b64_tr_b16 a[72:73], %[trd] offset:18432\n\t" \
    "ds_read_b64_tr_b16 a[74:75], %[trd] offset:18496\n\t" \
    "ds_read_b64_tr_b16 a[76:77], %[trd] offset:19456\n\t" \
    "ds_read_b64_tr_b16 a[78:79], %[trd] offset:19520\n\t" \
    "ds_read_b64_tr_b16 a[80:81], %[trd] offset:20480\n\t" \
    "ds_read_b64_tr_b16 a[82:83], %[trd] offset:20544\n\t" \
    "ds_read_b64_tr_b16 a[84:85], %[trd] offset:21504\n\t" \
    "ds_read_b64_tr_b16 a[86:87], %[trd] offset:21568\n\t" \
    "ds_read_b64_tr_b16 a[88:89], %[trd] offset:22528\n\t" \
    "ds_read_b64_tr_b16 a[90:91], %[trd] offset:22592\n\t" \
    "ds_read_b64_tr_b16 a[92:93], %[trd] offset:23552\n\t" \
    "ds_read_b64_tr_b16 a[94:95], %[trd] offset:23616\n\t" \
    "ds_read_b64_tr_b16 a[96:97], %[trd] offset:24576\n\t" \
    "ds_read_b64_tr_b16 a[98:99], %[trd] offset:24640\n\t" \
    "ds_read_b64_tr_b16 a[100:101], %[trd] offset:25600\n\t" \
    "ds_read_b64_tr_b16 a[102:103], %[trd] offset:25664\n\t" \
    "ds_read_b64_tr_b16 a[104:105], %[trd] offset:26624\n\t" \
    "ds_read_b64_tr_b16 a[106:107], %[trd] offset:26688\n\t" \
    "ds_read_b64_tr_b16 a[108:109], %[trd] offset:27648\n\t" \
    "ds_read_b64_tr_b16 a[110:111], %[trd] offset:27712\n\t" \
    "ds_read_b64_tr_b16 a[112:113], %[trd] offset:28672\n\t" \
    "ds_read_b64_tr_b16 a[114:115], %[trd] offset:28736\n\t" \
    "ds_read_b64_tr_b16 a[116:117], %[trd] offset:29696\n\t" \
    "ds_read_b64_tr_b16 a[118:119], %[trd] offset:29760\n\t" \
    "ds_read_b64_tr_b16 a[120:121], %[trd] offset:30720\n\t" \
    "ds_read_b64_tr_b16 a[122:123], %[trd] offset:30784\n\t" \
    "ds_read_b64_tr_b16 a[124:125], %[trd] offset:31744\n\t" \
    "ds_read_b64_tr_b16 a[126:127], %[trd] offset:31808\n\t" \
    "s_waitcnt lgkmcnt(0)\n\t" \
    "s_add_u32 m0, %[lwm], 16384\n\t" \
    "s_mov_b32 s60, 7077888\n\t" \
    "buffer_load_dwordx4 %[wsrc], %[srdk], s60 offen lds\n\t" \
    "buffer_load_dwordx4 %[wsrc2], %[srdk], s60 offen offset:1024 lds\n\t" \
    "s_add_u32 m0, %[lwm], 18432\n\t" \
    "s_mov_b32 s60, 3932288\n\t" \
    "buffer_load_dwordx4 %[wsrc], %[srdq], s60 offen lds\n\t" \
    "buffer_load_dwordx4 %[wsrc2], %[srdq], s60 offen offset:1024 lds\n\t" \
    "s_add_u32 m0, %[lwm], 20480\n\t" \
    "s_mov_b32 s60, 5505152\n\t" \
    "buffer_load_dwordx4 %[wsrc], %[srdq], s60 offen lds\n\t" \
    "buffer_load_dwordx4 %[wsrc2], %[srdq], s60 offen offset:1024 lds\n\t" \
    "s_add_u32 m0, %[lwm], 22528\n\t" \
    "s_mov_b32 s60, 6291584\n\t" \
    "buffer_load_dwordx4 %[wsrc], %[srdq], s60 offen lds\n\t" \
    "buffer_load_dwordx4 %[wsrc2], %[srdq], s60 offen offset:1024 lds\n\t" \
    "s_add_u32 m0, %[lwm], 24576\n\t" \
    "s_mov_b32 s60, 7078016\n\t" \
    "buffer_load_dwordx4 %[wsrc], %[srdq], s60 offen lds\n\t" \
    "buffer_load_dwordx4 %[wsrc2], %[srdq], s60 offen offset:1024 lds\n\t" \
    "s_add_u32 m0, %[lwm], 26624\n\t" \
    "s_mov_b32 s60, 3932288\n\t" \
    "buffer_load_dwordx4 %[wsrc], %[srdk], s60 offen lds\n\t" \
    "buffer_load_dwordx4 %[wsrc2], %[srdk], s60 offen offset:1024 lds\n\t" \
    "s_add_u32 m0, %[lwm], 28672\n\t" \
    "s_mov_b32 s60, 4718720\n\t" \
    "buffer_load_dwordx4 %[wsrc], %[srdk], s60 offen lds\n\t" \
    "buffer_load_dwordx4 %[wsrc2], %[srdk], s60 offen offset:1024 lds\n\t" \
    "s_add_u32 m0, %[lwm], 30720\n\t" \
    "s_mov_b32 s60, 5505152\n\t" \
    "buffer_load_dwordx4 %[wsrc], %[srdk], s60 offen lds\n\t" \
    "buffer_load_dwordx4 %[wsrc2], %[srdk], s60 offen offset:1024 lds\n\t" \
    "s_waitcnt vmcnt(16)\n\t" \
    "ds_read_b64_tr_b16 a[128:129], %[trd] offset:0\n\t" \
    "ds_read_b64_tr_b16 a[130:131], %[trd] offset:64\n\t" \
    "ds_read_b64_tr_b16 a[132:133], %[trd] offset:1024\n\t" \
    "ds_read_b64_tr_b16 a[134:135], %[trd] offset:1088\n\t" \
    "ds_read_b64_tr_b16 a[136:137], %[trd] offset:2048\n\t" \
    "ds_read_b64_tr_b16 a[138:139], %[trd] offset:2112\n\t" \
    "ds_read_b64_tr_b16 a[140:141], %[trd] offset:3072\n\t" \
    "ds_read_b64_tr_b16 a[142:143], %[trd] offset:3136\n\t" \
    "ds_read_b64_tr_b16 a[144:145], %[trd] offset:4096\n\t" \
    "ds_read_b64_tr_b16 a[146:147], %[trd] offset:4160\n\t" \
    "ds_read_b64_tr_b16 a[148:149], %[trd] offset:5120\n\t" \
    "ds_read_b64_tr_b16 a[150:151], %[trd] offset:5184\n\t" \
    "ds_read_b64_tr_b16 a[152:153], %[trd] offset:6144\n\t" \
    "ds_read_b64_tr_b16 a[154:155], %[trd] offset:6208\n\t" \
    "ds_read_b64_tr_b16 a[156:157], %[trd] offset:7168\n\t" \
    "ds_read_b64_tr_b16 a[158:159], %[trd] offset:7232\n\t" \
    "ds_read_b64_tr_b16 a[160:161], %[trd] offset:8192\n\t" \
    "ds_read_b64_tr_b16 a[162:163], %[trd] offset:8256\n\t" \
    "ds_read_b64_tr_b16 a[164:165], %[trd] offset:9216\n\t" \
    "ds_read_b64_tr_b16 a[166:167], %[trd] offset:9280\n\t" \
    "ds_read_b64_tr_b16 a[168:169], %[trd] offset:10240\n\t" \
    "ds_read_b64_tr_b16 a[170:171], %[trd] offset:10304\n\t" \
    "ds_read_b64_tr_b16 a[172:173], %[trd] offset:11264\n\t" \
    "ds_read_b64_tr_b16 a[174:175], %[trd] offset:11328\n\t" \
    "ds_read_b64_tr_b16 a[176:177], %[trd] offset:12288\n\t" \
    "ds_read_b64_tr_b16 a[178:179], %[trd] offset:12352\n\t" \
    "ds_read_b64_tr_b16 a[180:181], %[trd] offset:13312\n\t" \
    "ds_read_b64_tr_b16 a[182:183], %[trd] offset:13376\n\t" \
    "ds_read_b64_tr_b16 a[184:185], %[trd] offset:14336\n\t" \
    "ds_read_b64_tr_b16 a[186:187], %[trd] offset:14400\n\t" \
    "ds_read_b64_tr_b16 a[188:189], %[trd] offset:15360\n\t" \
    "ds_read_b64_tr_b16 a[190:191], %[trd] offset:15424\n\t" \
    "s_waitcnt lgkmcnt(0)\n\t" \
    "s_add_u32 m0, %[lwm], 0\n\t" \
    "s_mov_b32 s60, 6291584\n\t" \
    "buffer_load_dwordx4 %[wsrc], %[srdk], s60 offen lds\n\t" \
    "buffer_load_dwordx4 %[wsrc2], %[srdk], s60 offen offset:1024 lds\n\t" \
    "s_add_u32 m0, %[lwm], 2048\n\t" \
    "s_mov_b32 s60, 7078016\n\t" \
    "buffer_load_dwordx4 %[wsrc], %[srdk], s60 offen lds\n\t" \
    "buffer_load_dwordx4 %[wsrc2], %[srdk], s60 offen offset:1024 lds\n\t" \
    "s_add_u32 m0, %[lwm], 4096\n\t" \
    "s_mov_b32 s60, 3932416\n\t" \
    "buffer_load_dwordx4 %[wsrc], %[srdq], s60 offen lds\n\t" \
    "buffer_load_dwordx4 %[wsrc2], %[srdq], s60 offen offset:1024 lds\n\t" \
    "s_add_u32 m0, %[lwm], 6144\n\t" \
    "s_mov_b32 s60, 4718848\n\t" \
    "buffer_load_dwordx4 %[wsrc], %[srdq], s60 offen lds\n\t" \
    "buffer_load_dwordx4 %[wsrc2], %[srdq], s60 offen offset:1024 lds\n\t" \
    "s_add_u32 m0, %[lwm], 8192\n\t" \
    "s_mov_b32 s60, 5505280\n\t" \
    "buffer_load_dwordx4 %[wsrc], %[srdq], s60 offen lds\n\t" \
    "buffer_load_dwordx4 %[wsrc2], %[srdq], s60 offen offset:1024 lds\n\t" \
    "s_add_u32 m0, %[lwm], 10240\n\t" \
    "s_mov_b32 s60, 6291712\n\t" \
    "buffer_load_dwordx4 %[wsrc], %[srdq], s60 offen lds\n\t" \
    "buffer_load_dwordx4 %[wsrc2], %[srdq], s60 offen offset:1024 lds\n\t" \
    "s_add_u32 m0, %[lwm], 12288\n\t" \
    "s_mov_b32 s60, 7078144\n\t" \
    "buffer_load_dwordx4 %[wsrc], %[srdq], s60 offen lds\n\t" \
    "buffer_load_dwordx4 %[wsrc2], %[srdq], s60 offen offset:1024 lds\n\t" \
    "s_add_u32 m0, %[lwm], 14336\n\t" \
    "s_mov_b32 s60, 3932416\n\t" \
    "buffer_load_dwordx4 %[wsrc], %[srdk], s60 offen lds\n\t" \
    "buffer_load_dwordx4 %[wsrc2], %[srdk], s60 offen offset:1024 lds\n\t" \
    "s_waitcnt vmcnt(16)\n\t" \
    "ds_read_b64_tr_b16 a[192:193], %[trd] offset:16384\n\t" \
    "ds_read_b64_tr_b16 a[194:195], %[trd] offset:16448\n\t" \
    "ds_read_b64_tr_b16 a[196:197], %[trd] offset:17408\n\t" \
    "ds_read_b64_tr_b16 a[198:199], %[trd] offset:17472\n\t" \
    "ds_read_b64_tr_b16 a[200:201], %[trd] offset:18432\n\t" \
    "ds_read_b64_tr_b16 a[202:203], %[trd] offset:18496\n\t" \
    "ds_read_b64_tr_b16 a[204:205], %[trd] offset:19456\n\t" \
    "ds_read_b64_tr_b16 a[206:207], %[trd] offset:19520\n\t" \
    "ds_read_b64_tr_b16 a[208:209], %[trd] offset:20480\n\t" \
    "ds_read_b64_tr_b16 a[210:211], %[trd] offset:20544\n\t" \
    "ds_read_b64_tr_b16 a[212:213], %[trd] offset:21504\n\t" \
    "ds_read_b64_tr_b16 a[214:215], %[trd] offset:21568\n\t" \
    "ds_read_b64_tr_b16 a[216:217], %[trd] offset:22528\n\t" \
    "ds_read_b64_tr_b16 a[218:219], %[trd] offset:22592\n\t" \
    "ds_read_b64_tr_b16 a[220:221], %[trd] offset:23552\n\t" \
    "ds_read_b64_tr_b16 a[222:223], %[trd] offset:23616\n\t" \
    "ds_read_b64_tr_b16 a[224:225], %[trd] offset:24576\n\t" \
    "ds_read_b64_tr_b16 a[226:227], %[trd] offset:24640\n\t" \
    "ds_read_b64_tr_b16 a[228:229], %[trd] offset:25600\n\t" \
    "ds_read_b64_tr_b16 a[230:231], %[trd] offset:25664\n\t" \
    "ds_read_b64_tr_b16 a[232:233], %[trd] offset:26624\n\t" \
    "ds_read_b64_tr_b16 a[234:235], %[trd] offset:26688\n\t" \
    "ds_read_b64_tr_b16 a[236:237], %[trd] offset:27648\n\t" \
    "ds_read_b64_tr_b16 a[238:239], %[trd] offset:27712\n\t" \
    "ds_read_b64_tr_b16 a[240:241], %[trd] offset:28672\n\t" \
    "ds_read_b64_tr_b16 a[242:243], %[trd] offset:28736\n\t" \
    "ds_read_b64_tr_b16 a[244:245], %[trd] offset:29696\n\t" \
    "ds_read_b64_tr_b16 a[246:247], %[trd] offset:29760\n\t" \
    "ds_read_b64_tr_b16 a[248:249], %[trd] offset:30720\n\t" \
    "ds_read_b64_tr_b16 a[250:251], %[trd] offset:30784\n\t" \
    "ds_read_b64_tr_b16 a[252:253], %[trd] offset:31744\n\t" \
    "ds_read_b64_tr_b16 a[254:255], %[trd] offset:31808\n\t" \
    "s_waitcnt lgkmcnt(0)\n\t" \
    "s_add_u32 m0, %[lwm], 16384\n\t" \
    "s_mov_b32 s60, 4718848\n\t" \
    "buffer_load_dwordx4 %[wsrc], %[srdk], s60 offen lds\n\t" \
    "buffer_load_dwordx4 %[wsrc2], %[srdk], s60 offen offset:1024 lds\n\t" \
    "s_add_u32 m0, %[lwm], 18432\n\t" \
    "s_mov_b32 s60, 5505280\n\t" \
    "buffer_load_dwordx4 %[wsrc], %[srdk], s60 offen lds\n\t" \
    "buffer_load_dwordx4 %[wsrc2], %[srdk], s60 offen offset:1024 lds\n\t" \
    "s_add_u32 m0, %[lwm], 20480\n\t" \
    "s_mov_b32 s60, 6291712\n\t" \
    "buffer_load_dwordx4 %[wsrc], %[srdk], s60 offen lds\n\t" \
    "buffer_load_dwordx4 %[wsrc2], %[srdk], s60 offen offset:1024 lds\n\t" \
    "s_add_u32 m0, %[lwm], 22528\n\t" \
    "s_mov_b32 s60, 7078144\n\t" \
    "buffer_load_dwordx4 %[wsrc], %[srdk], s60 offen lds\n\t" \
    "buffer_load_dwordx4 %[wsrc2], %[srdk], s60 offen offset:1024 lds\n\t" \
    "s_waitcnt vmcnt(8)\n\t" \
    "ds_read_b64_tr_b16 v[24:25], %[trd] offset:0\n\t" \
    "ds_read_b64_tr_b16 v[26:27], %[trd] offset:64\n\t" \
    "ds_read_b64_tr_b16 v[28:29], %[trd] offset:1024\n\t" \
    "ds_read_b64_tr_b16 v[30:31], %[trd] offset:1088\n\t" \
    "ds_read_b64_tr_b16 v[32:33], %[trd] offset:2048\n\t" \
    "ds_read_b64_tr_b16 v[34:35], %[trd] offset:2112\n\t" \
    "ds_read_b64_tr_b16 v[36:37], %[trd] offset:3072\n\t" \
    "ds_read_b64_tr_b16 v[38:39], %[trd] offset:3136\n\t" \
    "ds_read_b64_tr_b16 v[40:41], %[trd] offset:4096\n\t" \
    "ds_read_b64_tr_b16 v[42:43], %[trd] offset:4160\n\t" \
    "ds_read_b64_tr_b16 v[44:45], %[trd] offset:5120\n\t" \
    "ds_read_b64_tr_b16 v[46:47], %[trd] offset:5184\n\t" \
    "ds_read_b64_tr_b16 v[48:49], %[trd] offset:6144\n\t" \
    "ds_read_b64_tr_b16 v[50:51], %[trd] offset:6208\n\t" \
    "ds_read_b64_tr_b16 v[52:53], %[trd] offset:7168\n\t" \
    "ds_read_b64_tr_b16 v[54:55], %[trd] offset:7232\n\t" \
    "ds_read_b64_tr_b16 v[56:57], %[trd] offset:8192\n\t" \
    "ds_read_b64_tr_b16 v[58:59], %[trd] offset:8256\n\t" \
    "ds_read_b64_tr_b16 v[60:61], %[trd] offset:9216\n\t" \
    "ds_read_b64_tr_b16 v[62:63], %[trd] offset:9280\n\t" \
    "ds_read_b64_tr_b16 v[64:65], %[trd] offset:10240\n\t" \
    "ds_read_b64_tr_b16 v[66:67], %[trd] offset:10304\n\t" \
    "ds_read_b64_tr_b16 v[68:69], %[trd] offset:11264\n\t" \
    "ds_read_b64_tr_b16 v[70:71], %[trd] offset:11328\n\t" \
    "ds_read_b64_tr_b16 v[72:73], %[trd] offset:12288\n\t" \
    "ds_read_b64_tr_b16 v[74:75], %[trd] offset:12352\n\t" \
    "ds_read_b64_tr_b16 v[76:77], %[trd] offset:13312\n\t" \
    "ds_read_b64_tr_b16 v[78:79], %[trd] offset:13376\n\t" \
    "ds_read_b64_tr_b16 v[80:81], %[trd] offset:14336\n\t" \
    "ds_read_b64_tr_b16 v[82:83], %[trd] offset:14400\n\t" \
    "ds_read_b64_tr_b16 v[84:85], %[trd] offset:15360\n\t" \
    "ds_read_b64_tr_b16 v[86:87], %[trd] offset:15424\n\t" \
    "s_waitcnt lgkmcnt(0)\n\t" \
    "s_add_u32 m0, %[lwm], 0\n\t" \
    "s_mov_b32 s60, 0\n\t" \
    "buffer_load_dwordx4 %[wsrc], %[srdq], s60 offen lds\n\t" \
    "buffer_load_dwordx4 %[wsrc2], %[srdq], s60 offen offset:1024 lds\n\t" \
    "s_add_u32 m0, %[lwm], 2048\n\t" \
    "s_mov_b32 s60, 128\n\t" \
    "buffer_load_dwordx4 %[wsrc], %[srdq], s60 offen lds\n\t" \
    "buffer_load_dwordx4 %[wsrc2], %[srdq], s60 offen offset:1024 lds\n\t" \
    "s_add_u32 m0, %[lwm], 4096\n\t" \
    "s_mov_b32 s60, 256\n\t" \
    "buffer_load_dwordx4 %[wsrc], %[srdk], s60 offen lds\n\t" \
    "buffer_load_dwordx4 %[wsrc2], %[srdk], s60 offen offset:1024 lds\n\t" \
    "s_add_u32 m0, %[lwm], 6144\n\t" \
    "s_mov_b32 s60, 786560\n\t" \
    "buffer_load_dwordx4 %[wsrc], %[srdq], s60 offen lds\n\t" \
    "buffer_load_dwordx4 %[wsrc2], %[srdq], s60 offen offset:1024 lds\n\t" \
    "s_add_u32 m0, %[lwm], 8192\n\t" \
    "s_mov_b32 s60, 786688\n\t" \
    "buffer_load_dwordx4 %[wsrc], %[srdk], s60 offen lds\n\t" \
    "buffer_load_dwordx4 %[wsrc2], %[srdk], s60 offen offset:1024 lds\n\t" \
    "s_add_u32 m0, %[lwm], 10240\n\t" \
    "s_mov_b32 s60, 1572864\n\t" \
    "buffer_load_dwordx4 %[wsrc], %[srdk], s60 offen lds\n\t" \
    "buffer_load_dwordx4 %[wsrc2], %[srdk], s60 offen offset:1024 lds\n\t" \
    "s_add_u32 m0, %[lwm], 12288\n\t" \
    "s_mov_b32 s60, 1573120\n\t" \
    "buffer_load_dwordx4 %[wsrc], %[srdq], s60 offen lds\n\t" \
    "buffer_load_dwordx4 %[wsrc2], %[srdq], s60 offen offset:1024 lds\n\t" \
    "s_add_u32 m0, %[lwm], 14336\n\t" \
    "s_mov_b32 s60, 2359296\n\t" \
    "buffer_load_dwordx4 %[wsrc], %[srdk], s60 offen lds\n\t" \
    "buffer_load_dwordx4 %[wsrc2], %[srdk], s60 offen offset:1024 lds\n\t" \
    "s_waitcnt vmcnt(16)\n\t" \
    "ds_read_b64_tr_b16 v[88:89], %[trd] offset:16384\n\t" \
    "ds_read_b64_tr_b16 v[90:91], %[trd] offset:16448\n\t" \
    "ds_read_b64_tr_b16 v[92:93], %[trd] offset:17408\n\t" \
    "ds_read_b64_tr_b16 v[94:95], %[trd] offset:17472\n\t" \
    "ds_read_b64_tr_b16 v[96:97], %[trd] offset:18432\n\t" \
    "ds_read_b64_tr_b16 v[98:99], %[trd] offset:18496\n\t" \
    "ds_read_b64_tr_b16 v[100:101], %[trd] offset:19456\n\t" \
    "ds_read_b64_tr_b16 v[102:103], %[trd] offset:19520\n\t" \
    "ds_read_b64_tr_b16 v[104:105], %[trd] offset:20480\n\t" \
    "ds_read_b64_tr_b16 v[106:107], %[trd] offset:20544\n\t" \
    "ds_read_b64_tr_b16 v[108:109], %[trd] offset:21504\n\t" \
    "ds_read_b64_tr_b16 v[110:111], %[trd] offset:21568\n\t" \
    "ds_read_b64_tr_b16 v[112:113], %[trd] offset:22528\n\t" \
    "ds_read_b64_tr_b16 v[114:115], %[trd] offset:22592\n\t" \
    "ds_read_b64_tr_b16 v[116:117], %[trd] offset:23552\n\t" \
    "ds_read_b64_tr_b16 v[118:119], %[trd] offset:23616\n\t" \
    "s_waitcnt lgkmcnt(0)\n\t" \
    "s_add_u32 m0, %[lwm], 16384\n\t" \
    "s_mov_b32 s60, 2359552\n\t" \
    "buffer_load_dwordx4 %[wsrc], %[srdq], s60 offen lds\n\t" \
    "buffer_load_dwordx4 %[wsrc2], %[srdq], s60 offen offset:1024 lds\n\t" \
    "s_add_u32 m0, %[lwm], 18432\n\t" \
    "s_mov_b32 s60, 3145728\n\t" \
    "buffer_load_dwordx4 %[wsrc], %[srdq], s60 offen lds\n\t" \
    "buffer_load_dwordx4 %[wsrc2], %[srdq], s60 offen offset:1024 lds\n\t" \
    "s_add_u32 m0, %[lwm], 20480\n\t" \
    "s_mov_b32 s60, 3145856\n\t" \
    "buffer_load_dwordx4 %[wsrc], %[srdk], s60 offen lds\n\t" \
    "buffer_load_dwordx4 %[wsrc2], %[srdk], s60 offen offset:1024 lds\n\t" \
    "s_add_u32 m0, %[lwm], 22528\n\t" \
    "s_mov_b32 s60, 3932160\n\t" \
    "buffer_load_dwordx4 %[wsrc], %[srdq], s60 offen lds\n\t" \
    "buffer_load_dwordx4 %[wsrc2], %[srdq], s60 offen offset:1024 lds\n\t" \
    "s_add_u32 m0, %[lwm], 24576\n\t" \
    "s_mov_b32 s60, 6291456\n\t" \
    "buffer_load_dwordx4 %[wsrc], %[srdq], s60 offen lds\n\t" \
    "buffer_load_dwordx4 %[wsrc2], %[srdq], s60 offen offset:1024 lds\n\t" \
    "s_add_u32 m0, %[lwm], 26624\n\t" \
    "s_mov_b32 s60, 3932160\n\t" \
    "buffer_load_dwordx4 %[wsrc], %[srdk], s60 offen lds\n\t" \
    "buffer_load_dwordx4 %[wsrc2], %[srdk], s60 offen offset:1024 lds\n\t" \
    "s_add_u32 m0, %[lwm], 28672\n\t" \
    "s_mov_b32 s60, 6291456\n\t" \
    "buffer_load_dwordx4 %[wsrc], %[srdk], s60 offen lds\n\t" \
    "buffer_load_dwordx4 %[wsrc2], %[srdk], s60 offen offset:1024 lds\n\t" \
    "s_add_u32 m0, %[lwm], 30720\n\t" \
    "s_mov_b32 s60, 4718720\n\t" \
    "buffer_load_dwordx4 %[wsrc], %[srdq], s60 offen lds\n\t" \
    "buffer_load_dwordx4 %[wsrc2], %[srdq], s60 offen offset:1024 lds\n\t" \
    "s_waitcnt vmcnt(16)\n\t" \
    "ds_read_b64_tr_b16 v[120:121], %[trd] offset:0\n\t" \
    "ds_read_b64_tr_b16 v[122:123], %[trd] offset:64\n\t" \
    "ds_read_b64_tr_b16 v[124:125], %[trd] offset:1024\n\t" \
    "ds_read_b64_tr_b16 v[126:127], %[trd] offset:1088\n\t" \
    "ds_read_b64_tr_b16 v[128:129], %[trd] offset:2048\n\t" \
    "ds_read_b64_tr_b16 v[130:131], %[trd] offset:2112\n\t" \
    "ds_read_b64_tr_b16 v[132:133], %[trd] offset:3072\n\t" \
    "ds_read_b64_tr_b16 v[134:135], %[trd] offset:3136\n\t" \
    "ds_read_b64_tr_b16 v[136:137], %[trd] offset:4096\n\t" \
    "ds_read_b64_tr_b16 v[138:139], %[trd] offset:4160\n\t" \
    "ds_read_b64_tr_b16 v[140:141], %[trd] offset:5120\n\t" \
    "ds_read_b64_tr_b16 v[142:143], %[trd] offset:5184\n\t" \
    "ds_read_b64_tr_b16 v[144:145], %[trd] offset:6144\n\t" \
    "ds_read_b64_tr_b16 v[146:147], %[trd] offset:6208\n\t" \
    "ds_read_b64_tr_b16 v[148:149], %[trd] offset:7168\n\t" \
    "ds_read_b64_tr_b16 v[150:151], %[trd] offset:7232\n\t" \
    "ds_read_b64_tr_b16 v[152:153], %[trd] offset:8192\n\t" \
    "ds_read_b64_tr_b16 v[154:155], %[trd] offset:8256\n\t" \
    "ds_read_b64_tr_b16 v[156:157], %[trd] offset:9216\n\t" \
    "ds_read_b64_tr_b16 v[158:159], %[trd] offset:9280\n\t" \
    "ds_read_b64_tr_b16 v[160:161], %[trd] offset:10240\n\t" \
    "ds_read_b64_tr_b16 v[162:163], %[trd] offset:10304\n\t" \
    "ds_read_b64_tr_b16 v[164:165], %[trd] offset:11264\n\t" \
    "ds_read_b64_tr_b16 v[166:167], %[trd] offset:11328\n\t" \
    "ds_read_b64_tr_b16 v[168:169], %[trd] offset:12288\n\t" \
    "ds_read_b64_tr_b16 v[170:171], %[trd] offset:12352\n\t" \
    "ds_read_b64_tr_b16 v[172:173], %[trd] offset:13312\n\t" \
    "ds_read_b64_tr_b16 v[174:175], %[trd] offset:13376\n\t" \
    "ds_read_b64_tr_b16 v[176:177], %[trd] offset:14336\n\t" \
    "ds_read_b64_tr_b16 v[178:179], %[trd] offset:14400\n\t" \
    "ds_read_b64_tr_b16 v[180:181], %[trd] offset:15360\n\t" \
    "ds_read_b64_tr_b16 v[182:183], %[trd] offset:15424\n\t" \
    "s_waitcnt lgkmcnt(0)\n\t" \
    "s_waitcnt vmcnt(0)\n\t" \
    "ds_read_b64_tr_b16 v[184:185], %[trd] offset:16384\n\t" \
    "ds_read_b64_tr_b16 v[186:187], %[trd] offset:16448\n\t" \
    "ds_read_b64_tr_b16 v[188:189], %[trd] offset:17408\n\t" \
    "ds_read_b64_tr_b16 v[190:191], %[trd] offset:17472\n\t" \
    "ds_read_b64_tr_b16 v[192:193], %[trd] offset:18432\n\t" \
    "ds_read_b64_tr_b16 v[194:195], %[trd] offset:18496\n\t" \
    "ds_read_b64_tr_b16 v[196:197], %[trd] offset:19456\n\t" \
    "ds_read_b64_tr_b16 v[198:199], %[trd] offset:19520\n\t" \
    "ds_read_b64_tr_b16 v[200:201], %[trd] offset:20480\n\t" \
    "ds_read_b64_tr_b16 v[202:203], %[trd] offset:20544\n\t" \
    "ds_read_b64_tr_b16 v[204:205], %[trd] offset:21504\n\t" \
    "ds_read_b64_tr_b16 v[206:207], %[trd] offset:21568\n\t" \
    "ds_read_b64_tr_b16 v[208:209], %[trd] offset:22528\n\t" \
    "ds_read_b64_tr_b16 v[210:211], %[trd] offset:22592\n\t" \
    "ds_read_b64_tr_b16 v[212:213], %[trd] offset:23552\n\t" \
    "ds_read_b64_tr_b16 v[214:215], %[trd] offset:23616\n\t" \
    "ds_read_b64_tr_b16 v[216:217], %[trd] offset:24576\n\t" \
    "ds_read_b64_tr_b16 v[218:219], %[trd] offset:24640\n\t" \
    "ds_read_b64_tr_b16 v[220:221], %[trd] offset:25600\n\t" \
    "ds_read_b64_tr_b16 v[222:223], %[trd] offset:25664\n\t" \
    "ds_read_b64_tr_b16 v[224:225], %[trd] offset:26624\n\t" \
    "ds_read_b64_tr_b16 v[226:227], %[trd] offset:26688\n\t" \
    "ds_read_b64_tr_b16 v[228:229], %[trd] offset:27648\n\t" \
    "ds_read_b64_tr_b16 v[230:231], %[trd] offset:27712\n\t" \
    "ds_read_b64_tr_b16 v[232:233], %[trd] offset:28672\n\t" \
    "ds_read_b64_tr_b16 v[234:235], %[trd] offset:28736\n\t" \
    "ds_read_b64_tr_b16 v[236:237], %[trd] offset:29696\n\t" \
    "ds_read_b64_tr_b16 v[238:239], %[trd] offset:29760\n\t" \
    "ds_read_b64_tr_b16 v[240:241], %[trd] offset:30720\n\t" \
    "ds_read_b64_tr_b16 v[242:243], %[trd] offset:30784\n\t" \
    "ds_read_b64_tr_b16 v[244:245], %[trd] offset:31744\n\t" \
    "ds_read_b64_tr_b16 v[246:247], %[trd] offset:31808\n\t" \
    "s_waitcnt lgkmcnt(0)\n\t" \
    "ds_write_b128 %[lw], v[120:123] offset:0\n\t" \
    "ds_write_b128 %[lw], v[124:127] offset:1024\n\t" \
    "ds_write_b128 %[lw], v[128:131] offset:2048\n\t" \
    "ds_write_b128 %[lw], v[132:135] offset:3072\n\t" \
    "ds_write_b128 %[lw], v[136:139] offset:4096\n\t" \
    "ds_write_b128 %[lw], v[140:143] offset:5120\n\t" \
    "ds_write_b128 %[lw], v[144:147] offset:6144\n\t" \
    "ds_write_b128 %[lw], v[148:151] offset:7168\n\t" \
    "ds_write_b128 %[lw], v[152:155] offset:8192\n\t" \
    "ds_write_b128 %[lw], v[156:159] offset:9216\n\t" \
    "ds_write_b128 %[lw], v[160:163] offset:10240\n\t" \
    "ds_write_b128 %[lw], v[164:167] offset:11264\n\t" \
    "ds_write_b128 %[lw], v[168:171] offset:12288\n\t" \
    "ds_write_b128 %[lw], v[172:175] offset:13312\n\t" \
    "ds_write_b128 %[lw], v[176:179] offset:14336\n\t" \
    "ds_write_b128 %[lw], v[180:183] offset:15360\n\t" \
    "ds_write_b128 %[lw], v[184:187] offset:16384\n\t" \
    "ds_write_b128 %[lw], v[188:191] offset:17408\n\t" \
    "ds_write_b128 %[lw], v[192:195] offset:18432\n\t" \
    "ds_write_b128 %[lw], v[196:199] offset:19456\n\t" \
    "ds_write_b128 %[lw], v[200:203] offset:20480\n\t" \
    "ds_write_b128 %[lw], v[204:207] offset:21504\n\t" \
    "ds_write_b128 %[lw], v[208:211] offset:22528\n\t" \
    "ds_write_b128 %[lw], v[212:215] offset:23552\n\t" \
    "ds_write_b128 %[lw], v[216:219] offset:24576\n\t" \
    "ds_write_b128 %[lw], v[220:223] offset:25600\n\t" \
    "ds_write_b128 %[lw], v[224:227] offset:26624\n\t" \
    "ds_write_b128 %[lw], v[228:231] offset:27648\n\t" \
    "ds_write_b128 %[lw], v[232:235] offset:28672\n\t" \
    "ds_write_b128 %[lw], v[236:239] offset:29696\n\t" \
    "ds_write_b128 %[lw], v[240:243] offset:30720\n\t" \
    "ds_write_b128 %[lw], v[244:247] offset:31744\n\t" \
    "s_waitcnt lgkmcnt(0)\n\t" \
    "s_mov_b32 s50, %[offkx]\n\t" \
    "s_mov_b32 s52, %[offkm]\n\t" \
    "v_mov_b32 v246, 0\n\t" \
    "s_nop 0\n\t" \
    "ds_write_b32 %[flw], v246\n\t" \
    "s_waitcnt vmcnt(0) lgkmcnt(0)\n\t" \
    "ds_read_b128 v[120:123], %[lw] offset:0\n\t" \
    "ds_read_b128 v[124:127], %[lw] offset:1024\n\t" \
    "ds_read_b128 v[128:131], %[lw] offset:2048\n\t" \
    "ds_read_b128 v[132:135], %[lw] offset:3072\n\t" \
    "ds_read_b128 v[136:139], %[lw] offset:4096\n\t" \
    "ds_read_b128 v[140:143], %[lw] offset:5120\n\t" \
    "ds_read_b128 v[144:147], %[lw] offset:6144\n\t" \
    "ds_read_b128 v[148:151], %[lw] offset:7168\n\t" \
    "s_cmp_eq_u32 %[w], 0\n\t" \
    "s_cbranch_scc1 s_w0_%=\n\t" \
    "s_add_u32 s57, s40, 0\n\t" \
    "s_and_b32 s54, s57, 0x43\n\t" \
    "s_cmp_lg_u32 s54, 0\n\t" \
    "s_cbranch_scc1 pk_1_%=\n\t" \
    "s_lshl_b32 s57, s57, 5\n\t" \
    "s_mov_b32 s53, 0\n\t" \
    "pa_2_%=:\n\t" \
    "v_mov_b32 v246, 0\n\t" \
    "s_nop 0\n\t" \
    "buffer_load_dword v247, v246, %[srd], s57 offen offset:512 sc1\n\t" \
    "s_waitcnt vmcnt(0)\n\t" \
    "v_readfirstlane_b32 s54, v247\n\t" \
    "s_nop 3\n\t" \
    "s_cmp_ge_u32 s54, 64\n\t" \
    "s_cbranch_scc1 pk_1_%=\n\t" \
    "s_sleep 8\n\t" \
    "s_add_u32 s53, s53, 1\n\t" \
    "s_cmp_gt_u32 s53, 131072\n\t" \
    "s_cbranch_scc1 ab_3_%=\n\t" \
    "s_and_b32 s54, s53, 63\n\t" \
    "s_cmp_lg_u32 s54, 0\n\t" \
    "s_cbranch_scc1 pa_2_%=\n\t" \
    "v_mov_b32 v248, 0\n\t" \
    "s_nop 0\n\t" \
    "buffer_load_dword v249, v248, %[srd], 0 offen sc1\n\t" \
    "s_waitcnt vmcnt(0)\n\t" \
    "v_readfirstlane_b32 s54, v249\n\t" \
    "s_nop 3\n\t" \
    "s_cmp_eq_u32 s54, 0\n\t" \
    "s_cbranch_scc1 pa_2_%=\n\t" \
    "s_branch s_end_%=\n\t" \
    "ab_3_%=:\n\t" \
    "v_mov_b32 v248, 0\n\t" \
    "v_mov_b32 v249, 1\n\t" \
    "s_nop 0\n\t" \
    "buffer_store_dword v249, v248, %[srd], 0 offen sc1\n\t" \
    "s_branch s_end_%=\n\t" \
    "pk_1_%=:\n\t" \
    "s_add_u32 s51, s50, 6144\n\t" \
    "buffer_load_dwordx4 v[216:219], %[kxo], %[srd], s50 offen offset:0 sc1\n\t" \
    "buffer_load_dwordx4 v[220:223], %[kxo], %[srd], s51 offen offset:0 sc1\n\t" \
    "buffer_load_dwordx4 v[224:227], %[kxo], %[srd], s50 offen offset:1024 sc1\n\t" \
    "buffer_load_dwordx4 v[228:231], %[kxo], %[srd], s51 offen offset:1024 sc1\n\t" \
    "s_waitcnt vmcnt(0)\n\t" \
    "ds_write_b128 %[kxl], v[216:219] offset:0\n\t" \
    "ds_write_b128 %[kxl], v[220:223] offset:1024\n\t" \
    "ds_write_b128 %[kxl], v[224:227] offset:2048\n\t" \
    "ds_write_b128 %[kxl], v[228:231] offset:3072\n\t" \
    "s_waitcnt lgkmcnt(0)\n\t" \
    "s_waitcnt lgkmcnt(0)\n\t" \
    "s_barrier\n\t" \
    "sl_4_%=:\n\t" \
    "v_mov_b32 v254, s61\n\t" \
    "v_add_u32 v255, 1, v254\n\t" \
    "s_cmp_eq_u32 s40, 0\n\t" \
    "s_cbranch_scc1 sn_5_%=\n\t" \
    "s_memrealtime s[58:59]\n\t" \
    "s_waitcnt lgkmcnt(0)\n\t" \
    "s_sub_i32 s54, s41, s58\n\t" \
    "s_cmp_le_i32 s54, 4\n\t" \
    "s_cbranch_scc1 wd_9_%=\n\t" \
    "s_cmp_gt_i32 s54, 2000\n\t" \
    "s_cbranch_scc1 wd_9_%=\n\t" \
    "w1_10_%=:\n\t" \
    "s_memrealtime s[58:59]\n\t" \
    "s_waitcnt lgkmcnt(0)\n\t" \
    "s_sub_i32 s54, s41, s58\n\t" \
    "s_cmp_gt_i32 s54, 4\n\t" \
    "s_cbranch_scc1 w1_10_%=\n\t" \
    "w2_11_%=:\n\t" \
    "s_cmp_lt_i32 s54, 2\n\t" \
    "s_cbranch_scc1 wd_9_%=\n\t" \
    "s_nop 3\n\t" \
    "s_sub_i32 s54, s54, 1\n\t" \
    "s_branch w2_11_%=\n\t" \
    "wd_9_%=:\n\t" \
    "sn_5_%=:\n\t" \
    "s_mov_b32 s53, 0\n\t" \
    "s_mov_b32 s62, 0\n\t" \
    "buffer_load_dwordx4 v[152:155], %[mlo], %[srd], s46 offen offset:0 sc1\n\t" \
    "s_nop 7\n\t" \
    "buffer_load_dwordx4 v[156:159], %[mlo], %[srd], s46 offen offset:1024 sc1\n\t" \
    "s_nop 7\n\t" \
    "buffer_load_dwordx4 v[160:163], %[mlo], %[srd], s46 offen offset:2048 sc1\n\t" \
    "s_nop 7\n\t" \
    "buffer_load_dwordx4 v[164:167], %[mlo], %[srd], s46 offen offset:3072 sc1\n\t" \
    "s_nop 7\n\t" \
    "buffer_load_dwordx4 v[168:171], %[mlo], %[srd], s47 offen offset:0 sc1\n\t" \
    "s_nop 7\n\t" \
    "buffer_load_dwordx4 v[172:175], %[mlo], %[srd], s47 offen offset:1024 sc1\n\t" \
    "s_nop 7\n\t" \
    "buffer_load_dwordx4 v[176:179], %[mlo], %[srd], s47 offen offset:2048 sc1\n\t" \
    "s_nop 7\n\t" \
    "buffer_load_dwordx4 v[180:183], %[mlo], %[srd], s47 offen offset:3072 sc1\n\t" \
    "s_nop 7\n\t" \
    "buffer_load_dwordx4 v[184:187], %[mlo], %[srd], s48 offen offset:0 sc1\n\t" \
    "s_nop 7\n\t" \
    "buffer_load_dwordx4 v[188:191], %[mlo], %[srd], s48 offen offset:1024 sc1\n\t" \
    "s_nop 7\n\t" \
    "buffer_load_dwordx2 v[244:245], %[tsa], %[srd], s46 offen sc1\n\t" \
    "sr_6_%=:\n\t" \
    "v_mov_b64 v[192:193], 0\n\t" \
    "v_mov_b64 v[194:195], 0\n\t" \
    "v_mov_b64 v[196:197], 0\n\t" \
    "v_mov_b64 v[198:199], 0\n\t" \
    "v_mov_b64 v[200:201], 0\n\t" \
    "v_mov_b64 v[202:203], 0\n\t" \
    "v_mov_b64 v[204:205], 0\n\t" \
    "v_mov_b64 v[206:207], 0\n\t" \
    "v_mov_b64 v[208:209], 0\n\t" \
    "v_mov_b64 v[210:211], 0\n\t" \
    "v_mov_b64 v[212:213], 0\n\t" \
    "v_mov_b64 v[214:215], 0\n\t" \
    "s_waitcnt vmcnt(10)\n\t" \
    "v_smfmac_f32_16x16x64_bf16 v[192:195], v[152:155], v[120:127], %[sidx]\n\t" \
    "ds_read_b128 v[120:123], %[lw] offset:8192\n\t" \
    "ds_read_b128 v[124:127], %[lw] offset:9216\n\t" \
    "v_smfmac_f32_16x16x64_bf16 v[196:199], v[152:155], a[0:7], %[sidx]\n\t" \
    "v_smfmac_f32_16x16x64_bf16 v[200:203], v[152:155], v[128:135], %[sidx]\n\t" \
    "ds_read_b128 v[128:131], %[lw] offset:10240\n\t" \
    "ds_read_b128 v[132:135], %[lw] offset:11264\n\t" \
    "v_smfmac_f32_16x16x64_bf16 v[204:207], v[152:155], a[8:15], %[sidx]\n\t" \
    "v_smfmac_f32_16x16x64_bf16 v[208:211], v[152:155], a[16:23], %[sidx]\n\t" \
    "v_smfmac_f32_16x16x64_bf16 v[212:215], v[152:155], v[136:143], %[sidx]\n\t" \
    "ds_read_b128 v[136:139], %[lw] offset:12288\n\t" \
    "ds_read_b128 v[140:143], %[lw] offset:13312\n\t" \
    "s_waitcnt vmcnt(9)\n\t" \
    "v_smfmac_f32_16x16x64_bf16 v[192:195], v[156:159], a[24:31], %[sidx]\n\t" \
    "v_smfmac_f32_16x16x64_bf16 v[196:199], v[156:159], a[32:39], %[sidx]\n\t" \
    "v_smfmac_f32_16x16x64_bf16 v[200:203], v[156:159], v[144:151], %[sidx]\n\t" \
    "ds_read_b128 v[144:147], %[lw] offset:14336\n\t" \
    "ds_read_b128 v[148:151], %[lw] offset:15360\n\t" \
    "v_smfmac_f32_16x16x64_bf16 v[204:207], v[156:159], a[40:47], %[sidx]\n\t" \
    "v_smfmac_f32_16x16x64_bf16 v[208:211], v[156:159], a[48:55], %[sidx]\n\t" \
    "s_waitcnt lgkmcnt(6)\n\t" \
    "v_smfmac_f32_16x16x64_bf16 v[212:215], v[156:159], v[120:127], %[sidx]\n\t" \
    "ds_read_b128 v[120:123], %[lw] offset:16384\n\t" \
    "ds_read_b128 v[124:127], %[lw] offset:17408\n\t" \
    "s_waitcnt vmcnt(8)\n\t" \
    "v_smfmac_f32_16x16x64_bf16 v[192:195], v[160:163], a[56:63], %[sidx]\n\t" \
    "s_waitcnt lgkmcnt(6)\n\t" \
    "v_smfmac_f32_16x16x64_bf16 v[196:199], v[160:163], v[128:135], %[sidx]\n\t" \
    "ds_read_b128 v[128:131], %[lw] offset:18432\n\t" \
    "ds_read_b128 v[132:135], %[lw] offset:19456\n\t" \
    "v_smfmac_f32_16x16x64_bf16 v[200:203], v[160:163], a[64:71], %[sidx]\n\t" \
    "v_smfmac_f32_16x16x64_bf16 v[204:207], v[160:163], a[72:79], %[sidx]\n\t" \
    "s_waitcnt lgkmcnt(6)\n\t" \
    "v_smfmac_f32_16x16x64_bf16 v[208:211], v[160:163], v[136:143], %[sidx]\n\t" \
    "ds_read_b128 v[136:139], %[lw] offset:20480\n\t" \
    "ds_read_b128 v[140:143], %[lw] offset:21504\n\t" \
    "v_smfmac_f32_16x16x64_bf16 v[212:215], v[160:163], a[80:87], %[sidx]\n\t" \
    "s_waitcnt vmcnt(7)\n\t" \
    "v_smfmac_f32_16x16x64_bf16 v[192:195], v[164:167], a[88:95], %[sidx]\n\t" \
    "s_waitcnt lgkmcnt(6)\n\t" \
    "v_smfmac_f32_16x16x64_bf16 v[196:199], v[164:167], v[144:151], %[sidx]\n\t" \
    "ds_read_b128 v[144:147], %[lw] offset:22528\n\t" \
    "ds_read_b128 v[148:151], %[lw] offset:23552\n\t" \
    "v_smfmac_f32_16x16x64_bf16 v[200:203], v[164:167], a[96:103], %[sidx]\n\t" \
    "v_smfmac_f32_16x16x64_bf16 v[204:207], v[164:167], a[104:111], %[sidx]\n\t" \
    "s_waitcnt lgkmcnt(6)\n\t" \
    "v_smfmac_f32_16x16x64_bf16 v[208:211], v[164:167], v[120:127], %[sidx]\n\t" \
    "ds_read_b128 v[120:123], %[lw] offset:24576\n\t" \
    "ds_read_b128 v[124:127], %[lw] offset:25600\n\t" \
    "v_smfmac_f32_16x16x64_bf16 v[212:215], v[164:167], a[112:119], %[sidx]\n\t" \
    "s_waitcnt vmcnt(6)\n\t" \
    "s_waitcnt lgkmcnt(6)\n\t" \
    "v_smfmac_f32_16x16x64_bf16 v[192:195], v[168:171], v[128:135], %[sidx]\n\t" \
    "ds_read_b128 v[128:131], %[lw] offset:26624\n\t" \
    "ds_read_b128 v[132:135], %[lw] offset:27648\n\t" \
    "v_smfmac_f32_16x16x64_bf16 v[196:199], v[168:171], a[120:127], %[sidx]\n\t" \
    "v_smfmac_f32_16x16x64_bf16 v[200:203], v[168:171], a[128:135], %[sidx]\n\t" \
    "s_waitcnt lgkmcnt(6)\n\t" \
    "v_smfmac_f32_16x16x64_bf16 v[204:207], v[168:171], v[136:143], %[sidx]\n\t" \
    "ds_read_b128 v[136:139], %[lw] offset:28672\n\t" \
    "ds_read_b128 v[140:143], %[lw] offset:29696\n\t" \
    "v_smfmac_f32_16x16x64_bf16 v[208:211], v[168:171], a[136:143], %[sidx]\n\t" \
    "v_smfmac_f32_16x16x64_bf16 v[212:215], v[168:171], a[144:151], %[sidx]\n\t" \
    "s_waitcnt vmcnt(5)\n\t" \
    "s_waitcnt lgkmcnt(6)\n\t" \
    "v_smfmac_f32_16x16x64_bf16 v[192:195], v[172:175], v[144:151], %[sidx]\n\t" \
    "ds_read_b128 v[144:147], %[lw] offset:30720\n\t" \
    "ds_read_b128 v[148:151], %[lw] offset:31744\n\t" \
    "s_waitcnt vmcnt(4)\n\t" \
    "v_smfmac_f32_16x16x64_bf16 v[192:195], v[176:179], a[152:159], %[sidx]\n\t" \
    "s_waitcnt vmcnt(3)\n\t" \
    "v_smfmac_f32_16x16x64_bf16 v[192:195], v[180:183], a[160:167], %[sidx]\n\t" \
    "s_waitcnt vmcnt(2)\n\t" \
    "s_waitcnt lgkmcnt(6)\n\t" \
    "v_smfmac_f32_16x16x64_bf16 v[192:195], v[184:187], v[120:127], %[sidx]\n\t" \
    "s_waitcnt vmcnt(1)\n\t" \
    "v_smfmac_f32_16x16x64_bf16 v[192:195], v[188:191], a[168:175], %[sidx]\n\t" \
    "s_waitcnt lgkmcnt(4)\n\t" \
    "v_smfmac_f32_16x16x64_bf16 v[196:199], v[172:175], v[128:135], %[sidx]\n\t" \
    "v_smfmac_f32_16x16x64_bf16 v[196:199], v[176:179], a[176:183], %[sidx]\n\t" \
    "v_smfmac_f32_16x16x64_bf16 v[196:199], v[180:183], a[184:191], %[sidx]\n\t" \
    "v_add_f32 v192, v192, v194\n\t" \
    "s_waitcnt lgkmcnt(2)\n\t" \
    "v_smfmac_f32_16x16x64_bf16 v[196:199], v[184:187], v[136:143], %[sidx]\n\t" \
    "v_add_f32 v193, v193, v195\n\t" \
    "v_smfmac_f32_16x16x64_bf16 v[196:199], v[188:191], a[192:199], %[sidx]\n\t" \
    "v_cmp_u_f32 vcc, v192, v193\n\t" \
    "v_smfmac_f32_16x16x64_bf16 v[200:203], v[172:175], a[200:207], %[sidx]\n\t" \
    "v_cvt_pk_bf16_f32 v246, v192, v193\n\t" \
    "s_cbranch_vccnz ss_8_%=\n\t" \
    "so_7_%=:\n\t" \
    "s_waitcnt lgkmcnt(0)\n\t" \
    "v_smfmac_f32_16x16x64_bf16 v[200:203], v[176:179], v[144:151], %[sidx]\n\t" \
    "ds_write_b32 %[qkw], v246 offset:0\n\t" \
    "v_smfmac_f32_16x16x64_bf16 v[200:203], v[180:183], a[208:215], %[sidx]\n\t" \
    "v_add_f32 v196, v196, v198\n\t" \
    "v_smfmac_f32_16x16x64_bf16 v[200:203], v[184:187], a[216:223], %[sidx]\n\t" \
    "v_add_f32 v197, v197, v199\n\t" \
    "v_smfmac_f32_16x16x64_bf16 v[200:203], v[188:191], a[224:231], %[sidx]\n\t" \
    "v_cvt_pk_bf16_f32 v247, v196, v197\n\t" \
    "v_smfmac_f32_16x16x64_bf16 v[204:207], v[172:175], a[232:239], %[sidx]\n\t" \
    "ds_write_b32 %[qkw], v247 offset:16\n\t" \
    "ds_write_b32 %[flw], v254\n\t" \
    "v_smfmac_f32_16x16x64_bf16 v[204:207], v[176:179], a[240:247], %[sidx]\n\t" \
    "s_cmp_lt_u32 s40, 63\n\t" \
    "s_cselect_b32 s54, 393216, 0\n\t" \
    "s_add_u32 s50, s50, s54\n\t" \
    "s_add_u32 s51, s50, 6144\n\t" \
    "buffer_load_dwordx4 v[216:219], %[kxo], %[srd], s50 offen offset:0 sc1\n\t" \
    "buffer_load_dwordx4 v[220:223], %[kxo], %[srd], s51 offen offset:0 sc1\n\t" \
    "buffer_load_dwordx4 v[224:227], %[kxo], %[srd], s50 offen offset:1024 sc1\n\t" \
    "buffer_load_dwordx4 v[228:231], %[kxo], %[srd], s51 offen offset:1024 sc1\n\t" \
    "v_smfmac_f32_16x16x64_bf16 v[204:207], v[180:183], a[248:255], %[sidx]\n\t" \
    "v_add_f32 v200, v200, v202\n\t" \
    "v_smfmac_f32_16x16x64_bf16 v[204:207], v[184:187], v[24:31], %[sidx]\n\t" \
    "v_add_f32 v201, v201, v203\n\t" \
    "v_smfmac_f32_16x16x64_bf16 v[204:207], v[188:191], v[32:39], %[sidx]\n\t" \
    "v_cvt_pk_bf16_f32 v246, v200, v201\n\t" \
    "v_smfmac_f32_16x16x64_bf16 v[208:211], v[172:175], v[40:47], %[sidx]\n\t" \
    "ds_write_b32 %[qkw], v246 offset:2560\n\t" \
    "v_smfmac_f32_16x16x64_bf16 v[208:211], v[176:179], v[48:55], %[sidx]\n\t" \
    "v_smfmac_f32_16x16x64_bf16 v[208:211], v[180:183], v[56:63], %[sidx]\n\t" \
    "v_add_f32 v204, v204, v206\n\t" \
    "v_smfmac_f32_16x16x64_bf16 v[208:211], v[184:187], v[64:71], %[sidx]\n\t" \
    "v_add_f32 v205, v205, v207\n\t" \
    "v_smfmac_f32_16x16x64_bf16 v[208:211], v[188:191], v[72:79], %[sidx]\n\t" \
    "v_cvt_pk_bf16_f32 v247, v204, v205\n\t" \
    "v_smfmac_f32_16x16x64_bf16 v[212:215], v[172:175], v[80:87], %[sidx]\n\t" \
    "ds_write_b32 %[qkw], v247 offset:2576\n\t" \
    "ds_write_b32 %[flw], v255\n\t" \
    "v_smfmac_f32_16x16x64_bf16 v[212:215], v[176:179], v[88:95], %[sidx]\n\t" \
    "v_smfmac_f32_16x16x64_bf16 v[212:215], v[180:183], v[96:103], %[sidx]\n\t" \
    "v_add_f32 v208, v208, v210\n\t" \
    "v_smfmac_f32_16x16x64_bf16 v[212:215], v[184:187], v[104:111], %[sidx]\n\t" \
    "v_add_f32 v209, v209, v211\n\t" \
    "v_smfmac_f32_16x16x64_bf16 v[212:215], v[188:191], v[112:119], %[sidx]\n\t" \
    "v_cvt_pk_bf16_f32 v246, v208, v209\n\t" \
    "ds_write_b32 %[qkw], v246 offset:5120\n\t" \
    "s_nop 7\n\t" \
    "v_add_f32 v212, v212, v214\n\t" \
    "v_add_f32 v213, v213, v215\n\t" \
    "v_cvt_pk_bf16_f32 v247, v212, v213\n\t" \
    "ds_write_b32 %[qkw], v247 offset:5136\n\t" \
    "s_waitcnt lgkmcnt(0)\n\t" \
    "s_barrier\n\t" \
    "s_cmp_eq_u32 %[tsw], 0\n\t" \
    "s_cbranch_scc1 sk_12_%=\n\t" \
    "s_memrealtime s[58:59]\n\t" \
    "s_waitcnt lgkmcnt(0)\n\t" \
    "s_min_u32 s58, s58, -2\n\t" \
    "v_mov_b32 v246, s58\n\t" \
    "s_nop 0\n\t" \
    "buffer_store_dword v246, %[tso], %[srd], s49 offen sc1\n\t" \
    "sk_12_%=:\n\t" \
    "s_barrier\n\t" \
    "s_waitcnt vmcnt(0)\n\t" \
    "ds_write_b128 %[kxl], v[216:219] offset:0\n\t" \
    "ds_write_b128 %[kxl], v[220:223] offset:1024\n\t" \
    "ds_write_b128 %[kxl], v[224:227] offset:2048\n\t" \
    "ds_write_b128 %[kxl], v[228:231] offset:3072\n\t" \
    "s_add_u32 s57, s40, 2\n\t" \
    "s_and_b32 s54, s57, 0x43\n\t" \
    "s_cmp_lg_u32 s54, 0\n\t" \
    "s_cbranch_scc1 pk_13_%=\n\t" \
    "s_lshl_b32 s57, s57, 5\n\t" \
    "s_mov_b32 s53, 0\n\t" \
    "pa_14_%=:\n\t" \
    "v_mov_b32 v246, 0\n\t" \
    "s_nop 0\n\t" \
    "buffer_load_dword v247, v246, %[srd], s57 offen offset:512 sc1\n\t" \
    "s_waitcnt vmcnt(0)\n\t" \
    "v_readfirstlane_b32 s54, v247\n\t" \
    "s_nop 3\n\t" \
    "s_cmp_ge_u32 s54, 64\n\t" \
    "s_cbranch_scc1 pk_13_%=\n\t" \
    "s_sleep 8\n\t" \
    "s_add_u32 s53, s53, 1\n\t" \
    "s_cmp_gt_u32 s53, 131072\n\t" \
    "s_cbranch_scc1 ab_15_%=\n\t" \
    "s_and_b32 s54, s53, 63\n\t" \
    "s_cmp_lg_u32 s54, 0\n\t" \
    "s_cbranch_scc1 pa_14_%=\n\t" \
    "v_mov_b32 v248, 0\n\t" \
    "s_nop 0\n\t" \
    "buffer_load_dword v249, v248, %[srd], 0 offen sc1\n\t" \
    "s_waitcnt vmcnt(0)\n\t" \
    "v_readfirstlane_b32 s54, v249\n\t" \
    "s_nop 3\n\t" \
    "s_cmp_eq_u32 s54, 0\n\t" \
    "s_cbranch_scc1 pa_14_%=\n\t" \
    "s_branch s_end_%=\n\t" \
    "ab_15_%=:\n\t" \
    "v_mov_b32 v248, 0\n\t" \
    "v_mov_b32 v249, 1\n\t" \
    "s_nop 0\n\t" \
    "buffer_store_dword v249, v248, %[srd], 0 offen sc1\n\t" \
    "s_branch s_end_%=\n\t" \
    "pk_13_%=:\n\t" \
    "ds_read_b128 v[120:123], %[lw] offset:0\n\t" \
    "ds_read_b128 v[124:127], %[lw] offset:1024\n\t" \
    "ds_read_b128 v[128:131], %[lw] offset:2048\n\t" \
    "ds_read_b128 v[132:135], %[lw] offset:3072\n\t" \
    "ds_read_b128 v[136:139], %[lw] offset:4096\n\t" \
    "ds_read_b128 v[140:143], %[lw] offset:5120\n\t" \
    "ds_read_b128 v[144:147], %[lw] offset:6144\n\t" \
    "ds_read_b128 v[148:151], %[lw] offset:7168\n\t" \
    "sg_18_%=:\n\t" \
    "v_max_u32 v246, v244, v245\n\t" \
    "v_cmp_eq_u32 vcc, -1, v246\n\t" \
    "s_cbranch_vccz st_17_%=\n\t" \
    "buffer_load_dwordx2 v[244:245], %[tsa], %[srd], s46 offen sc1\n\t" \
    "s_waitcnt vmcnt(0)\n\t" \
    "s_add_u32 s53, s53, 1\n\t" \
    "s_cmp_gt_u32 s53, 131072\n\t" \
    "s_cbranch_scc1 ab_19_%=\n\t" \
    "s_and_b32 s54, s53, 63\n\t" \
    "s_cmp_lg_u32 s54, 0\n\t" \
    "s_cbranch_scc1 sg_18_%=\n\t" \
    "v_mov_b32 v248, 0\n\t" \
    "s_nop 0\n\t" \
    "buffer_load_dword v249, v248, %[srd], 0 offen sc1\n\t" \
    "s_waitcnt vmcnt(0)\n\t" \
    "v_readfirstlane_b32 s54, v249\n\t" \
    "s_nop 3\n\t" \
    "s_cmp_eq_u32 s54, 0\n\t" \
    "s_cbranch_scc1 sg_18_%=\n\t" \
    "s_branch s_end_%=\n\t" \
    "ab_19_%=:\n\t" \
    "v_mov_b32 v248, 0\n\t" \
    "v_mov_b32 v249, 1\n\t" \
    "s_nop 0\n\t" \
    "buffer_store_dword v249, v248, %[srd], 0 offen sc1\n\t" \
    "s_branch s_end_%=\n\t" \
    "st_17_%=:\n\t" \
    "v_readfirstlane_b32 s54, v244\n\t" \
    "v_readfirstlane_b32 s55, v245\n\t" \
    "s_nop 1\n\t" \
    "v_subrev_u32 v246, s54, v244\n\t" \
    "s_nop 1\n\t" \
    "v_max_i32_dpp v246, v246, v246 row_ror:8 row_mask:0xf bank_mask:0xf\n\t" \
    "s_nop 1\n\t" \
    "v_max_i32_dpp v246, v246, v246 row_ror:4 row_mask:0xf bank_mask:0xf\n\t" \
    "s_nop 1\n\t" \
    "v_max_i32_dpp v246, v246, v246 row_ror:2 row_mask:0xf bank_mask:0xf\n\t" \
    "s_nop 1\n\t" \
    "v_max_i32_dpp v246, v246, v246 row_ror:1 row_mask:0xf bank_mask:0xf\n\t" \
    "s_nop 0\n\t" \
    "v_readfirstlane_b32 s56, v246\n\t" \
    "s_nop 3\n\t" \
    "s_add_u32 s54, s54, s56\n\t" \
    "s_cmp_lt_u32 s40, 3\n\t" \
    "s_cbranch_scc1 su_21_%=\n\t" \
    "s_sub_i32 s56, s54, s42\n\t" \
    "s_cmp_eq_u32 s65, 0\n\t" \
    "s_cbranch_scc1 s1_22_%=\n\t" \
    "s_add_i32 s57, s44, 14\n\t" \
    "s_cmp_gt_i32 s55, s57\n\t" \
    "s_cbranch_scc1 sx_23_%=\n\t" \
    "s_sub_i32 s44, s44, %[decay]\n\t" \
    "s_max_i32 s44, s44, s55\n\t" \
    "s1_22_%=:\n\t" \
    "s_add_i32 s57, s45, 14\n\t" \
    "s_cmp_gt_i32 s56, s57\n\t" \
    "s_cbranch_scc1 sy_24_%=\n\t" \
    "s_sub_i32 s45, s45, %[decay]\n\t" \
    "s_max_i32 s45, s45, s56\n\t" \
    "s_branch su_21_%=\n\t" \
    "sx_23_%=:\n\t" \
    "s_add_i32 s44, s44, 6\n\t" \
    "s_add_i32 s64, s64, 4\n\t" \
    "s_min_i32 s64, s64, 40\n\t" \
    "s_branch su_21_%=\n\t" \
    "sy_24_%=:\n\t" \
    "s_cmp_eq_u32 s65, 0\n\t" \
    "s_cbranch_scc1 su_21_%=\n\t" \
    "s_add_i32 s45, s45, 6\n\t" \
    "s_add_i32 s63, s63, 4\n\t" \
    "s_min_i32 s63, s63, 40\n\t" \
    "su_21_%=:\n\t" \
    "s_min_i32 s44, s44, 300\n\t" \
    "s_min_i32 s45, s45, 300\n\t" \
    "s_mov_b32 s65, 0\n\t" \
    "s_add_u32 s43, s54, %[pena]\n\t" \
    "s_cmp_eq_u32 s40, 0\n\t" \
    "s_cbranch_scc1 se_16_%=\n\t" \
    "s_add_u32 s56, s54, %[tva]\n\t" \
    "s_sub_i32 s56, s41, s56\n\t" \
    "s_cmp_lt_i32 s56, -14\n\t" \
    "s_cbranch_scc1 sl_20_%=\n\t" \
    "s_mov_b32 s43, s41\n\t" \
    "s_mov_b32 s65, 1\n\t" \
    "s_branch se_16_%=\n\t" \
    "sl_20_%=:\n\t" \
    "s_add_u32 s57, s41, 90\n\t" \
    "s_cmp_gt_i32 s56, -30\n\t" \
    "s_cselect_b32 s43, s57, s43\n\t" \
    "se_16_%=:\n\t" \
    "s_add_u32 s42, s43, s44\n\t" \
    "s_add_u32 s42, s42, %[tvb]\n\t" \
    "s_add_u32 s42, s42, s63\n\t" \
    "s_add_u32 s41, s42, s45\n\t" \
    "s_add_u32 s41, s41, %[tva]\n\t" \
    "s_add_u32 s41, s41, s64\n\t" \
    "s_add_u32 s46, s46, 12288\n\t" \
    "s_add_u32 s47, s47, 12288\n\t" \
    "s_add_u32 s48, s48, 12288\n\t" \
    "s_add_u32 s49, s49, 22528\n\t" \
    "s_add_u32 s40, s40, 1\n\t" \
    "s_add_u32 s61, s61, 2\n\t" \
    "s_waitcnt lgkmcnt(0)\n\t" \
    "s_barrier\n\t" \
    "s_cmp_lt_u32 s40, 64\n\t" \
    "s_cbranch_scc1 sl_4_%=\n\t" \
    "s_waitcnt vmcnt(0) lgkmcnt(0)\n\t" \
    "s_barrier\n\t" \
    "s_cmp_lg_u32 %[w], 0\n\t" \
    "s_cbranch_scc1 fz_26_%=\n\t" \
    "v_mov_b32 v246, 1\n\t" \
    "s_nop 0\n\t" \
    "buffer_atomic_add v246, %[fino], %[srd], 0 offen\n\t" \
    "fz_26_%=:\n\t" \
    "s_branch s_end_%=\n\t" \
    "ss_8_%=:\n\t" \
    "s_nop 7\n\t" \
    "s_nop 7\n\t" \
    "s_waitcnt lgkmcnt(0)\n\t" \
    "s_cmp_lg_u32 s62, 0\n\t" \
    "s_cbranch_scc1 so_7_%=\n\t" \
    "s_mov_b32 s62, 1\n\t" \
    "s_waitcnt vmcnt(0)\n\t" \
    "sa_28_%=:\n\t" \
    "buffer_load_dwordx4 v[152:155], %[mlo], %[srd], s46 offen offset:0 sc1\n\t" \
    "s_nop 7\n\t" \
    "buffer_load_dwordx4 v[156:159], %[mlo], %[srd], s46 offen offset:1024 sc1\n\t" \
    "s_nop 7\n\t" \
    "buffer_load_dwordx4 v[160:163], %[mlo], %[srd], s46 offen offset:2048 sc1\n\t" \
    "s_nop 7\n\t" \
    "buffer_load_dwordx4 v[164:167], %[mlo], %[srd], s46 offen offset:3072 sc1\n\t" \
    "s_nop 7\n\t" \
    "buffer_load_dwordx4 v[168:171], %[mlo], %[srd], s47 offen offset:0 sc1\n\t" \
    "s_nop 7\n\t" \
    "buffer_load_dwordx4 v[172:175], %[mlo], %[srd], s47 offen offset:1024 sc1\n\t" \
    "s_nop 7\n\t" \
    "buffer_load_dwordx4 v[176:179], %[mlo], %[srd], s47 offen offset:2048 sc1\n\t" \
    "s_nop 7\n\t" \
    "buffer_load_dwordx4 v[180:183], %[mlo], %[srd], s47 offen offset:3072 sc1\n\t" \
    "s_nop 7\n\t" \
    "buffer_load_dwordx4 v[184:187], %[mlo], %[srd], s48 offen offset:0 sc1\n\t" \
    "s_nop 7\n\t" \
    "buffer_load_dwordx4 v[188:191], %[mlo], %[srd], s48 offen offset:1024 sc1\n\t" \
    "s_nop 7\n\t" \
    "buffer_load_dwordx2 v[244:245], %[tsa], %[srd], s46 offen sc1\n\t" \
    "s_waitcnt vmcnt(0)\n\t" \
    "v_max_u32 v248, v152, v153\n\t" \
    "v_max3_u32 v248, v248, v154, v155\n\t" \
    "v_max3_u32 v248, v248, v156, v157\n\t" \
    "v_max3_u32 v248, v248, v158, v159\n\t" \
    "v_max3_u32 v248, v248, v160, v161\n\t" \
    "v_max3_u32 v248, v248, v162, v163\n\t" \
    "v_max3_u32 v248, v248, v164, v165\n\t" \
    "v_max3_u32 v248, v248, v166, v167\n\t" \
    "v_max3_u32 v248, v248, v168, v169\n\t" \
    "v_max3_u32 v248, v248, v170, v171\n\t" \
    "v_max3_u32 v248, v248, v172, v173\n\t" \
    "v_max3_u32 v248, v248, v174, v175\n\t" \
    "v_max3_u32 v248, v248, v176, v177\n\t" \
    "v_max3_u32 v248, v248, v178, v179\n\t" \
    "v_max3_u32 v248, v248, v180, v181\n\t" \
    "v_max3_u32 v248, v248, v182, v183\n\t" \
    "v_max3_u32 v248, v248, v184, v185\n\t" \
    "v_max3_u32 v248, v248, v186, v187\n\t" \
    "v_max3_u32 v248, v248, v188, v189\n\t" \
    "v_max3_u32 v248, v248, v190, v191\n\t" \
    "v_cmp_eq_u32 vcc, -1, v248\n\t" \
    "s_cbranch_vccz r2_27_%=\n\t" \
    "s_add_u32 s53, s53, 1\n\t" \
    "s_cmp_gt_u32 s53, 131072\n\t" \
    "s_cbranch_scc1 ab_29_%=\n\t" \
    "s_and_b32 s54, s53, 63\n\t" \
    "s_cmp_lg_u32 s54, 0\n\t" \
    "s_cbranch_scc1 sa_28_%=\n\t" \
    "v_mov_b32 v250, 0\n\t" \
    "s_nop 0\n\t" \
    "buffer_load_dword v251, v250, %[srd], 0 offen sc1\n\t" \
    "s_waitcnt vmcnt(0)\n\t" \
    "v_readfirstlane_b32 s54, v251\n\t" \
    "s_nop 3\n\t" \
    "s_cmp_eq_u32 s54, 0\n\t" \
    "s_cbranch_scc1 sa_28_%=\n\t" \
    "s_branch s_end_%=\n\t" \
    "ab_29_%=:\n\t" \
    "v_mov_b32 v250, 0\n\t" \
    "v_mov_b32 v251, 1\n\t" \
    "s_nop 0\n\t" \
    "buffer_store_dword v251, v250, %[srd], 0 offen sc1\n\t" \
    "s_branch s_end_%=\n\t" \
    "r2_27_%=:\n\t" \
    "ds_read_b128 v[120:123], %[lw] offset:0\n\t" \
    "ds_read_b128 v[124:127], %[lw] offset:1024\n\t" \
    "ds_read_b128 v[128:131], %[lw] offset:2048\n\t" \
    "ds_read_b128 v[132:135], %[lw] offset:3072\n\t" \
    "ds_read_b128 v[136:139], %[lw] offset:4096\n\t" \
    "ds_read_b128 v[140:143], %[lw] offset:5120\n\t" \
    "ds_read_b128 v[144:147], %[lw] offset:6144\n\t" \
    "ds_read_b128 v[148:151], %[lw] offset:7168\n\t" \
    "s_waitcnt lgkmcnt(0)\n\t" \
    "s_branch sr_6_%=\n\t" \
    "s_w0_%=:\n\t" \
    "s_waitcnt lgkmcnt(0)\n\t" \
    "s_barrier\n\t" \
    "sl_30_%=:\n\t" \
    "v_mov_b32 v254, s61\n\t" \
    "v_add_u32 v255, 1, v254\n\t" \
    "v_mov_b32 v240, %[mb]\n\t" \
    "v_mov_b32 v241, %[mb]\n\t" \
    "v_mov_b32 v242, %[mb]\n\t" \
    "v_mov_b32 v243, %[mb]\n\t" \
    "ds_read_b128 v[216:219], %[l16] offset:8192\n\t" \
    "ds_read_b128 v[220:223], %[l16] offset:9216\n\t" \
    "ds_read_b128 v[224:227], %[l16] offset:10240\n\t" \
    "ds_read_b128 v[228:231], %[l16] offset:11264\n\t" \
    "s_waitcnt lgkmcnt(0)\n\t" \
    "s_cmp_eq_u32 s40, 0\n\t" \
    "s_cbranch_scc1 sn_31_%=\n\t" \
    "s_memrealtime s[58:59]\n\t" \
    "s_waitcnt lgkmcnt(0)\n\t" \
    "s_sub_i32 s54, s41, s58\n\t" \
    "s_cmp_le_i32 s54, 4\n\t" \
    "s_cbranch_scc1 wd_35_%=\n\t" \
    "s_cmp_gt_i32 s54, 2000\n\t" \
    "s_cbranch_scc1 wd_35_%=\n\t" \
    "w1_36_%=:\n\t" \
    "s_memrealtime s[58:59]\n\t" \
    "s_waitcnt lgkmcnt(0)\n\t" \
    "s_sub_i32 s54, s41, s58\n\t" \
    "s_cmp_gt_i32 s54, 4\n\t" \
    "s_cbranch_scc1 w1_36_%=\n\t" \
    "w2_37_%=:\n\t" \
    "s_cmp_lt_i32 s54, 2\n\t" \
    "s_cbranch_scc1 wd_35_%=\n\t" \
    "s_nop 3\n\t" \
    "s_sub_i32 s54, s54, 1\n\t" \
    "s_branch w2_37_%=\n\t" \
    "wd_35_%=:\n\t" \
    "sn_31_%=:\n\t" \
    "s_mov_b32 s53, 0\n\t" \
    "s_mov_b32 s62, 0\n\t" \
    "buffer_load_dwordx4 v[152:155], %[mlo], %[srd], s46 offen offset:0 sc1\n\t" \
    "s_nop 7\n\t" \
    "buffer_load_dwordx4 v[156:159], %[mlo], %[srd], s46 offen offset:1024 sc1\n\t" \
    "s_nop 7\n\t" \
    "buffer_load_dwordx4 v[160:163], %[mlo], %[srd], s46 offen offset:2048 sc1\n\t" \
    "s_nop 7\n\t" \
    "buffer_load_dwordx4 v[164:167], %[mlo], %[srd], s46 offen offset:3072 sc1\n\t" \
    "s_nop 7\n\t" \
    "buffer_load_dwordx4 v[168:171], %[mlo], %[srd], s47 offen offset:0 sc1\n\t" \
    "s_nop 7\n\t" \
    "buffer_load_dwordx4 v[172:175], %[mlo], %[srd], s47 offen offset:1024 sc1\n\t" \
    "s_nop 7\n\t" \
    "buffer_load_dwordx4 v[176:179], %[mlo], %[srd], s47 offen offset:2048 sc1\n\t" \
    "s_nop 7\n\t" \
    "buffer_load_dwordx4 v[180:183], %[mlo], %[srd], s47 offen offset:3072 sc1\n\t" \
    "s_nop 7\n\t" \
    "buffer_load_dwordx4 v[184:187], %[mlo], %[srd], s48 offen offset:0 sc1\n\t" \
    "s_nop 7\n\t" \
    "buffer_load_dwordx4 v[188:191], %[mlo], %[srd], s48 offen offset:1024 sc1\n\t" \
    "s_nop 7\n\t" \
    "buffer_load_dwordx2 v[244:245], %[tsa], %[srd], s46 offen sc1\n\t" \
    "sr_32_%=:\n\t" \
    "v_mov_b64 v[192:193], 0\n\t" \
    "v_mov_b64 v[194:195], 0\n\t" \
    "v_mov_b64 v[196:197], 0\n\t" \
    "v_mov_b64 v[198:199], 0\n\t" \
    "v_mov_b64 v[200:201], 0\n\t" \
    "v_mov_b64 v[202:203], 0\n\t" \
    "v_mov_b64 v[204:205], 0\n\t" \
    "v_mov_b64 v[206:207], 0\n\t" \
    "v_mov_b64 v[208:209], 0\n\t" \
    "v_mov_b64 v[210:211], 0\n\t" \
    "v_mov_b64 v[212:213], 0\n\t" \
    "v_mov_b64 v[214:215], 0\n\t" \
    "s_waitcnt vmcnt(10)\n\t" \
    "v_smfmac_f32_16x16x64_bf16 v[192:195], v[152:155], v[120:127], %[sidx]\n\t" \
    "ds_read_b128 v[120:123], %[lw] offset:8192\n\t" \
    "ds_read_b128 v[124:127], %[lw] offset:9216\n\t" \
    "v_smfmac_f32_16x16x64_bf16 v[196:199], v[152:155], a[0:7], %[sidx]\n\t" \
    "v_smfmac_f32_16x16x64_bf16 v[200:203], v[152:155], v[128:135], %[sidx]\n\t" \
    "ds_read_b128 v[128:131], %[lw] offset:10240\n\t" \
    "ds_read_b128 v[132:135], %[lw] offset:11264\n\t" \
    "v_smfmac_f32_16x16x64_bf16 v[204:207], v[152:155], a[8:15], %[sidx]\n\t" \
    "v_smfmac_f32_16x16x64_bf16 v[208:211], v[152:155], a[16:23], %[sidx]\n\t" \
    "v_smfmac_f32_16x16x64_bf16 v[212:215], v[152:155], v[136:143], %[sidx]\n\t" \
    "ds_read_b128 v[136:139], %[lw] offset:12288\n\t" \
    "ds_read_b128 v[140:143], %[lw] offset:13312\n\t" \
    "s_waitcnt vmcnt(9)\n\t" \
    "v_smfmac_f32_16x16x64_bf16 v[192:195], v[156:159], a[24:31], %[sidx]\n\t" \
    "v_smfmac_f32_16x16x64_bf16 v[196:199], v[156:159], a[32:39], %[sidx]\n\t" \
    "v_smfmac_f32_16x16x64_bf16 v[200:203], v[156:159], v[144:151], %[sidx]\n\t" \
    "ds_read_b128 v[144:147], %[lw] offset:14336\n\t" \
    "ds_read_b128 v[148:151], %[lw] offset:15360\n\t" \
    "v_smfmac_f32_16x16x64_bf16 v[204:207], v[156:159], a[40:47], %[sidx]\n\t" \
    "v_smfmac_f32_16x16x64_bf16 v[208:211], v[156:159], a[48:55], %[sidx]\n\t" \
    "s_waitcnt lgkmcnt(6)\n\t" \
    "v_smfmac_f32_16x16x64_bf16 v[212:215], v[156:159], v[120:127], %[sidx]\n\t" \
    "ds_read_b128 v[120:123], %[lw] offset:16384\n\t" \
    "ds_read_b128 v[124:127], %[lw] offset:17408\n\t" \
    "s_waitcnt vmcnt(8)\n\t" \
    "v_smfmac_f32_16x16x64_bf16 v[192:195], v[160:163], a[56:63], %[sidx]\n\t" \
    "s_waitcnt lgkmcnt(6)\n\t" \
    "v_smfmac_f32_16x16x64_bf16 v[196:199], v[160:163], v[128:135], %[sidx]\n\t" \
    "ds_read_b128 v[128:131], %[lw] offset:18432\n\t" \
    "ds_read_b128 v[132:135], %[lw] offset:19456\n\t" \
    "v_smfmac_f32_16x16x64_bf16 v[200:203], v[160:163], a[64:71], %[sidx]\n\t" \
    "v_smfmac_f32_16x16x64_bf16 v[204:207], v[160:163], a[72:79], %[sidx]\n\t" \
    "s_waitcnt lgkmcnt(6)\n\t" \
    "v_smfmac_f32_16x16x64_bf16 v[208:211], v[160:163], v[136:143], %[sidx]\n\t" \
    "ds_read_b128 v[136:139], %[lw] offset:20480\n\t" \
    "ds_read_b128 v[140:143], %[lw] offset:21504\n\t" \
    "v_smfmac_f32_16x16x64_bf16 v[212:215], v[160:163], a[80:87], %[sidx]\n\t" \
    "s_waitcnt vmcnt(7)\n\t" \
    "v_smfmac_f32_16x16x64_bf16 v[192:195], v[164:167], a[88:95], %[sidx]\n\t" \
    "s_waitcnt lgkmcnt(6)\n\t" \
    "v_smfmac_f32_16x16x64_bf16 v[196:199], v[164:167], v[144:151], %[sidx]\n\t" \
    "ds_read_b128 v[144:147], %[lw] offset:22528\n\t" \
    "ds_read_b128 v[148:151], %[lw] offset:23552\n\t" \
    "v_smfmac_f32_16x16x64_bf16 v[200:203], v[164:167], a[96:103], %[sidx]\n\t" \
    "v_smfmac_f32_16x16x64_bf16 v[204:207], v[164:167], a[104:111], %[sidx]\n\t" \
    "s_waitcnt lgkmcnt(6)\n\t" \
    "v_smfmac_f32_16x16x64_bf16 v[208:211], v[164:167], v[120:127], %[sidx]\n\t" \
    "ds_read_b128 v[120:123], %[lw] offset:24576\n\t" \
    "ds_read_b128 v[124:127], %[lw] offset:25600\n\t" \
    "v_smfmac_f32_16x16x64_bf16 v[212:215], v[164:167], a[112:119], %[sidx]\n\t" \
    "s_waitcnt vmcnt(6)\n\t" \
    "s_waitcnt lgkmcnt(6)\n\t" \
    "v_smfmac_f32_16x16x64_bf16 v[192:195], v[168:171], v[128:135], %[sidx]\n\t" \
    "ds_read_b128 v[128:131], %[lw] offset:26624\n\t" \
    "ds_read_b128 v[132:135], %[lw] offset:27648\n\t" \
    "v_smfmac_f32_16x16x64_bf16 v[196:199], v[168:171], a[120:127], %[sidx]\n\t" \
    "ds_read_b128 v[152:155], %[l16] offset:12288\n\t" \
    "v_smfmac_f32_16x16x64_bf16 v[200:203], v[168:171], a[128:135], %[sidx]\n\t" \
    "ds_read_b128 v[156:159], %[l16] offset:13312\n\t" \
    "s_waitcnt lgkmcnt(8)\n\t" \
    "v_smfmac_f32_16x16x64_bf16 v[204:207], v[168:171], v[136:143], %[sidx]\n\t" \
    "ds_read_b128 v[136:139], %[lw] offset:28672\n\t" \
    "ds_read_b128 v[140:143], %[lw] offset:29696\n\t" \
    "v_smfmac_f32_16x16x64_bf16 v[208:211], v[168:171], a[136:143], %[sidx]\n\t" \
    "ds_read_b128 v[160:163], %[l16] offset:14336\n\t" \
    "v_smfmac_f32_16x16x64_bf16 v[212:215], v[168:171], a[144:151], %[sidx]\n\t" \
    "ds_read_b128 v[164:167], %[l16] offset:15360\n\t" \
    "s_waitcnt vmcnt(5)\n\t" \
    "s_waitcnt lgkmcnt(10)\n\t" \
    "v_smfmac_f32_16x16x64_bf16 v[192:195], v[172:175], v[144:151], %[sidx]\n\t" \
    "ds_read_b128 v[144:147], %[lw] offset:30720\n\t" \
    "ds_read_b128 v[148:151], %[lw] offset:31744\n\t" \
    "s_waitcnt vmcnt(4)\n\t" \
    "v_smfmac_f32_16x16x64_bf16 v[192:195], v[176:179], a[152:159], %[sidx]\n\t" \
    "s_waitcnt vmcnt(3)\n\t" \
    "v_smfmac_f32_16x16x64_bf16 v[192:195], v[180:183], a[160:167], %[sidx]\n\t" \
    "s_waitcnt vmcnt(2)\n\t" \
    "s_waitcnt lgkmcnt(10)\n\t" \
    "v_smfmac_f32_16x16x64_bf16 v[192:195], v[184:187], v[120:127], %[sidx]\n\t" \
    "s_waitcnt vmcnt(1)\n\t" \
    "v_smfmac_f32_16x16x64_bf16 v[192:195], v[188:191], a[168:175], %[sidx]\n\t" \
    "ds_read_b128 v[120:123], %[l16] offset:16384\n\t" \
    "ds_read_b128 v[124:127], %[l16] offset:17408\n\t" \
    "s_waitcnt lgkmcnt(10)\n\t" \
    "v_smfmac_f32_16x16x64_bf16 v[196:199], v[172:175], v[128:135], %[sidx]\n\t" \
    "v_smfmac_f32_16x16x64_bf16 v[196:199], v[176:179], a[176:183], %[sidx]\n\t" \
    "ds_read_b128 v[128:131], %[l16] offset:18432\n\t" \
    "ds_read_b128 v[132:135], %[l16] offset:19456\n\t" \
    "v_smfmac_f32_16x16x64_bf16 v[196:199], v[180:183], a[184:191], %[sidx]\n\t" \
    "v_add_f32 v192, v192, v194\n\t" \
    "s_waitcnt lgkmcnt(8)\n\t" \
    "v_smfmac_f32_16x16x64_bf16 v[196:199], v[184:187], v[136:143], %[sidx]\n\t" \
    "v_add_f32 v193, v193, v195\n\t" \
    "v_smfmac_f32_16x16x64_bf16 v[196:199], v[188:191], a[192:199], %[sidx]\n\t" \
    "v_cmp_u_f32 vcc, v192, v193\n\t" \
    "v_smfmac_f32_16x16x64_bf16 v[200:203], v[172:175], a[200:207], %[sidx]\n\t" \
    "v_cvt_pk_bf16_f32 v246, v192, v193\n\t" \
    "s_cbranch_vccnz ss_34_%=\n\t" \
    "so_33_%=:\n\t" \
    "s_waitcnt lgkmcnt(4)\n\t" \
    "v_smfmac_f32_16x16x64_bf16 v[200:203], v[176:179], v[144:151], %[sidx]\n\t" \
    "ds_write_b32 %[qkw], v246 offset:0\n\t" \
    "v_smfmac_f32_16x16x64_bf16 v[200:203], v[180:183], a[208:215], %[sidx]\n\t" \
    "v_add_f32 v196, v196, v198\n\t" \
    "v_smfmac_f32_16x16x64_bf16 v[200:203], v[184:187], a[216:223], %[sidx]\n\t" \
    "v_add_f32 v197, v197, v199\n\t" \
    "v_smfmac_f32_16x16x64_bf16 v[200:203], v[188:191], a[224:231], %[sidx]\n\t" \
    "v_cvt_pk_bf16_f32 v247, v196, v197\n\t" \
    "v_smfmac_f32_16x16x64_bf16 v[204:207], v[172:175], a[232:239], %[sidx]\n\t" \
    "ds_write_b32 %[qkw], v247 offset:16\n\t" \
    "ds_write_b32 %[flw], v254\n\t" \
    "v_smfmac_f32_16x16x64_bf16 v[204:207], v[176:179], a[240:247], %[sidx]\n\t" \
    "v_smfmac_f32_16x16x64_bf16 v[204:207], v[180:183], a[248:255], %[sidx]\n\t" \
    "v_add_f32 v200, v200, v202\n\t" \
    "v_smfmac_f32_16x16x64_bf16 v[204:207], v[184:187], v[24:31], %[sidx]\n\t" \
    "v_add_f32 v201, v201, v203\n\t" \
    "v_smfmac_f32_16x16x64_bf16 v[204:207], v[188:191], v[32:39], %[sidx]\n\t" \
    "v_cvt_pk_bf16_f32 v246, v200, v201\n\t" \
    "v_smfmac_f32_16x16x64_bf16 v[208:211], v[172:175], v[40:47], %[sidx]\n\t" \
    "ds_write_b32 %[qkw], v246 offset:2560\n\t" \
    "v_smfmac_f32_16x16x64_bf16 v[208:211], v[176:179], v[48:55], %[sidx]\n\t" \
    "ds_read_b128 v[168:171], %[flr]\n\t" \
    "ds_read_b64_tr_b16 v[192:193], %[qkr] offset:0\n\t" \
    "ds_read_b64_tr_b16 v[194:195], %[qkr] offset:160\n\t" \
    "v_smfmac_f32_16x16x64_bf16 v[208:211], v[180:183], v[56:63], %[sidx]\n\t" \
    "v_add_f32 v204, v204, v206\n\t" \
    "v_smfmac_f32_16x16x64_bf16 v[208:211], v[184:187], v[64:71], %[sidx]\n\t" \
    "v_add_f32 v205, v205, v207\n\t" \
    "v_smfmac_f32_16x16x64_bf16 v[208:211], v[188:191], v[72:79], %[sidx]\n\t" \
    "v_cvt_pk_bf16_f32 v247, v204, v205\n\t" \
    "v_smfmac_f32_16x16x64_bf16 v[212:215], v[172:175], v[80:87], %[sidx]\n\t" \
    "ds_write_b32 %[qkw], v247 offset:2576\n\t" \
    "ds_write_b32 %[flw], v255\n\t" \
    "v_smfmac_f32_16x16x64_bf16 v[212:215], v[176:179], v[88:95], %[sidx]\n\t" \
    "ds_read_b64_tr_b16 v[196:197], %[qkr] offset:1280\n\t" \
    "ds_read_b64_tr_b16 v[198:199], %[qkr] offset:1440\n\t" \
    "v_smfmac_f32_16x16x64_bf16 v[212:215], v[180:183], v[96:103], %[sidx]\n\t" \
    "v_add_f32 v208, v208, v210\n\t" \
    "v_smfmac_f32_16x16x64_bf16 v[212:215], v[184:187], v[104:111], %[sidx]\n\t" \
    "v_add_f32 v209, v209, v211\n\t" \
    "v_smfmac_f32_16x16x64_bf16 v[212:215], v[188:191], v[112:119], %[sidx]\n\t" \
    "v_cvt_pk_bf16_f32 v246, v208, v209\n\t" \
    "ds_write_b32 %[qkw], v246 offset:5120\n\t" \
    "fr_39_%=:\n\t" \
    "s_waitcnt lgkmcnt(7)\n\t" \
    "v_min3_u32 v168, v169, v170, v171\n\t" \
    "v_cmp_gt_u32 vcc, s61, v168\n\t" \
    "s_cbranch_vccz fo_38_%=\n\t" \
    "s_waitcnt lgkmcnt(0)\n\t" \
    "ds_read_b128 v[168:171], %[flr]\n\t" \
    "ds_read_b64_tr_b16 v[192:193], %[qkr] offset:0\n\t" \
    "ds_read_b64_tr_b16 v[194:195], %[qkr] offset:160\n\t" \
    "ds_read_b64_tr_b16 v[196:197], %[qkr] offset:1280\n\t" \
    "ds_read_b64_tr_b16 v[198:199], %[qkr] offset:1440\n\t" \
    "s_waitcnt lgkmcnt(0)\n\t" \
    "s_add_u32 s53, s53, 1\n\t" \
    "s_cmp_gt_u32 s53, 131072\n\t" \
    "s_cbranch_scc1 ab_40_%=\n\t" \
    "s_and_b32 s54, s53, 63\n\t" \
    "s_cmp_lg_u32 s54, 0\n\t" \
    "s_cbranch_scc1 fr_39_%=\n\t" \
    "v_mov_b32 v248, 0\n\t" \
    "s_nop 0\n\t" \
    "buffer_load_dword v249, v248, %[srd], 0 offen sc1\n\t" \
    "s_waitcnt vmcnt(0)\n\t" \
    "v_readfirstlane_b32 s54, v249\n\t" \
    "s_nop 3\n\t" \
    "s_cmp_eq_u32 s54, 0\n\t" \
    "s_cbranch_scc1 fr_39_%=\n\t" \
    "s_branch s_end_%=\n\t" \
    "ab_40_%=:\n\t" \
    "v_mov_b32 v248, 0\n\t" \
    "v_mov_b32 v249, 1\n\t" \
    "s_nop 0\n\t" \
    "buffer_store_dword v249, v248, %[srd], 0 offen sc1\n\t" \
    "s_branch s_end_%=\n\t" \
    "fo_38_%=:\n\t" \
    "s_waitcnt lgkmcnt(5)\n\t" \
    "v_mfma_f32_16x16x32_bf16 v[232:235], v[216:219], v[192:195], 0\n\t" \
    "ds_read_b128 v[172:175], %[flr]\n\t" \
    "ds_read_b64_tr_b16 v[200:201], %[qkr] offset:2560\n\t" \
    "ds_read_b64_tr_b16 v[202:203], %[qkr] offset:2720\n\t" \
    "v_mfma_f32_16x16x32_bf16 v[236:239], v[220:223], v[192:195], 0\n\t" \
    "v_add_f32 v212, v212, v214\n\t" \
    "v_add_f32 v213, v213, v215\n\t" \
    "v_mfma_f32_16x16x32_bf16 v[240:243], v[192:195], v[192:195], v[240:243]\n\t" \
    "v_cvt_pk_bf16_f32 v247, v212, v213\n\t" \
    "ds_write_b32 %[qkw], v247 offset:5136\n\t" \
    "s_waitcnt lgkmcnt(5)\n\t" \
    "v_mfma_f32_16x16x32_bf16 v[232:235], v[224:227], v[196:199], v[232:235]\n\t" \
    "ds_read_b64_tr_b16 v[204:205], %[qkr] offset:3840\n\t" \
    "ds_read_b64_tr_b16 v[206:207], %[qkr] offset:4000\n\t" \
    "v_mfma_f32_16x16x32_bf16 v[236:239], v[228:231], v[196:199], v[236:239]\n\t" \
    "s_waitcnt lgkmcnt(5)\n\t" \
    "v_min3_u32 v172, v173, v174, v175\n\t" \
    "v_cmp_gt_u32 vcc, v255, v172\n\t" \
    "v_mfma_f32_16x16x32_bf16 v[240:243], v[196:199], v[196:199], v[240:243]\n\t" \
    "s_waitcnt lgkmcnt(2)\n\t" \
    "s_barrier\n\t" \
    "s_cbranch_vccz bo_41_%=\n\t" \
    "s_waitcnt lgkmcnt(0)\n\t" \
    "ds_read_b64_tr_b16 v[200:201], %[qkr] offset:2560\n\t" \
    "ds_read_b64_tr_b16 v[202:203], %[qkr] offset:2720\n\t" \
    "ds_read_b64_tr_b16 v[204:205], %[qkr] offset:3840\n\t" \
    "ds_read_b64_tr_b16 v[206:207], %[qkr] offset:4000\n\t" \
    "s_waitcnt lgkmcnt(0)\n\t" \
    "bo_41_%=:\n\t" \
    "ds_read_b64_tr_b16 v[208:209], %[qkr] offset:5120\n\t" \
    "ds_read_b64_tr_b16 v[210:211], %[qkr] offset:5280\n\t" \
    "ds_read_b64_tr_b16 v[212:213], %[qkr] offset:6400\n\t" \
    "ds_read_b64_tr_b16 v[214:215], %[qkr] offset:6560\n\t" \
    "v_mfma_f32_16x16x32_bf16 v[232:235], v[152:155], v[200:203], v[232:235]\n\t" \
    "v_mfma_f32_16x16x32_bf16 v[236:239], v[156:159], v[200:203], v[236:239]\n\t" \
    "v_mfma_f32_16x16x32_bf16 v[240:243], v[200:203], v[200:203], v[240:243]\n\t" \
    "s_waitcnt lgkmcnt(4)\n\t" \
    "v_mfma_f32_16x16x32_bf16 v[232:235], v[160:163], v[204:207], v[232:235]\n\t" \
    "v_mfma_f32_16x16x32_bf16 v[236:239], v[164:167], v[204:207], v[236:239]\n\t" \
    "v_mfma_f32_16x16x32_bf16 v[240:243], v[204:207], v[204:207], v[240:243]\n\t" \
    "s_cmp_eq_u32 s40, 63\n\t" \
    "s_cbranch_scc1 ls_42_%=\n\t" \
    "s_waitcnt lgkmcnt(2)\n\t" \
    "v_mfma_f32_16x16x32_bf16 v[232:235], v[120:123], v[208:211], v[232:235]\n\t" \
    "v_mfma_f32_16x16x32_bf16 v[236:239], v[124:127], v[208:211], v[236:239]\n\t" \
    "v_mfma_f32_16x16x32_bf16 v[240:243], v[208:211], v[208:211], v[240:243]\n\t" \
    "s_waitcnt lgkmcnt(0)\n\t" \
    "v_mfma_f32_16x16x32_bf16 v[232:235], v[128:131], v[212:215], v[232:235]\n\t" \
    "v_mfma_f32_16x16x32_bf16 v[236:239], v[132:135], v[212:215], v[236:239]\n\t" \
    "v_mfma_f32_16x16x32_bf16 v[240:243], v[212:215], v[212:215], v[240:243]\n\t" \
    "s_nop 5\n\t" \
    "v_max3_f32 v246, v232, v233, v234\n\t" \
    "v_max3_f32 v247, v236, v237, v238\n\t" \
    "v_max3_f32 v246, v246, v235, v239\n\t" \
    "v_max3_f32 v247, v247, v240, v241\n\t" \
    "v_max3_f32 v246, v246, v242, v243\n\t" \
    "v_max_f32 v248, v246, v247\n\t" \
    "v_max_f32 v246, v246, v247\n\t" \
    "s_nop 1\n\t" \
    "v_permlane16_swap_b32 v248, v246\n\t" \
    "v_max_f32 v247, v248, v246\n\t" \
    "v_max_f32 v248, v248, v246\n\t" \
    "s_nop 1\n\t" \
    "v_permlane32_swap_b32 v248, v247\n\t" \
    "v_max_f32 v248, v248, v247\n\t" \
    "v_mul_f32 v249, %[nsc], v248\n\t" \
    "v_fma_f32 v232, v232, %[sc], v249\n\t" \
    "v_fma_f32 v233, v233, %[sc], v249\n\t" \
    "v_exp_f32 v232, v232\n\t" \
    "v_fma_f32 v234, v234, %[sc], v249\n\t" \
    "v_exp_f32 v233, v233\n\t" \
    "v_fma_f32 v235, v235, %[sc], v249\n\t" \
    "v_exp_f32 v234, v234\n\t" \
    "v_fma_f32 v236, v236, %[sc], v249\n\t" \
    "v_exp_f32 v235, v235\n\t" \
    "v_fma_f32 v237, v237, %[sc], v249\n\t" \
    "v_exp_f32 v236, v236\n\t" \
    "v_fma_f32 v238, v238, %[sc], v249\n\t" \
    "v_exp_f32 v237, v237\n\t" \
    "v_fma_f32 v239, v239, %[sc], v249\n\t" \
    "v_exp_f32 v238, v238\n\t" \
    "v_fma_f32 v240, v240, %[sc], v249\n\t" \
    "v_exp_f32 v239, v239\n\t" \
    "v_fma_f32 v241, v241, %[sc], v249\n\t" \
    "v_exp_f32 v240, v240\n\t" \
    "v_fma_f32 v242, v242, %[sc], v249\n\t" \
    "v_exp_f32 v241, v241\n\t" \
    "v_fma_f32 v243, v243, %[sc], v249\n\t" \
    "v_exp_f32 v242, v242\n\t" \
    "v_pk_add_f32 v[246:247], v[232:233], v[234:235]\n\t" \
    "v_exp_f32 v243, v243\n\t" \
    "v_pk_add_f32 v[246:247], v[246:247], v[236:237]\n\t" \
    "v_pk_add_f32 v[246:247], v[246:247], v[238:239]\n\t" \
    "v_pk_add_f32 v[246:247], v[246:247], v[240:241]\n\t" \
    "v_pk_add_f32 v[246:247], v[246:247], v[242:243]\n\t" \
    "v_add_f32 v248, v246, v247\n\t" \
    "v_add_f32 v246, v246, v247\n\t" \
    "s_nop 1\n\t" \
    "v_permlane16_swap_b32 v248, v246\n\t" \
    "v_add_f32 v247, v248, v246\n\t" \
    "v_add_f32 v248, v248, v246\n\t" \
    "s_nop 1\n\t" \
    "v_permlane32_swap_b32 v248, v247\n\t" \
    "v_add_f32 v248, v248, v247\n\t" \
    "v_rcp_f32 v248, v248\n\t" \
    "s_nop 0\n\t" \
    "v_pk_mul_f32 v[232:233], v[232:233], v[248:249] op_sel_hi:[1,0]\n\t" \
    "v_pk_mul_f32 v[234:235], v[234:235], v[248:249] op_sel_hi:[1,0]\n\t" \
    "v_cvt_pk_bf16_f32 v232, |v232|, |v233|\n\t" \
    "v_cvt_pk_bf16_f32 v233, |v234|, |v235|\n\t" \
    "buffer_store_dwordx2 v[232:233], %[pox], %[srd], s49 offen offset:0 sc1\n\t" \
    "v_pk_mul_f32 v[236:237], v[236:237], v[248:249] op_sel_hi:[1,0]\n\t" \
    "v_pk_mul_f32 v[238:239], v[238:239], v[248:249] op_sel_hi:[1,0]\n\t" \
    "v_cvt_pk_bf16_f32 v236, |v236|, |v237|\n\t" \
    "v_cvt_pk_bf16_f32 v237, |v238|, |v239|\n\t" \
    "buffer_store_dwordx2 v[236:237], %[pox], %[srd], s49 offen offset:256 sc1\n\t" \
    "v_pk_mul_f32 v[240:241], v[240:241], v[248:249] op_sel_hi:[1,0]\n\t" \
    "v_pk_mul_f32 v[242:243], v[242:243], v[248:249] op_sel_hi:[1,0]\n\t" \
    "v_cvt_pk_bf16_f32 v240, |v240|, |v241|\n\t" \
    "v_cvt_pk_bf16_f32 v241, |v242|, |v243|\n\t" \
    "buffer_store_dwordx2 v[240:241], %[pom], %[srd], s49 offen offset:0 sc1\n\t" \
    "ls_42_%=:\n\t" \
    "s_waitcnt lgkmcnt(0)\n\t" \
    "buffer_store_dwordx4 v[192:195], %[kmo], %[srd], s52 offen offset:0 sc1\n\t" \
    "buffer_store_dwordx4 v[196:199], %[kmo], %[srd], s52 offen offset:64 sc1\n\t" \
    "buffer_store_dwordx4 v[200:203], %[kmo], %[srd], s52 offen offset:128 sc1\n\t" \
    "buffer_store_dwordx4 v[204:207], %[kmo], %[srd], s52 offen offset:192 sc1\n\t" \
    "buffer_store_dwordx4 v[208:211], %[kmo], %[srd], s52 offen offset:256 sc1\n\t" \
    "buffer_store_dwordx4 v[212:215], %[kmo], %[srd], s52 offen offset:320 sc1\n\t" \
    "s_add_u32 s52, s52, 98304\n\t" \
    "s_barrier\n\t" \
    "ds_read_b128 v[120:123], %[lw] offset:0\n\t" \
    "ds_read_b128 v[124:127], %[lw] offset:1024\n\t" \
    "ds_read_b128 v[128:131], %[lw] offset:2048\n\t" \
    "ds_read_b128 v[132:135], %[lw] offset:3072\n\t" \
    "ds_read_b128 v[136:139], %[lw] offset:4096\n\t" \
    "ds_read_b128 v[140:143], %[lw] offset:5120\n\t" \
    "ds_read_b128 v[144:147], %[lw] offset:6144\n\t" \
    "ds_read_b128 v[148:151], %[lw] offset:7168\n\t" \
    "s_waitcnt vmcnt(9)\n\t" \
    "sg_45_%=:\n\t" \
    "v_max_u32 v246, v244, v245\n\t" \
    "v_cmp_eq_u32 vcc, -1, v246\n\t" \
    "s_cbranch_vccz st_44_%=\n\t" \
    "buffer_load_dwordx2 v[244:245], %[tsa], %[srd], s46 offen sc1\n\t" \
    "s_waitcnt vmcnt(0)\n\t" \
    "s_add_u32 s53, s53, 1\n\t" \
    "s_cmp_gt_u32 s53, 131072\n\t" \
    "s_cbranch_scc1 ab_46_%=\n\t" \
    "s_and_b32 s54, s53, 63\n\t" \
    "s_cmp_lg_u32 s54, 0\n\t" \
    "s_cbranch_scc1 sg_45_%=\n\t" \
    "v_mov_b32 v248, 0\n\t" \
    "s_nop 0\n\t" \
    "buffer_load_dword v249, v248, %[srd], 0 offen sc1\n\t" \
    "s_waitcnt vmcnt(0)\n\t" \
    "v_readfirstlane_b32 s54, v249\n\t" \
    "s_nop 3\n\t" \
    "s_cmp_eq_u32 s54, 0\n\t" \
    "s_cbranch_scc1 sg_45_%=\n\t" \
    "s_branch s_end_%=\n\t" \
    "ab_46_%=:\n\t" \
    "v_mov_b32 v248, 0\n\t" \
    "v_mov_b32 v249, 1\n\t" \
    "s_nop 0\n\t" \
    "buffer_store_dword v249, v248, %[srd], 0 offen sc1\n\t" \
    "s_branch s_end_%=\n\t" \
    "st_44_%=:\n\t" \
    "v_readfirstlane_b32 s54, v244\n\t" \
    "v_readfirstlane_b32 s55, v245\n\t" \
    "s_nop 1\n\t" \
    "v_subrev_u32 v246, s54, v244\n\t" \
    "s_nop 1\n\t" \
    "v_max_i32_dpp v246, v246, v246 row_ror:8 row_mask:0xf bank_mask:0xf\n\t" \
    "s_nop 1\n\t" \
    "v_max_i32_dpp v246, v246, v246 row_ror:4 row_mask:0xf bank_mask:0xf\n\t" \
    "s_nop 1\n\t" \
    "v_max_i32_dpp v246, v246, v246 row_ror:2 row_mask:0xf bank_mask:0xf\n\t" \
    "s_nop 1\n\t" \
    "v_max_i32_dpp v246, v246, v246 row_ror:1 row_mask:0xf bank_mask:0xf\n\t" \
    "s_nop 0\n\t" \
    "v_readfirstlane_b32 s56, v246\n\t" \
    "s_nop 3\n\t" \
    "s_add_u32 s54, s54, s56\n\t" \
    "s_cmp_lt_u32 s40, 3\n\t" \
    "s_cbranch_scc1 su_48_%=\n\t" \
    "s_sub_i32 s56, s54, s42\n\t" \
    "s_cmp_eq_u32 s65, 0\n\t" \
    "s_cbranch_scc1 s1_49_%=\n\t" \
    "s_add_i32 s57, s44, 14\n\t" \
    "s_cmp_gt_i32 s55, s57\n\t" \
    "s_cbranch_scc1 sx_50_%=\n\t" \
    "s_sub_i32 s44, s44, %[decay]\n\t" \
    "s_max_i32 s44, s44, s55\n\t" \
    "s1_49_%=:\n\t" \
    "s_add_i32 s57, s45, 14\n\t" \
    "s_cmp_gt_i32 s56, s57\n\t" \
    "s_cbranch_scc1 sy_51_%=\n\t" \
    "s_sub_i32 s45, s45, %[decay]\n\t" \
    "s_max_i32 s45, s45, s56\n\t" \
    "s_branch su_48_%=\n\t" \
    "sx_50_%=:\n\t" \
    "s_add_i32 s44, s44, 6\n\t" \
    "s_add_i32 s64, s64, 4\n\t" \
    "s_min_i32 s64, s64, 40\n\t" \
    "s_branch su_48_%=\n\t" \
    "sy_51_%=:\n\t" \
    "s_cmp_eq_u32 s65, 0\n\t" \
    "s_cbranch_scc1 su_48_%=\n\t" \
    "s_add_i32 s45, s45, 6\n\t" \
    "s_add_i32 s63, s63, 4\n\t" \
    "s_min_i32 s63, s63, 40\n\t" \
    "su_48_%=:\n\t" \
    "s_min_i32 s44, s44, 300\n\t" \
    "s_min_i32 s45, s45, 300\n\t" \
    "s_mov_b32 s65, 0\n\t" \
    "s_add_u32 s43, s54, %[pena]\n\t" \
    "s_cmp_eq_u32 s40, 0\n\t" \
    "s_cbranch_scc1 se_43_%=\n\t" \
    "s_add_u32 s56, s54, %[tva]\n\t" \
    "s_sub_i32 s56, s41, s56\n\t" \
    "s_cmp_lt_i32 s56, -14\n\t" \
    "s_cbranch_scc1 sl_47_%=\n\t" \
    "s_mov_b32 s43, s41\n\t" \
    "s_mov_b32 s65, 1\n\t" \
    "s_branch se_43_%=\n\t" \
    "sl_47_%=:\n\t" \
    "s_add_u32 s57, s41, 90\n\t" \
    "s_cmp_gt_i32 s56, -30\n\t" \
    "s_cselect_b32 s43, s57, s43\n\t" \
    "se_43_%=:\n\t" \
    "s_add_u32 s42, s43, s44\n\t" \
    "s_add_u32 s42, s42, %[tvb]\n\t" \
    "s_add_u32 s42, s42, s63\n\t" \
    "s_add_u32 s41, s42, s45\n\t" \
    "s_add_u32 s41, s41, %[tva]\n\t" \
    "s_add_u32 s41, s41, s64\n\t" \
    "s_add_u32 s46, s46, 12288\n\t" \
    "s_add_u32 s47, s47, 12288\n\t" \
    "s_add_u32 s48, s48, 12288\n\t" \
    "s_add_u32 s49, s49, 22528\n\t" \
    "s_add_u32 s40, s40, 1\n\t" \
    "s_add_u32 s61, s61, 2\n\t" \
    "s_waitcnt lgkmcnt(0)\n\t" \
    "s_barrier\n\t" \
    "s_cmp_lt_u32 s40, 64\n\t" \
    "s_cbranch_scc1 sl_30_%=\n\t" \
    "s_waitcnt vmcnt(0) lgkmcnt(0)\n\t" \
    "s_barrier\n\t" \
    "s_cmp_lg_u32 %[w], 0\n\t" \
    "s_cbranch_scc1 fz_53_%=\n\t" \
    "v_mov_b32 v246, 1\n\t" \
    "s_nop 0\n\t" \
    "buffer_atomic_add v246, %[fino], %[srd], 0 offen\n\t" \
    "fz_53_%=:\n\t" \
    "s_branch s_end_%=\n\t" \
    "ss_34_%=:\n\t" \
    "s_nop 7\n\t" \
    "s_nop 7\n\t" \
    "s_waitcnt lgkmcnt(0)\n\t" \
    "s_cmp_lg_u32 s62, 0\n\t" \
    "s_cbranch_scc1 so_33_%=\n\t" \
    "s_mov_b32 s62, 1\n\t" \
    "s_waitcnt vmcnt(0)\n\t" \
    "sa_55_%=:\n\t" \
    "buffer_load_dwordx4 v[152:155], %[mlo], %[srd], s46 offen offset:0 sc1\n\t" \
    "s_nop 7\n\t" \
    "buffer_load_dwordx4 v[156:159], %[mlo], %[srd], s46 offen offset:1024 sc1\n\t" \
    "s_nop 7\n\t" \
    "buffer_load_dwordx4 v[160:163], %[mlo], %[srd], s46 offen offset:2048 sc1\n\t" \
    "s_nop 7\n\t" \
    "buffer_load_dwordx4 v[164:167], %[mlo], %[srd], s46 offen offset:3072 sc1\n\t" \
    "s_nop 7\n\t" \
    "buffer_load_dwordx4 v[168:171], %[mlo], %[srd], s47 offen offset:0 sc1\n\t" \
    "s_nop 7\n\t" \
    "buffer_load_dwordx4 v[172:175], %[mlo], %[srd], s47 offen offset:1024 sc1\n\t" \
    "s_nop 7\n\t" \
    "buffer_load_dwordx4 v[176:179], %[mlo], %[srd], s47 offen offset:2048 sc1\n\t" \
    "s_nop 7\n\t" \
    "buffer_load_dwordx4 v[180:183], %[mlo], %[srd], s47 offen offset:3072 sc1\n\t" \
    "s_nop 7\n\t" \
    "buffer_load_dwordx4 v[184:187], %[mlo], %[srd], s48 offen offset:0 sc1\n\t" \
    "s_nop 7\n\t" \
    "buffer_load_dwordx4 v[188:191], %[mlo], %[srd], s48 offen offset:1024 sc1\n\t" \
    "s_nop 7\n\t" \
    "buffer_load_dwordx2 v[244:245], %[tsa], %[srd], s46 offen sc1\n\t" \
    "s_waitcnt vmcnt(0)\n\t" \
    "v_max_u32 v248, v152, v153\n\t" \
    "v_max3_u32 v248, v248, v154, v155\n\t" \
    "v_max3_u32 v248, v248, v156, v157\n\t" \
    "v_max3_u32 v248, v248, v158, v159\n\t" \
    "v_max3_u32 v248, v248, v160, v161\n\t" \
    "v_max3_u32 v248, v248, v162, v163\n\t" \
    "v_max3_u32 v248, v248, v164, v165\n\t" \
    "v_max3_u32 v248, v248, v166, v167\n\t" \
    "v_max3_u32 v248, v248, v168, v169\n\t" \
    "v_max3_u32 v248, v248, v170, v171\n\t" \
    "v_max3_u32 v248, v248, v172, v173\n\t" \
    "v_max3_u32 v248, v248, v174, v175\n\t" \
    "v_max3_u32 v248, v248, v176, v177\n\t" \
    "v_max3_u32 v248, v248, v178, v179\n\t" \
    "v_max3_u32 v248, v248, v180, v181\n\t" \
    "v_max3_u32 v248, v248, v182, v183\n\t" \
    "v_max3_u32 v248, v248, v184, v185\n\t" \
    "v_max3_u32 v248, v248, v186, v187\n\t" \
    "v_max3_u32 v248, v248, v188, v189\n\t" \
    "v_max3_u32 v248, v248, v190, v191\n\t" \
    "v_cmp_eq_u32 vcc, -1, v248\n\t" \
    "s_cbranch_vccz r2_54_%=\n\t" \
    "s_add_u32 s53, s53, 1\n\t" \
    "s_cmp_gt_u32 s53, 131072\n\t" \
    "s_cbranch_scc1 ab_56_%=\n\t" \
    "s_and_b32 s54, s53, 63\n\t" \
    "s_cmp_lg_u32 s54, 0\n\t" \
    "s_cbranch_scc1 sa_55_%=\n\t" \
    "v_mov_b32 v250, 0\n\t" \
    "s_nop 0\n\t" \
    "buffer_load_dword v251, v250, %[srd], 0 offen sc1\n\t" \
    "s_waitcnt vmcnt(0)\n\t" \
    "v_readfirstlane_b32 s54, v251\n\t" \
    "s_nop 3\n\t" \
    "s_cmp_eq_u32 s54, 0\n\t" \
    "s_cbranch_scc1 sa_55_%=\n\t" \
    "s_branch s_end_%=\n\t" \
    "ab_56_%=:\n\t" \
    "v_mov_b32 v250, 0\n\t" \
    "v_mov_b32 v251, 1\n\t" \
    "s_nop 0\n\t" \
    "buffer_store_dword v251, v250, %[srd], 0 offen sc1\n\t" \
    "s_branch s_end_%=\n\t" \
    "r2_54_%=:\n\t" \
    "ds_read_b128 v[120:123], %[lw] offset:0\n\t" \
    "ds_read_b128 v[124:127], %[lw] offset:1024\n\t" \
    "ds_read_b128 v[128:131], %[lw] offset:2048\n\t" \
    "ds_read_b128 v[132:135], %[lw] offset:3072\n\t" \
    "ds_read_b128 v[136:139], %[lw] offset:4096\n\t" \
    "ds_read_b128 v[140:143], %[lw] offset:5120\n\t" \
    "ds_read_b128 v[144:147], %[lw] offset:6144\n\t" \
    "ds_read_b128 v[148:151], %[lw] offset:7168\n\t" \
    "s_waitcnt lgkmcnt(0)\n\t" \
    "s_branch sr_32_%=\n\t" \
    "s_end_%=:\n\t" \
    "s_waitcnt vmcnt(0) lgkmcnt(0)\n\t"
#define RK_ASM_Z \
    "s_mov_b32 s40, 0\n\t" \
    "s_mov_b32 s41, 0\n\t" \
    "s_mov_b32 s42, 0\n\t" \
    "s_mov_b32 s43, 0\n\t" \
    "s_mov_b32 s44, 124\n\t" \
    "s_mov_b32 s45, 88\n\t" \
    "s_mov_b32 s63, 0\n\t" \
    "s_mov_b32 s64, 0\n\t" \
    "s_mov_b32 s65, 0\n\t" \
    "s_mov_b32 s46, %[offm]\n\t" \
    "s_add_u32 s47, s46, 4096\n\t" \
    "s_add_u32 s48, s46, 8192\n\t" \
    "s_mov_b32 s49, 0\n\t" \
    "s_mov_b32 s61, 1\n\t" \
    "s_mov_b32 s60, %[wsoff]\n\t" \
    "buffer_load_dwordx4 a[0:3], %[g16b], %[srd], s60 offen sc1\n\t" \
    "buffer_load_dwordx4 a[4:7], %[g16b], %[srd], s60 offen offset:1024 sc1\n\t" \
    "s_add_u32 s60, s60, 2048\n\t" \
    "buffer_load_dwordx4 a[8:11], %[g16b], %[srd], s60 offen sc1\n\t" \
    "buffer_load_dwordx4 a[12:15], %[g16b], %[srd], s60 offen offset:1024 sc1\n\t" \
    "s_add_u32 s60, s60, 2048\n\t" \
    "buffer_load_dwordx4 a[16:19], %[g16b], %[srd], s60 offen sc1\n\t" \
    "buffer_load_dwordx4 a[20:23], %[g16b], %[srd], s60 offen offset:1024 sc1\n\t" \
    "s_add_u32 s60, s60, 2048\n\t" \
    "buffer_load_dwordx4 a[24:27], %[g16b], %[srd], s60 offen sc1\n\t" \
    "buffer_load_dwordx4 a[28:31], %[g16b], %[srd], s60 offen offset:1024 sc1\n\t" \
    "s_add_u32 s60, s60, 2048\n\t" \
    "buffer_load_dwordx4 a[32:35], %[g16b], %[srd], s60 offen sc1\n\t" \
    "buffer_load_dwordx4 a[36:39], %[g16b], %[srd], s60 offen offset:1024 sc1\n\t" \
    "s_add_u32 s60, s60, 2048\n\t" \
    "buffer_load_dwordx4 a[40:43], %[g16b], %[srd], s60 offen sc1\n\t" \
    "buffer_load_dwordx4 a[44:47], %[g16b], %[srd], s60 offen offset:1024 sc1\n\t" \
    "s_add_u32 s60, s60, 2048\n\t" \
    "buffer_load_dwordx4 a[48:51], %[g16b], %[srd], s60 offen sc1\n\t" \
    "buffer_load_dwordx4 a[52:55], %[g16b], %[srd], s60 offen offset:1024 sc1\n\t" \
    "s_add_u32 s60, s60, 2048\n\t" \
    "buffer_load_dwordx4 a[56:59], %[g16b], %[srd], s60 offen sc1\n\t" \
    "buffer_load_dwordx4 a[60:63], %[g16b], %[srd], s60 offen offset:1024 sc1\n\t" \
    "s_add_u32 s60, s60, 2048\n\t" \
    "buffer_load_dwordx4 a[64:67], %[g16b], %[srd], s60 offen sc1\n\t" \
    "buffer_load_dwordx4 a[68:71], %[g16b], %[srd], s60 offen offset:1024 sc1\n\t" \
    "s_add_u32 s60, s60, 2048\n\t" \
    "buffer_load_dwordx4 a[72:75], %[g16b], %[srd], s60 offen sc1\n\t" \
    "buffer_load_dwordx4 a[76:79], %[g16b], %[srd], s60 offen offset:1024 sc1\n\t" \
    "s_add_u32 s60, s60, 2048\n\t" \
    "buffer_load_dwordx4 a[80:83], %[g16b], %[srd], s60 offen sc1\n\t" \
    "buffer_load_dwordx4 a[84:87], %[g16b], %[srd], s60 offen offset:1024 sc1\n\t" \
    "s_add_u32 s60, s60, 2048\n\t" \
    "buffer_load_dwordx4 a[88:91], %[g16b], %[srd], s60 offen sc1\n\t" \
    "buffer_load_dwordx4 a[92:95], %[g16b], %[srd], s60 offen offset:1024 sc1\n\t" \
    "s_add_u32 s60, s60, 2048\n\t" \
    "buffer_load_dwordx4 a[96:99], %[g16b], %[srd], s60 offen sc1\n\t" \
    "buffer_load_dwordx4 a[100:103], %[g16b], %[srd], s60 offen offset:1024 sc1\n\t" \
    "s_add_u32 s60, s60, 2048\n\t" \
    "buffer_load_dwordx4 a[104:107], %[g16b], %[srd], s60 offen sc1\n\t" \
    "buffer_load_dwordx4 a[108:111], %[g16b], %[srd], s60 offen offset:1024 sc1\n\t" \
    "s_add_u32 s60, s60, 2048\n\t" \
    "buffer_load_dwordx4 a[112:115], %[g16b], %[srd], s60 offen sc1\n\t" \
    "buffer_load_dwordx4 a[116:119], %[g16b], %[srd], s60 offen offset:1024 sc1\n\t" \
    "s_add_u32 s60, s60, 2048\n\t" \
    "buffer_load_dwordx4 a[120:123], %[g16b], %[srd], s60 offen sc1\n\t" \
    "buffer_load_dwordx4 a[124:127], %[g16b], %[srd], s60 offen offset:1024 sc1\n\t" \
    "s_add_u32 s60, s60, 2048\n\t" \
    "buffer_load_dwordx4 a[128:131], %[g16b], %[srd], s60 offen sc1\n\t" \
    "buffer_load_dwordx4 a[132:135], %[g16b], %[srd], s60 offen offset:1024 sc1\n\t" \
    "s_add_u32 s60, s60, 2048\n\t" \
    "buffer_load_dwordx4 a[136:139], %[g16b], %[srd], s60 offen sc1\n\t" \
    "buffer_load_dwordx4 a[140:143], %[g16b], %[srd], s60 offen offset:1024 sc1\n\t" \
    "s_add_u32 s60, s60, 2048\n\t" \
    "buffer_load_dwordx4 a[144:147], %[g16b], %[srd], s60 offen sc1\n\t" \
    "buffer_load_dwordx4 a[148:151], %[g16b], %[srd], s60 offen offset:1024 sc1\n\t" \
    "s_add_u32 s60, s60, 2048\n\t" \
    "buffer_load_dwordx4 a[152:155], %[g16b], %[srd], s60 offen sc1\n\t" \
    "buffer_load_dwordx4 a[156:159], %[g16b], %[srd], s60 offen offset:1024 sc1\n\t" \
    "s_add_u32 s60, s60, 2048\n\t" \
    "buffer_load_dwordx4 a[160:163], %[g16b], %[srd], s60 offen sc1\n\t" \
    "buffer_load_dwordx4 a[164:167], %[g16b], %[srd], s60 offen offset:1024 sc1\n\t" \
    "s_add_u32 s60, s60, 2048\n\t" \
    "buffer_load_dwordx4 a[168:171], %[g16b], %[srd], s60 offen sc1\n\t" \
    "buffer_load_dwordx4 a[172:175], %[g16b], %[srd], s60 offen offset:1024 sc1\n\t" \
    "s_add_u32 s60, s60, 2048\n\t" \
    "buffer_load_dwordx4 a[176:179], %[g16b], %[srd], s60 offen sc1\n\t" \
    "buffer_load_dwordx4 a[180:183], %[g16b], %[srd], s60 offen offset:1024 sc1\n\t" \
    "s_add_u32 s60, s60, 2048\n\t" \
    "buffer_load_dwordx4 a[184:187], %[g16b], %[srd], s60 offen sc1\n\t" \
    "buffer_load_dwordx4 a[188:191], %[g16b], %[srd], s60 offen offset:1024 sc1\n\t" \
    "s_add_u32 s60, s60, 2048\n\t" \
    "buffer_load_dwordx4 a[192:195], %[g16b], %[srd], s60 offen sc1\n\t" \
    "buffer_load_dwordx4 a[196:199], %[g16b], %[srd], s60 offen offset:1024 sc1\n\t" \
    "s_add_u32 s60, s60, 2048\n\t" \
    "buffer_load_dwordx4 a[200:203], %[g16b], %[srd], s60 offen sc1\n\t" \
    "buffer_load_dwordx4 a[204:207], %[g16b], %[srd], s60 offen offset:1024 sc1\n\t" \
    "s_add_u32 s60, s60, 2048\n\t" \
    "buffer_load_dwordx4 a[208:211], %[g16b], %[srd], s60 offen sc1\n\t" \
    "buffer_load_dwordx4 a[212:215], %[g16b], %[srd], s60 offen offset:1024 sc1\n\t" \
    "s_add_u32 s60, s60, 2048\n\t" \
    "buffer_load_dwordx4 a[216:219], %[g16b], %[srd], s60 offen sc1\n\t" \
    "buffer_load_dwordx4 a[220:223], %[g16b], %[srd], s60 offen offset:1024 sc1\n\t" \
    "s_add_u32 s60, s60, 2048\n\t" \
    "buffer_load_dwordx4 a[224:227], %[g16b], %[srd], s60 offen sc1\n\t" \
    "buffer_load_dwordx4 a[228:231], %[g16b], %[srd], s60 offen offset:1024 sc1\n\t" \
    "s_add_u32 s60, s60, 2048\n\t" \
    "buffer_load_dwordx4 a[232:235], %[g16b], %[srd], s60 offen sc1\n\t" \
    "buffer_load_dwordx4 a[236:239], %[g16b], %[srd], s60 offen offset:1024 sc1\n\t" \
    "s_add_u32 s60, s60, 2048\n\t" \
    "buffer_load_dwordx4 a[240:243], %[g16b], %[srd], s60 offen sc1\n\t" \
    "buffer_load_dwordx4 a[244:247], %[g16b], %[srd], s60 offen offset:1024 sc1\n\t" \
    "s_add_u32 s60, s60, 2048\n\t" \
    "s_waitcnt vmcnt(32)\n\t" \
    "buffer_load_dwordx4 a[248:251], %[g16b], %[srd], s60 offen sc1\n\t" \
    "buffer_load_dwordx4 a[252:255], %[g16b], %[srd], s60 offen offset:1024 sc1\n\t" \
    "s_add_u32 s60, s60, 2048\n\t" \
    "buffer_load_dwordx4 v[24:27], %[g16b], %[srd], s60 offen sc1\n\t" \
    "buffer_load_dwordx4 v[28:31], %[g16b], %[srd], s60 offen offset:1024 sc1\n\t" \
    "s_add_u32 s60, s60, 2048\n\t" \
    "buffer_load_dwordx4 v[32:35], %[g16b], %[srd], s60 offen sc1\n\t" \
    "buffer_load_dwordx4 v[36:39], %[g16b], %[srd], s60 offen offset:1024 sc1\n\t" \
    "s_add_u32 s60, s60, 2048\n\t" \
    "buffer_load_dwordx4 v[40:43], %[g16b], %[srd], s60 offen sc1\n\t" \
    "buffer_load_dwordx4 v[44:47], %[g16b], %[srd], s60 offen offset:1024 sc1\n\t" \
    "s_add_u32 s60, s60, 2048\n\t" \
    "buffer_load_dwordx4 v[48:51], %[g16b], %[srd], s60 offen sc1\n\t" \
    "buffer_load_dwordx4 v[52:55], %[g16b], %[srd], s60 offen offset:1024 sc1\n\t" \
    "s_add_u32 s60, s60, 2048\n\t" \
    "buffer_load_dwordx4 v[56:59], %[g16b], %[srd], s60 offen sc1\n\t" \
    "buffer_load_dwordx4 v[60:63], %[g16b], %[srd], s60 offen offset:1024 sc1\n\t" \
    "s_add_u32 s60, s60, 2048\n\t" \
    "buffer_load_dwordx4 v[64:67], %[g16b], %[srd], s60 offen sc1\n\t" \
    "buffer_load_dwordx4 v[68:71], %[g16b], %[srd], s60 offen offset:1024 sc1\n\t" \
    "s_add_u32 s60, s60, 2048\n\t" \
    "buffer_load_dwordx4 v[72:75], %[g16b], %[srd], s60 offen sc1\n\t" \
    "buffer_load_dwordx4 v[76:79], %[g16b], %[srd], s60 offen offset:1024 sc1\n\t" \
    "s_add_u32 s60, s60, 2048\n\t" \
    "buffer_load_dwordx4 v[80:83], %[g16b], %[srd], s60 offen sc1\n\t" \
    "buffer_load_dwordx4 v[84:87], %[g16b], %[srd], s60 offen offset:1024 sc1\n\t" \
    "s_add_u32 s60, s60, 2048\n\t" \
    "s_mov_b32 s50, %[offg]\n\t" \
    "s_add_u32 s57, s40, 0\n\t" \
    "s_and_b32 s54, s57, 0x43\n\t" \
    "s_cmp_lg_u32 s54, 0\n\t" \
    "s_cbranch_scc1 pk_1_%=\n\t" \
    "s_lshl_b32 s57, s57, 5\n\t" \
    "s_mov_b32 s53, 0\n\t" \
    "pa_2_%=:\n\t" \
    "v_mov_b32 v206, 0\n\t" \
    "s_nop 0\n\t" \
    "buffer_load_dword v207, v206, %[srd], s57 offen offset:512 sc1\n\t" \
    "s_waitcnt vmcnt(0)\n\t" \
    "v_readfirstlane_b32 s54, v207\n\t" \
    "s_nop 3\n\t" \
    "s_cmp_ge_u32 s54, 64\n\t" \
    "s_cbranch_scc1 pk_1_%=\n\t" \
    "s_sleep 8\n\t" \
    "s_add_u32 s53, s53, 1\n\t" \
    "s_cmp_gt_u32 s53, 131072\n\t" \
    "s_cbranch_scc1 ab_3_%=\n\t" \
    "s_and_b32 s54, s53, 63\n\t" \
    "s_cmp_lg_u32 s54, 0\n\t" \
    "s_cbranch_scc1 pa_2_%=\n\t" \
    "v_mov_b32 v208, 0\n\t" \
    "s_nop 0\n\t" \
    "buffer_load_dword v209, v208, %[srd], 0 offen sc1\n\t" \
    "s_waitcnt vmcnt(0)\n\t" \
    "v_readfirstlane_b32 s54, v209\n\t" \
    "s_nop 3\n\t" \
    "s_cmp_eq_u32 s54, 0\n\t" \
    "s_cbranch_scc1 pa_2_%=\n\t" \
    "s_branch z_end_%=\n\t" \
    "ab_3_%=:\n\t" \
    "v_mov_b32 v208, 0\n\t" \
    "v_mov_b32 v209, 1\n\t" \
    "s_nop 0\n\t" \
    "buffer_store_dword v209, v208, %[srd], 0 offen sc1\n\t" \
    "s_branch z_end_%=\n\t" \
    "pk_1_%=:\n\t" \
    "buffer_load_dwordx4 v[144:147], %[guo], %[srd], s50 offen offset:0 sc1\n\t" \
    "buffer_load_dwordx4 v[148:151], %[guo], %[srd], s50 offen offset:1024 sc1\n\t" \
    "buffer_load_dwordx4 v[152:155], %[guo], %[srd], s50 offen offset:2048 sc1\n\t" \
    "buffer_load_dwordx4 v[156:159], %[guo], %[srd], s50 offen offset:3072 sc1\n\t" \
    "s_waitcnt vmcnt(0)\n\t" \
    "zl_4_%=:\n\t" \
    "s_add_u32 s57, s40, 1\n\t" \
    "s_and_b32 s54, s57, 0x43\n\t" \
    "s_cmp_lg_u32 s54, 0\n\t" \
    "s_cbranch_scc1 pk_10_%=\n\t" \
    "s_lshl_b32 s57, s57, 5\n\t" \
    "s_mov_b32 s53, 0\n\t" \
    "pa_11_%=:\n\t" \
    "v_mov_b32 v206, 0\n\t" \
    "s_nop 0\n\t" \
    "buffer_load_dword v207, v206, %[srd], s57 offen offset:512 sc1\n\t" \
    "s_waitcnt vmcnt(0)\n\t" \
    "v_readfirstlane_b32 s54, v207\n\t" \
    "s_nop 3\n\t" \
    "s_cmp_ge_u32 s54, 64\n\t" \
    "s_cbranch_scc1 pk_10_%=\n\t" \
    "s_sleep 8\n\t" \
    "s_add_u32 s53, s53, 1\n\t" \
    "s_cmp_gt_u32 s53, 131072\n\t" \
    "s_cbranch_scc1 ab_12_%=\n\t" \
    "s_and_b32 s54, s53, 63\n\t" \
    "s_cmp_lg_u32 s54, 0\n\t" \
    "s_cbranch_scc1 pa_11_%=\n\t" \
    "v_mov_b32 v208, 0\n\t" \
    "s_nop 0\n\t" \
    "buffer_load_dword v209, v208, %[srd], 0 offen sc1\n\t" \
    "s_waitcnt vmcnt(0)\n\t" \
    "v_readfirstlane_b32 s54, v209\n\t" \
    "s_nop 3\n\t" \
    "s_cmp_eq_u32 s54, 0\n\t" \
    "s_cbranch_scc1 pa_11_%=\n\t" \
    "s_branch z_end_%=\n\t" \
    "ab_12_%=:\n\t" \
    "v_mov_b32 v208, 0\n\t" \
    "v_mov_b32 v209, 1\n\t" \
    "s_nop 0\n\t" \
    "buffer_store_dword v209, v208, %[srd], 0 offen sc1\n\t" \
    "s_branch z_end_%=\n\t" \
    "pk_10_%=:\n\t" \
    "s_cmp_eq_u32 s40, 0\n\t" \
    "s_cbranch_scc1 zn_6_%=\n\t" \
    "s_add_u32 s57, s41, %[zlag]\n\t" \
    "s_memrealtime s[58:59]\n\t" \
    "s_waitcnt lgkmcnt(0)\n\t" \
    "s_sub_i32 s54, s57, s58\n\t" \
    "s_cmp_le_i32 s54, 4\n\t" \
    "s_cbranch_scc1 wd_13_%=\n\t" \
    "s_cmp_gt_i32 s54, 2000\n\t" \
    "s_cbranch_scc1 wd_13_%=\n\t" \
    "w1_14_%=:\n\t" \
    "s_memrealtime s[58:59]\n\t" \
    "s_waitcnt lgkmcnt(0)\n\t" \
    "s_sub_i32 s54, s57, s58\n\t" \
    "s_cmp_gt_i32 s54, 4\n\t" \
    "s_cbranch_scc1 w1_14_%=\n\t" \
    "w2_15_%=:\n\t" \
    "s_cmp_lt_i32 s54, 2\n\t" \
    "s_cbranch_scc1 wd_13_%=\n\t" \
    "s_nop 3\n\t" \
    "s_sub_i32 s54, s54, 1\n\t" \
    "s_branch w2_15_%=\n\t" \
    "wd_13_%=:\n\t" \
    "zn_6_%=:\n\t" \
    "s_mov_b32 s53, 0\n\t" \
    "s_mov_b32 s62, 0\n\t" \
    "buffer_load_dwordx4 v[88:91], %[mlo], %[srd], s46 offen offset:0 sc1\n\t" \
    "s_nop 7\n\t" \
    "buffer_load_dwordx4 v[92:95], %[mlo], %[srd], s46 offen offset:1024 sc1\n\t" \
    "s_nop 7\n\t" \
    "buffer_load_dwordx4 v[96:99], %[mlo], %[srd], s46 offen offset:2048 sc1\n\t" \
    "s_nop 7\n\t" \
    "buffer_load_dwordx4 v[100:103], %[mlo], %[srd], s46 offen offset:3072 sc1\n\t" \
    "s_nop 7\n\t" \
    "buffer_load_dwordx4 v[104:107], %[mlo], %[srd], s47 offen offset:0 sc1\n\t" \
    "s_nop 7\n\t" \
    "buffer_load_dwordx4 v[108:111], %[mlo], %[srd], s47 offen offset:1024 sc1\n\t" \
    "s_nop 7\n\t" \
    "buffer_load_dwordx4 v[112:115], %[mlo], %[srd], s47 offen offset:2048 sc1\n\t" \
    "s_nop 7\n\t" \
    "buffer_load_dwordx4 v[116:119], %[mlo], %[srd], s47 offen offset:3072 sc1\n\t" \
    "s_nop 7\n\t" \
    "buffer_load_dwordx4 v[120:123], %[mlo], %[srd], s48 offen offset:0 sc1\n\t" \
    "s_nop 7\n\t" \
    "buffer_load_dwordx4 v[124:127], %[mlo], %[srd], s48 offen offset:1024 sc1\n\t" \
    "s_nop 7\n\t" \
    "buffer_load_dwordx2 v[204:205], %[tsa], %[srd], s46 offen sc1\n\t" \
    "s_cmp_lt_u32 s40, 63\n\t" \
    "s_cselect_b32 s54, 1638400, 0\n\t" \
    "s_add_u32 s51, s50, s54\n\t" \
    "zr_7_%=:\n\t" \
    "v_mov_b64 v[128:129], 0\n\t" \
    "v_mov_b64 v[130:131], 0\n\t" \
    "v_mov_b64 v[132:133], 0\n\t" \
    "v_mov_b64 v[134:135], 0\n\t" \
    "v_mov_b64 v[136:137], 0\n\t" \
    "v_mov_b64 v[138:139], 0\n\t" \
    "v_mov_b64 v[140:141], 0\n\t" \
    "v_mov_b64 v[142:143], 0\n\t" \
    "s_waitcnt vmcnt(10)\n\t" \
    "v_smfmac_f32_16x16x64_bf16 v[128:131], v[88:91], a[0:7], %[sidx]\n\t" \
    "v_smfmac_f32_16x16x64_bf16 v[132:135], v[88:91], a[80:87], %[sidx]\n\t" \
    "v_smfmac_f32_16x16x64_bf16 v[136:139], v[88:91], a[160:167], %[sidx]\n\t" \
    "v_smfmac_f32_16x16x64_bf16 v[140:143], v[88:91], a[240:247], %[sidx]\n\t" \
    "s_waitcnt vmcnt(9)\n\t" \
    "v_smfmac_f32_16x16x64_bf16 v[128:131], v[92:95], a[8:15], %[sidx]\n\t" \
    "v_smfmac_f32_16x16x64_bf16 v[132:135], v[92:95], a[88:95], %[sidx]\n\t" \
    "v_smfmac_f32_16x16x64_bf16 v[136:139], v[92:95], a[168:175], %[sidx]\n\t" \
    "v_smfmac_f32_16x16x64_bf16 v[140:143], v[92:95], a[248:255], %[sidx]\n\t" \
    "s_waitcnt vmcnt(8)\n\t" \
    "v_smfmac_f32_16x16x64_bf16 v[128:131], v[96:99], a[16:23], %[sidx]\n\t" \
    "v_smfmac_f32_16x16x64_bf16 v[132:135], v[96:99], a[96:103], %[sidx]\n\t" \
    "v_smfmac_f32_16x16x64_bf16 v[136:139], v[96:99], a[176:183], %[sidx]\n\t" \
    "v_smfmac_f32_16x16x64_bf16 v[140:143], v[96:99], v[24:31], %[sidx]\n\t" \
    "s_waitcnt vmcnt(7)\n\t" \
    "v_smfmac_f32_16x16x64_bf16 v[128:131], v[100:103], a[24:31], %[sidx]\n\t" \
    "v_smfmac_f32_16x16x64_bf16 v[132:135], v[100:103], a[104:111], %[sidx]\n\t" \
    "v_smfmac_f32_16x16x64_bf16 v[136:139], v[100:103], a[184:191], %[sidx]\n\t" \
    "v_smfmac_f32_16x16x64_bf16 v[140:143], v[100:103], v[32:39], %[sidx]\n\t" \
    "s_waitcnt vmcnt(6)\n\t" \
    "v_smfmac_f32_16x16x64_bf16 v[128:131], v[104:107], a[32:39], %[sidx]\n\t" \
    "v_smfmac_f32_16x16x64_bf16 v[132:135], v[104:107], a[112:119], %[sidx]\n\t" \
    "v_smfmac_f32_16x16x64_bf16 v[136:139], v[104:107], a[192:199], %[sidx]\n\t" \
    "v_smfmac_f32_16x16x64_bf16 v[140:143], v[104:107], v[40:47], %[sidx]\n\t" \
    "s_waitcnt vmcnt(5)\n\t" \
    "v_smfmac_f32_16x16x64_bf16 v[128:131], v[108:111], a[40:47], %[sidx]\n\t" \
    "v_smfmac_f32_16x16x64_bf16 v[132:135], v[108:111], a[120:127], %[sidx]\n\t" \
    "v_smfmac_f32_16x16x64_bf16 v[136:139], v[108:111], a[200:207], %[sidx]\n\t" \
    "v_smfmac_f32_16x16x64_bf16 v[140:143], v[108:111], v[48:55], %[sidx]\n\t" \
    "s_waitcnt vmcnt(4)\n\t" \
    "v_smfmac_f32_16x16x64_bf16 v[128:131], v[112:115], a[48:55], %[sidx]\n\t" \
    "v_smfmac_f32_16x16x64_bf16 v[132:135], v[112:115], a[128:135], %[sidx]\n\t" \
    "v_smfmac_f32_16x16x64_bf16 v[136:139], v[112:115], a[208:215], %[sidx]\n\t" \
    "v_smfmac_f32_16x16x64_bf16 v[140:143], v[112:115], v[56:63], %[sidx]\n\t" \
    "s_waitcnt vmcnt(3)\n\t" \
    "v_smfmac_f32_16x16x64_bf16 v[128:131], v[116:119], a[56:63], %[sidx]\n\t" \
    "v_smfmac_f32_16x16x64_bf16 v[132:135], v[116:119], a[136:143], %[sidx]\n\t" \
    "v_smfmac_f32_16x16x64_bf16 v[136:139], v[116:119], a[216:223], %[sidx]\n\t" \
    "v_smfmac_f32_16x16x64_bf16 v[140:143], v[116:119], v[64:71], %[sidx]\n\t" \
    "s_waitcnt vmcnt(2)\n\t" \
    "v_smfmac_f32_16x16x64_bf16 v[128:131], v[120:123], a[64:71], %[sidx]\n\t" \
    "v_smfmac_f32_16x16x64_bf16 v[132:135], v[120:123], a[144:151], %[sidx]\n\t" \
    "v_smfmac_f32_16x16x64_bf16 v[136:139], v[120:123], a[224:231], %[sidx]\n\t" \
    "v_smfmac_f32_16x16x64_bf16 v[140:143], v[120:123], v[72:79], %[sidx]\n\t" \
    "s_waitcnt vmcnt(1)\n\t" \
    "v_smfmac_f32_16x16x64_bf16 v[128:131], v[124:127], a[72:79], %[sidx]\n\t" \
    "v_smfmac_f32_16x16x64_bf16 v[132:135], v[124:127], a[152:159], %[sidx]\n\t" \
    "v_smfmac_f32_16x16x64_bf16 v[136:139], v[124:127], a[232:239], %[sidx]\n\t" \
    "v_smfmac_f32_16x16x64_bf16 v[140:143], v[124:127], v[80:87], %[sidx]\n\t" \
    "s_nop 7\n\t" \
    "v_add_f32 v128, v128, v130\n\t" \
    "v_add_f32 v129, v129, v131\n\t" \
    "v_cmp_u_f32 vcc, v128, v129\n\t" \
    "s_cbranch_vccnz zs_9_%=\n\t" \
    "zo_8_%=:\n\t" \
    "v_cvt_pk_bf16_f32 v128, v128, v129\n\t" \
    "ds_write_b32 %[ztw], v128 offset:0\n\t" \
    "v_add_f32 v132, v132, v134\n\t" \
    "v_add_f32 v133, v133, v135\n\t" \
    "v_cvt_pk_bf16_f32 v132, v132, v133\n\t" \
    "ds_write_b32 %[ztw], v132 offset:256\n\t" \
    "v_add_f32 v136, v136, v138\n\t" \
    "v_add_f32 v137, v137, v139\n\t" \
    "v_cvt_pk_bf16_f32 v136, v136, v137\n\t" \
    "ds_write_b32 %[ztw], v136 offset:512\n\t" \
    "v_add_f32 v140, v140, v142\n\t" \
    "v_add_f32 v141, v141, v143\n\t" \
    "v_cvt_pk_bf16_f32 v140, v140, v141\n\t" \
    "ds_write_b32 %[ztw], v140 offset:768\n\t" \
    "ds_read_b128 v[176:179], %[ztr]\n\t" \
    "s_waitcnt vmcnt(0)\n\t" \
    "sg_18_%=:\n\t" \
    "v_max_u32 v206, v204, v205\n\t" \
    "v_cmp_eq_u32 vcc, -1, v206\n\t" \
    "s_cbranch_vccz st_17_%=\n\t" \
    "buffer_load_dwordx2 v[204:205], %[tsa], %[srd], s46 offen sc1\n\t" \
    "s_waitcnt vmcnt(0)\n\t" \
    "s_add_u32 s53, s53, 1\n\t" \
    "s_cmp_gt_u32 s53, 131072\n\t" \
    "s_cbranch_scc1 ab_19_%=\n\t" \
    "s_and_b32 s54, s53, 63\n\t" \
    "s_cmp_lg_u32 s54, 0\n\t" \
    "s_cbranch_scc1 sg_18_%=\n\t" \
    "v_mov_b32 v208, 0\n\t" \
    "s_nop 0\n\t" \
    "buffer_load_dword v209, v208, %[srd], 0 offen sc1\n\t" \
    "s_waitcnt vmcnt(0)\n\t" \
    "v_readfirstlane_b32 s54, v209\n\t" \
    "s_nop 3\n\t" \
    "s_cmp_eq_u32 s54, 0\n\t" \
    "s_cbranch_scc1 sg_18_%=\n\t" \
    "s_branch z_end_%=\n\t" \
    "ab_19_%=:\n\t" \
    "v_mov_b32 v208, 0\n\t" \
    "v_mov_b32 v209, 1\n\t" \
    "s_nop 0\n\t" \
    "buffer_store_dword v209, v208, %[srd], 0 offen sc1\n\t" \
    "s_branch z_end_%=\n\t" \
    "st_17_%=:\n\t" \
    "v_readfirstlane_b32 s54, v204\n\t" \
    "v_readfirstlane_b32 s55, v205\n\t" \
    "s_nop 1\n\t" \
    "v_subrev_u32 v206, s54, v204\n\t" \
    "s_nop 1\n\t" \
    "v_max_i32_dpp v206, v206, v206 row_ror:8 row_mask:0xf bank_mask:0xf\n\t" \
    "s_nop 1\n\t" \
    "v_max_i32_dpp v206, v206, v206 row_ror:4 row_mask:0xf bank_mask:0xf\n\t" \
    "s_nop 1\n\t" \
    "v_max_i32_dpp v206, v206, v206 row_ror:2 row_mask:0xf bank_mask:0xf\n\t" \
    "s_nop 1\n\t" \
    "v_max_i32_dpp v206, v206, v206 row_ror:1 row_mask:0xf bank_mask:0xf\n\t" \
    "s_nop 0\n\t" \
    "v_readfirstlane_b32 s56, v206\n\t" \
    "s_nop 3\n\t" \
    "s_add_u32 s54, s54, s56\n\t" \
    "s_cmp_lt_u32 s40, 3\n\t" \
    "s_cbranch_scc1 su_21_%=\n\t" \
    "s_sub_i32 s56, s54, s42\n\t" \
    "s_cmp_eq_u32 s65, 0\n\t" \
    "s_cbranch_scc1 s1_22_%=\n\t" \
    "s_add_i32 s57, s44, 14\n\t" \
    "s_cmp_gt_i32 s55, s57\n\t" \
    "s_cbranch_scc1 sx_23_%=\n\t" \
    "s_sub_i32 s44, s44, %[decay]\n\t" \
    "s_max_i32 s44, s44, s55\n\t" \
    "s1_22_%=:\n\t" \
    "s_add_i32 s57, s45, 14\n\t" \
    "s_cmp_gt_i32 s56, s57\n\t" \
    "s_cbranch_scc1 sy_24_%=\n\t" \
    "s_sub_i32 s45, s45, %[decay]\n\t" \
    "s_max_i32 s45, s45, s56\n\t" \
    "s_branch su_21_%=\n\t" \
    "sx_23_%=:\n\t" \
    "s_add_i32 s44, s44, 6\n\t" \
    "s_add_i32 s64, s64, 4\n\t" \
    "s_min_i32 s64, s64, 40\n\t" \
    "s_branch su_21_%=\n\t" \
    "sy_24_%=:\n\t" \
    "s_cmp_eq_u32 s65, 0\n\t" \
    "s_cbranch_scc1 su_21_%=\n\t" \
    "s_add_i32 s45, s45, 6\n\t" \
    "s_add_i32 s63, s63, 4\n\t" \
    "s_min_i32 s63, s63, 40\n\t" \
    "su_21_%=:\n\t" \
    "s_min_i32 s44, s44, 300\n\t" \
    "s_min_i32 s45, s45, 300\n\t" \
    "s_mov_b32 s65, 0\n\t" \
    "s_add_u32 s43, s54, %[pena]\n\t" \
    "s_cmp_eq_u32 s40, 0\n\t" \
    "s_cbranch_scc1 se_16_%=\n\t" \
    "s_add_u32 s56, s54, %[tva]\n\t" \
    "s_sub_i32 s56, s41, s56\n\t" \
    "s_cmp_lt_i32 s56, -14\n\t" \
    "s_cbranch_scc1 sl_20_%=\n\t" \
    "s_mov_b32 s43, s41\n\t" \
    "s_mov_b32 s65, 1\n\t" \
    "s_branch se_16_%=\n\t" \
    "sl_20_%=:\n\t" \
    "s_add_u32 s57, s41, 90\n\t" \
    "s_cmp_gt_i32 s56, -30\n\t" \
    "s_cselect_b32 s43, s57, s43\n\t" \
    "se_16_%=:\n\t" \
    "s_add_u32 s42, s43, s44\n\t" \
    "s_add_u32 s42, s42, %[tvb]\n\t" \
    "s_add_u32 s42, s42, s63\n\t" \
    "s_add_u32 s41, s42, s45\n\t" \
    "s_add_u32 s41, s41, %[tva]\n\t" \
    "s_add_u32 s41, s41, s64\n\t" \
    "s_waitcnt lgkmcnt(0)\n\t" \
    "buffer_store_dwordx4 v[176:179], %[gzo], %[srd], s50 offen sc1\n\t" \
    "s_mov_b32 s50, s51\n\t" \
    "s_memrealtime s[58:59]\n\t" \
    "s_waitcnt lgkmcnt(0)\n\t" \
    "s_sub_i32 s54, s42, s58\n\t" \
    "s_cmp_le_i32 s54, 4\n\t" \
    "s_cbranch_scc1 wd_26_%=\n\t" \
    "s_cmp_gt_i32 s54, 2000\n\t" \
    "s_cbranch_scc1 wd_26_%=\n\t" \
    "w1_27_%=:\n\t" \
    "s_memrealtime s[58:59]\n\t" \
    "s_waitcnt lgkmcnt(0)\n\t" \
    "s_sub_i32 s54, s42, s58\n\t" \
    "s_cmp_gt_i32 s54, 4\n\t" \
    "s_cbranch_scc1 w1_27_%=\n\t" \
    "w2_28_%=:\n\t" \
    "s_cmp_lt_i32 s54, 2\n\t" \
    "s_cbranch_scc1 wd_26_%=\n\t" \
    "s_nop 3\n\t" \
    "s_sub_i32 s54, s54, 1\n\t" \
    "s_branch w2_28_%=\n\t" \
    "wd_26_%=:\n\t" \
    "s_mov_b32 s53, 0\n\t" \
    "buffer_load_dwordx4 v[180:183], %[pofx], %[srd], s49 offen offset:0 sc1\n\t" \
    "buffer_load_dwordx4 v[184:187], %[pofx], %[srd], s49 offen offset:1024 sc1\n\t" \
    "buffer_load_dwordx4 v[188:191], %[pofx], %[srd], s49 offen offset:2048 sc1\n\t" \
    "buffer_load_dwordx4 v[192:195], %[pofx], %[srd], s49 offen offset:3072 sc1\n\t" \
    "buffer_load_dwordx4 v[196:199], %[pofm], %[srd], s49 offen sc1\n\t" \
    "buffer_load_dword v207, %[poft], %[srd], s49 offen sc1\n\t" \
    "zq_29_%=:\n\t" \
    "s_waitcnt vmcnt(5)\n\t" \
    "v_mfma_f32_16x16x32_bf16 v[200:203], v[144:147], v[180:183], 0\n\t" \
    "s_waitcnt vmcnt(4)\n\t" \
    "v_mfma_f32_16x16x32_bf16 v[200:203], v[148:151], v[184:187], v[200:203]\n\t" \
    "s_waitcnt vmcnt(3)\n\t" \
    "v_mfma_f32_16x16x32_bf16 v[200:203], v[152:155], v[188:191], v[200:203]\n\t" \
    "s_waitcnt vmcnt(2)\n\t" \
    "v_mfma_f32_16x16x32_bf16 v[200:203], v[156:159], v[192:195], v[200:203]\n\t" \
    "s_waitcnt vmcnt(1)\n\t" \
    "v_mfma_f32_16x16x32_bf16 v[200:203], v[176:179], v[196:199], v[200:203]\n\t" \
    "s_nop 7\n\t" \
    "ds_write2_b32 %[ppw], v200, v201 offset1:8\n\t" \
    "ds_write2_b32 %[ppw], v202, v203 offset0:16 offset1:24\n\t" \
    "v_cmp_u_f32 vcc, v200, v200\n\t" \
    "s_cbranch_vccnz zt_31_%=\n\t" \
    "zp_30_%=:\n\t" \
    "s_waitcnt lgkmcnt(0)\n\t" \
    "s_barrier\n\t" \
    "s_cmp_lg_u32 %[w], 0\n\t" \
    "s_cbranch_scc1 z3_32_%=\n\t" \
    "ds_read_b128 v[180:183], %[ppr]\n\t" \
    "ds_read_b128 v[184:187], %[ppr] offset:16\n\t" \
    "s_waitcnt lgkmcnt(0)\n\t" \
    "v_pk_add_f32 v[180:181], v[180:181], v[182:183]\n\t" \
    "v_pk_add_f32 v[184:185], v[184:185], v[186:187]\n\t" \
    "v_pk_add_f32 v[180:181], v[180:181], v[184:185]\n\t" \
    "v_add_f32 v180, v180, v181\n\t" \
    "s_nop 1\n\t" \
    "v_mov_b32_dpp v181, v180 quad_perm:[1,0,3,2] row_mask:0xf bank_mask:0xf\n\t" \
    "v_cvt_pk_bf16_f32 v180, v180, v181\n\t" \
    "v_min_u32 v180, -2, v180\n\t" \
    "buffer_store_dword v180, %[mo], %[srd], s46 offen sc1\n\t" \
    "s_branch zf_33_%=\n\t" \
    "z3_32_%=:\n\t" \
    "s_cmp_eq_u32 %[tsw], 0\n\t" \
    "s_cbranch_scc1 zf_33_%=\n\t" \
    "s_memrealtime s[58:59]\n\t" \
    "tg_35_%=:\n\t" \
    "s_waitcnt vmcnt(0)\n\t" \
    "v_cmp_eq_u32 vcc, -1, v207\n\t" \
    "s_cbranch_vccz tk_34_%=\n\t" \
    "buffer_load_dword v207, %[poft], %[srd], s49 offen sc1\n\t" \
    "s_add_u32 s53, s53, 1\n\t" \
    "s_cmp_gt_u32 s53, 131072\n\t" \
    "s_cbranch_scc1 ab_36_%=\n\t" \
    "s_and_b32 s54, s53, 63\n\t" \
    "s_cmp_lg_u32 s54, 0\n\t" \
    "s_cbranch_scc1 tg_35_%=\n\t" \
    "v_mov_b32 v208, 0\n\t" \
    "s_nop 0\n\t" \
    "buffer_load_dword v209, v208, %[srd], 0 offen sc1\n\t" \
    "s_waitcnt vmcnt(0)\n\t" \
    "v_readfirstlane_b32 s54, v209\n\t" \
    "s_nop 3\n\t" \
    "s_cmp_eq_u32 s54, 0\n\t" \
    "s_cbranch_scc1 tg_35_%=\n\t" \
    "s_branch z_end_%=\n\t" \
    "ab_36_%=:\n\t" \
    "v_mov_b32 v208, 0\n\t" \
    "v_mov_b32 v209, 1\n\t" \
    "s_nop 0\n\t" \
    "buffer_store_dword v209, v208, %[srd], 0 offen sc1\n\t" \
    "s_branch z_end_%=\n\t" \
    "tk_34_%=:\n\t" \
    "v_subrev_u32 v181, s43, v207\n\t" \
    "s_nop 1\n\t" \
    "v_max_i32_dpp v181, v181, v181 row_ror:8 row_mask:0xf bank_mask:0xf\n\t" \
    "s_nop 1\n\t" \
    "v_max_i32_dpp v181, v181, v181 row_ror:4 row_mask:0xf bank_mask:0xf\n\t" \
    "s_nop 1\n\t" \
    "v_max_i32_dpp v181, v181, v181 row_ror:2 row_mask:0xf bank_mask:0xf\n\t" \
    "s_nop 1\n\t" \
    "v_max_i32_dpp v181, v181, v181 row_ror:1 row_mask:0xf bank_mask:0xf\n\t" \
    "v_max_i32 v181, 0, v181\n\t" \
    "s_waitcnt lgkmcnt(0)\n\t" \
    "s_min_u32 s58, s58, -2\n\t" \
    "v_mov_b32 v180, s58\n\t" \
    "s_nop 0\n\t" \
    "buffer_store_dwordx2 v[180:181], %[tso], %[srd], s46 offen sc1\n\t" \
    "v_mov_b32 v182, s40\n\t" \
    "s_nop 0\n\t" \
    "buffer_store_dword v182, %[prgo], %[srd], 0 offen sc1\n\t" \
    "zf_33_%=:\n\t" \
    "buffer_load_dwordx4 v[160:163], %[guo], %[srd], s50 offen offset:0 sc1\n\t" \
    "buffer_load_dwordx4 v[164:167], %[guo], %[srd], s50 offen offset:1024 sc1\n\t" \
    "buffer_load_dwordx4 v[168:171], %[guo], %[srd], s50 offen offset:2048 sc1\n\t" \
    "buffer_load_dwordx4 v[172:175], %[guo], %[srd], s50 offen offset:3072 sc1\n\t" \
    "s_add_u32 s46, s46, 12288\n\t" \
    "s_add_u32 s47, s47, 12288\n\t" \
    "s_add_u32 s48, s48, 12288\n\t" \
    "s_add_u32 s49, s49, 22528\n\t" \
    "s_add_u32 s40, s40, 1\n\t" \
    "s_add_u32 s61, s61, 2\n\t" \
    "s_waitcnt lgkmcnt(0)\n\t" \
    "s_barrier\n\t" \
    "s_cmp_lt_u32 s40, 64\n\t" \
    "s_cbranch_scc1 zx_37_%=\n\t" \
    "s_branch z_end_%=\n\t" \
    "zx_37_%=:\n\t" \
    "s_add_u32 s57, s40, 1\n\t" \
    "s_and_b32 s54, s57, 0x43\n\t" \
    "s_cmp_lg_u32 s54, 0\n\t" \
    "s_cbranch_scc1 pk_42_%=\n\t" \
    "s_lshl_b32 s57, s57, 5\n\t" \
    "s_mov_b32 s53, 0\n\t" \
    "pa_43_%=:\n\t" \
    "v_mov_b32 v206, 0\n\t" \
    "s_nop 0\n\t" \
    "buffer_load_dword v207, v206, %[srd], s57 offen offset:512 sc1\n\t" \
    "s_waitcnt vmcnt(0)\n\t" \
    "v_readfirstlane_b32 s54, v207\n\t" \
    "s_nop 3\n\t" \
    "s_cmp_ge_u32 s54, 64\n\t" \
    "s_cbranch_scc1 pk_42_%=\n\t" \
    "s_sleep 8\n\t" \
    "s_add_u32 s53, s53, 1\n\t" \
    "s_cmp_gt_u32 s53, 131072\n\t" \
    "s_cbranch_scc1 ab_44_%=\n\t" \
    "s_and_b32 s54, s53, 63\n\t" \
    "s_cmp_lg_u32 s54, 0\n\t" \
    "s_cbranch_scc1 pa_43_%=\n\t" \
    "v_mov_b32 v208, 0\n\t" \
    "s_nop 0\n\t" \
    "buffer_load_dword v209, v208, %[srd], 0 offen sc1\n\t" \
    "s_waitcnt vmcnt(0)\n\t" \
    "v_readfirstlane_b32 s54, v209\n\t" \
    "s_nop 3\n\t" \
    "s_cmp_eq_u32 s54, 0\n\t" \
    "s_cbranch_scc1 pa_43_%=\n\t" \
    "s_branch z_end_%=\n\t" \
    "ab_44_%=:\n\t" \
    "v_mov_b32 v208, 0\n\t" \
    "v_mov_b32 v209, 1\n\t" \
    "s_nop 0\n\t" \
    "buffer_store_dword v209, v208, %[srd], 0 offen sc1\n\t" \
    "s_branch z_end_%=\n\t" \
    "pk_42_%=:\n\t" \
    "s_cmp_eq_u32 s40, 0\n\t" \
    "s_cbranch_scc1 zn_38_%=\n\t" \
    "s_add_u32 s57, s41, %[zlag]\n\t" \
    "s_memrealtime s[58:59]\n\t" \
    "s_waitcnt lgkmcnt(0)\n\t" \
    "s_sub_i32 s54, s57, s58\n\t" \
    "s_cmp_le_i32 s54, 4\n\t" \
    "s_cbranch_scc1 wd_45_%=\n\t" \
    "s_cmp_gt_i32 s54, 2000\n\t" \
    "s_cbranch_scc1 wd_45_%=\n\t" \
    "w1_46_%=:\n\t" \
    "s_memrealtime s[58:59]\n\t" \
    "s_waitcnt lgkmcnt(0)\n\t" \
    "s_sub_i32 s54, s57, s58\n\t" \
    "s_cmp_gt_i32 s54, 4\n\t" \
    "s_cbranch_scc1 w1_46_%=\n\t" \
    "w2_47_%=:\n\t" \
    "s_cmp_lt_i32 s54, 2\n\t" \
    "s_cbranch_scc1 wd_45_%=\n\t" \
    "s_nop 3\n\t" \
    "s_sub_i32 s54, s54, 1\n\t" \
    "s_branch w2_47_%=\n\t" \
    "wd_45_%=:\n\t" \
    "zn_38_%=:\n\t" \
    "s_mov_b32 s53, 0\n\t" \
    "s_mov_b32 s62, 0\n\t" \
    "buffer_load_dwordx4 v[88:91], %[mlo], %[srd], s46 offen offset:0 sc1\n\t" \
    "s_nop 7\n\t" \
    "buffer_load_dwordx4 v[92:95], %[mlo], %[srd], s46 offen offset:1024 sc1\n\t" \
    "s_nop 7\n\t" \
    "buffer_load_dwordx4 v[96:99], %[mlo], %[srd], s46 offen offset:2048 sc1\n\t" \
    "s_nop 7\n\t" \
    "buffer_load_dwordx4 v[100:103], %[mlo], %[srd], s46 offen offset:3072 sc1\n\t" \
    "s_nop 7\n\t" \
    "buffer_load_dwordx4 v[104:107], %[mlo], %[srd], s47 offen offset:0 sc1\n\t" \
    "s_nop 7\n\t" \
    "buffer_load_dwordx4 v[108:111], %[mlo], %[srd], s47 offen offset:1024 sc1\n\t" \
    "s_nop 7\n\t" \
    "buffer_load_dwordx4 v[112:115], %[mlo], %[srd], s47 offen offset:2048 sc1\n\t" \
    "s_nop 7\n\t" \
    "buffer_load_dwordx4 v[116:119], %[mlo], %[srd], s47 offen offset:3072 sc1\n\t" \
    "s_nop 7\n\t" \
    "buffer_load_dwordx4 v[120:123], %[mlo], %[srd], s48 offen offset:0 sc1\n\t" \
    "s_nop 7\n\t" \
    "buffer_load_dwordx4 v[124:127], %[mlo], %[srd], s48 offen offset:1024 sc1\n\t" \
    "s_nop 7\n\t" \
    "buffer_load_dwordx2 v[204:205], %[tsa], %[srd], s46 offen sc1\n\t" \
    "s_cmp_lt_u32 s40, 63\n\t" \
    "s_cselect_b32 s54, 1638400, 0\n\t" \
    "s_add_u32 s51, s50, s54\n\t" \
    "zr_39_%=:\n\t" \
    "v_mov_b64 v[128:129], 0\n\t" \
    "v_mov_b64 v[130:131], 0\n\t" \
    "v_mov_b64 v[132:133], 0\n\t" \
    "v_mov_b64 v[134:135], 0\n\t" \
    "v_mov_b64 v[136:137], 0\n\t" \
    "v_mov_b64 v[138:139], 0\n\t" \
    "v_mov_b64 v[140:141], 0\n\t" \
    "v_mov_b64 v[142:143], 0\n\t" \
    "s_waitcnt vmcnt(10)\n\t" \
    "v_smfmac_f32_16x16x64_bf16 v[128:131], v[88:91], a[0:7], %[sidx]\n\t" \
    "v_smfmac_f32_16x16x64_bf16 v[132:135], v[88:91], a[80:87], %[sidx]\n\t" \
    "v_smfmac_f32_16x16x64_bf16 v[136:139], v[88:91], a[160:167], %[sidx]\n\t" \
    "v_smfmac_f32_16x16x64_bf16 v[140:143], v[88:91], a[240:247], %[sidx]\n\t" \
    "s_waitcnt vmcnt(9)\n\t" \
    "v_smfmac_f32_16x16x64_bf16 v[128:131], v[92:95], a[8:15], %[sidx]\n\t" \
    "v_smfmac_f32_16x16x64_bf16 v[132:135], v[92:95], a[88:95], %[sidx]\n\t" \
    "v_smfmac_f32_16x16x64_bf16 v[136:139], v[92:95], a[168:175], %[sidx]\n\t" \
    "v_smfmac_f32_16x16x64_bf16 v[140:143], v[92:95], a[248:255], %[sidx]\n\t" \
    "s_waitcnt vmcnt(8)\n\t" \
    "v_smfmac_f32_16x16x64_bf16 v[128:131], v[96:99], a[16:23], %[sidx]\n\t" \
    "v_smfmac_f32_16x16x64_bf16 v[132:135], v[96:99], a[96:103], %[sidx]\n\t" \
    "v_smfmac_f32_16x16x64_bf16 v[136:139], v[96:99], a[176:183], %[sidx]\n\t" \
    "v_smfmac_f32_16x16x64_bf16 v[140:143], v[96:99], v[24:31], %[sidx]\n\t" \
    "s_waitcnt vmcnt(7)\n\t" \
    "v_smfmac_f32_16x16x64_bf16 v[128:131], v[100:103], a[24:31], %[sidx]\n\t" \
    "v_smfmac_f32_16x16x64_bf16 v[132:135], v[100:103], a[104:111], %[sidx]\n\t" \
    "v_smfmac_f32_16x16x64_bf16 v[136:139], v[100:103], a[184:191], %[sidx]\n\t" \
    "v_smfmac_f32_16x16x64_bf16 v[140:143], v[100:103], v[32:39], %[sidx]\n\t" \
    "s_waitcnt vmcnt(6)\n\t" \
    "v_smfmac_f32_16x16x64_bf16 v[128:131], v[104:107], a[32:39], %[sidx]\n\t" \
    "v_smfmac_f32_16x16x64_bf16 v[132:135], v[104:107], a[112:119], %[sidx]\n\t" \
    "v_smfmac_f32_16x16x64_bf16 v[136:139], v[104:107], a[192:199], %[sidx]\n\t" \
    "v_smfmac_f32_16x16x64_bf16 v[140:143], v[104:107], v[40:47], %[sidx]\n\t" \
    "s_waitcnt vmcnt(5)\n\t" \
    "v_smfmac_f32_16x16x64_bf16 v[128:131], v[108:111], a[40:47], %[sidx]\n\t" \
    "v_smfmac_f32_16x16x64_bf16 v[132:135], v[108:111], a[120:127], %[sidx]\n\t" \
    "v_smfmac_f32_16x16x64_bf16 v[136:139], v[108:111], a[200:207], %[sidx]\n\t" \
    "v_smfmac_f32_16x16x64_bf16 v[140:143], v[108:111], v[48:55], %[sidx]\n\t" \
    "s_waitcnt vmcnt(4)\n\t" \
    "v_smfmac_f32_16x16x64_bf16 v[128:131], v[112:115], a[48:55], %[sidx]\n\t" \
    "v_smfmac_f32_16x16x64_bf16 v[132:135], v[112:115], a[128:135], %[sidx]\n\t" \
    "v_smfmac_f32_16x16x64_bf16 v[136:139], v[112:115], a[208:215], %[sidx]\n\t" \
    "v_smfmac_f32_16x16x64_bf16 v[140:143], v[112:115], v[56:63], %[sidx]\n\t" \
    "s_waitcnt vmcnt(3)\n\t" \
    "v_smfmac_f32_16x16x64_bf16 v[128:131], v[116:119], a[56:63], %[sidx]\n\t" \
    "v_smfmac_f32_16x16x64_bf16 v[132:135], v[116:119], a[136:143], %[sidx]\n\t" \
    "v_smfmac_f32_16x16x64_bf16 v[136:139], v[116:119], a[216:223], %[sidx]\n\t" \
    "v_smfmac_f32_16x16x64_bf16 v[140:143], v[116:119], v[64:71], %[sidx]\n\t" \
    "s_waitcnt vmcnt(2)\n\t" \
    "v_smfmac_f32_16x16x64_bf16 v[128:131], v[120:123], a[64:71], %[sidx]\n\t" \
    "v_smfmac_f32_16x16x64_bf16 v[132:135], v[120:123], a[144:151], %[sidx]\n\t" \
    "v_smfmac_f32_16x16x64_bf16 v[136:139], v[120:123], a[224:231], %[sidx]\n\t" \
    "v_smfmac_f32_16x16x64_bf16 v[140:143], v[120:123], v[72:79], %[sidx]\n\t" \
    "s_waitcnt vmcnt(1)\n\t" \
    "v_smfmac_f32_16x16x64_bf16 v[128:131], v[124:127], a[72:79], %[sidx]\n\t" \
    "v_smfmac_f32_16x16x64_bf16 v[132:135], v[124:127], a[152:159], %[sidx]\n\t" \
    "v_smfmac_f32_16x16x64_bf16 v[136:139], v[124:127], a[232:239], %[sidx]\n\t" \
    "v_smfmac_f32_16x16x64_bf16 v[140:143], v[124:127], v[80:87], %[sidx]\n\t" \
    "s_nop 7\n\t" \
    "v_add_f32 v128, v128, v130\n\t" \
    "v_add_f32 v129, v129, v131\n\t" \
    "v_cmp_u_f32 vcc, v128, v129\n\t" \
    "s_cbranch_vccnz zs_41_%=\n\t" \
    "zo_40_%=:\n\t" \
    "v_cvt_pk_bf16_f32 v128, v128, v129\n\t" \
    "ds_write_b32 %[ztw], v128 offset:0\n\t" \
    "v_add_f32 v132, v132, v134\n\t" \
    "v_add_f32 v133, v133, v135\n\t" \
    "v_cvt_pk_bf16_f32 v132, v132, v133\n\t" \
    "ds_write_b32 %[ztw], v132 offset:256\n\t" \
    "v_add_f32 v136, v136, v138\n\t" \
    "v_add_f32 v137, v137, v139\n\t" \
    "v_cvt_pk_bf16_f32 v136, v136, v137\n\t" \
    "ds_write_b32 %[ztw], v136 offset:512\n\t" \
    "v_add_f32 v140, v140, v142\n\t" \
    "v_add_f32 v141, v141, v143\n\t" \
    "v_cvt_pk_bf16_f32 v140, v140, v141\n\t" \
    "ds_write_b32 %[ztw], v140 offset:768\n\t" \
    "ds_read_b128 v[176:179], %[ztr]\n\t" \
    "s_waitcnt vmcnt(0)\n\t" \
    "sg_50_%=:\n\t" \
    "v_max_u32 v206, v204, v205\n\t" \
    "v_cmp_eq_u32 vcc, -1, v206\n\t" \
    "s_cbranch_vccz st_49_%=\n\t" \
    "buffer_load_dwordx2 v[204:205], %[tsa], %[srd], s46 offen sc1\n\t" \
    "s_waitcnt vmcnt(0)\n\t" \
    "s_add_u32 s53, s53, 1\n\t" \
    "s_cmp_gt_u32 s53, 131072\n\t" \
    "s_cbranch_scc1 ab_51_%=\n\t" \
    "s_and_b32 s54, s53, 63\n\t" \
    "s_cmp_lg_u32 s54, 0\n\t" \
    "s_cbranch_scc1 sg_50_%=\n\t" \
    "v_mov_b32 v208, 0\n\t" \
    "s_nop 0\n\t" \
    "buffer_load_dword v209, v208, %[srd], 0 offen sc1\n\t" \
    "s_waitcnt vmcnt(0)\n\t" \
    "v_readfirstlane_b32 s54, v209\n\t" \
    "s_nop 3\n\t" \
    "s_cmp_eq_u32 s54, 0\n\t" \
    "s_cbranch_scc1 sg_50_%=\n\t" \
    "s_branch z_end_%=\n\t" \
    "ab_51_%=:\n\t" \
    "v_mov_b32 v208, 0\n\t" \
    "v_mov_b32 v209, 1\n\t" \
    "s_nop 0\n\t" \
    "buffer_store_dword v209, v208, %[srd], 0 offen sc1\n\t" \
    "s_branch z_end_%=\n\t" \
    "st_49_%=:\n\t" \
    "v_readfirstlane_b32 s54, v204\n\t" \
    "v_readfirstlane_b32 s55, v205\n\t" \
    "s_nop 1\n\t" \
    "v_subrev_u32 v206, s54, v204\n\t" \
    "s_nop 1\n\t" \
    "v_max_i32_dpp v206, v206, v206 row_ror:8 row_mask:0xf bank_mask:0xf\n\t" \
    "s_nop 1\n\t" \
    "v_max_i32_dpp v206, v206, v206 row_ror:4 row_mask:0xf bank_mask:0xf\n\t" \
    "s_nop 1\n\t" \
    "v_max_i32_dpp v206, v206, v206 row_ror:2 row_mask:0xf bank_mask:0xf\n\t" \
    "s_nop 1\n\t" \
    "v_max_i32_dpp v206, v206, v206 row_ror:1 row_mask:0xf bank_mask:0xf\n\t" \
    "s_nop 0\n\t" \
    "v_readfirstlane_b32 s56, v206\n\t" \
    "s_nop 3\n\t" \
    "s_add_u32 s54, s54, s56\n\t" \
    "s_cmp_lt_u32 s40, 3\n\t" \
    "s_cbranch_scc1 su_53_%=\n\t" \
    "s_sub_i32 s56, s54, s42\n\t" \
    "s_cmp_eq_u32 s65, 0\n\t" \
    "s_cbranch_scc1 s1_54_%=\n\t" \
    "s_add_i32 s57, s44, 14\n\t" \
    "s_cmp_gt_i32 s55, s57\n\t" \
    "s_cbranch_scc1 sx_55_%=\n\t" \
    "s_sub_i32 s44, s44, %[decay]\n\t" \
    "s_max_i32 s44, s44, s55\n\t" \
    "s1_54_%=:\n\t" \
    "s_add_i32 s57, s45, 14\n\t" \
    "s_cmp_gt_i32 s56, s57\n\t" \
    "s_cbranch_scc1 sy_56_%=\n\t" \
    "s_sub_i32 s45, s45, %[decay]\n\t" \
    "s_max_i32 s45, s45, s56\n\t" \
    "s_branch su_53_%=\n\t" \
    "sx_55_%=:\n\t" \
    "s_add_i32 s44, s44, 6\n\t" \
    "s_add_i32 s64, s64, 4\n\t" \
    "s_min_i32 s64, s64, 40\n\t" \
    "s_branch su_53_%=\n\t" \
    "sy_56_%=:\n\t" \
    "s_cmp_eq_u32 s65, 0\n\t" \
    "s_cbranch_scc1 su_53_%=\n\t" \
    "s_add_i32 s45, s45, 6\n\t" \
    "s_add_i32 s63, s63, 4\n\t" \
    "s_min_i32 s63, s63, 40\n\t" \
    "su_53_%=:\n\t" \
    "s_min_i32 s44, s44, 300\n\t" \
    "s_min_i32 s45, s45, 300\n\t" \
    "s_mov_b32 s65, 0\n\t" \
    "s_add_u32 s43, s54, %[pena]\n\t" \
    "s_cmp_eq_u32 s40, 0\n\t" \
    "s_cbranch_scc1 se_48_%=\n\t" \
    "s_add_u32 s56, s54, %[tva]\n\t" \
    "s_sub_i32 s56, s41, s56\n\t" \
    "s_cmp_lt_i32 s56, -14\n\t" \
    "s_cbranch_scc1 sl_52_%=\n\t" \
    "s_mov_b32 s43, s41\n\t" \
    "s_mov_b32 s65, 1\n\t" \
    "s_branch se_48_%=\n\t" \
    "sl_52_%=:\n\t" \
    "s_add_u32 s57, s41, 90\n\t" \
    "s_cmp_gt_i32 s56, -30\n\t" \
    "s_cselect_b32 s43, s57, s43\n\t" \
    "se_48_%=:\n\t" \
    "s_add_u32 s42, s43, s44\n\t" \
    "s_add_u32 s42, s42, %[tvb]\n\t" \
    "s_add_u32 s42, s42, s63\n\t" \
    "s_add_u32 s41, s42, s45\n\t" \
    "s_add_u32 s41, s41, %[tva]\n\t" \
    "s_add_u32 s41, s41, s64\n\t" \
    "s_waitcnt lgkmcnt(0)\n\t" \
    "buffer_store_dwordx4 v[176:179], %[gzo], %[srd], s50 offen sc1\n\t" \
    "s_mov_b32 s50, s51\n\t" \
    "s_cmp_eq_u32 s40, 63\n\t" \
    "s_cbranch_scc1 ze_5_%=\n\t" \
    "s_memrealtime s[58:59]\n\t" \
    "s_waitcnt lgkmcnt(0)\n\t" \
    "s_sub_i32 s54, s42, s58\n\t" \
    "s_cmp_le_i32 s54, 4\n\t" \
    "s_cbranch_scc1 wd_58_%=\n\t" \
    "s_cmp_gt_i32 s54, 2000\n\t" \
    "s_cbranch_scc1 wd_58_%=\n\t" \
    "w1_59_%=:\n\t" \
    "s_memrealtime s[58:59]\n\t" \
    "s_waitcnt lgkmcnt(0)\n\t" \
    "s_sub_i32 s54, s42, s58\n\t" \
    "s_cmp_gt_i32 s54, 4\n\t" \
    "s_cbranch_scc1 w1_59_%=\n\t" \
    "w2_60_%=:\n\t" \
    "s_cmp_lt_i32 s54, 2\n\t" \
    "s_cbranch_scc1 wd_58_%=\n\t" \
    "s_nop 3\n\t" \
    "s_sub_i32 s54, s54, 1\n\t" \
    "s_branch w2_60_%=\n\t" \
    "wd_58_%=:\n\t" \
    "s_mov_b32 s53, 0\n\t" \
    "buffer_load_dwordx4 v[180:183], %[pofx], %[srd], s49 offen offset:0 sc1\n\t" \
    "buffer_load_dwordx4 v[184:187], %[pofx], %[srd], s49 offen offset:1024 sc1\n\t" \
    "buffer_load_dwordx4 v[188:191], %[pofx], %[srd], s49 offen offset:2048 sc1\n\t" \
    "buffer_load_dwordx4 v[192:195], %[pofx], %[srd], s49 offen offset:3072 sc1\n\t" \
    "buffer_load_dwordx4 v[196:199], %[pofm], %[srd], s49 offen sc1\n\t" \
    "buffer_load_dword v207, %[poft], %[srd], s49 offen sc1\n\t" \
    "zq_61_%=:\n\t" \
    "s_waitcnt vmcnt(5)\n\t" \
    "v_mfma_f32_16x16x32_bf16 v[200:203], v[160:163], v[180:183], 0\n\t" \
    "s_waitcnt vmcnt(4)\n\t" \
    "v_mfma_f32_16x16x32_bf16 v[200:203], v[164:167], v[184:187], v[200:203]\n\t" \
    "s_waitcnt vmcnt(3)\n\t" \
    "v_mfma_f32_16x16x32_bf16 v[200:203], v[168:171], v[188:191], v[200:203]\n\t" \
    "s_waitcnt vmcnt(2)\n\t" \
    "v_mfma_f32_16x16x32_bf16 v[200:203], v[172:175], v[192:195], v[200:203]\n\t" \
    "s_waitcnt vmcnt(1)\n\t" \
    "v_mfma_f32_16x16x32_bf16 v[200:203], v[176:179], v[196:199], v[200:203]\n\t" \
    "s_nop 7\n\t" \
    "ds_write2_b32 %[ppw], v200, v201 offset1:8\n\t" \
    "ds_write2_b32 %[ppw], v202, v203 offset0:16 offset1:24\n\t" \
    "v_cmp_u_f32 vcc, v200, v200\n\t" \
    "s_cbranch_vccnz zt_63_%=\n\t" \
    "zp_62_%=:\n\t" \
    "s_waitcnt lgkmcnt(0)\n\t" \
    "s_barrier\n\t" \
    "s_cmp_lg_u32 %[w], 0\n\t" \
    "s_cbranch_scc1 z3_64_%=\n\t" \
    "ds_read_b128 v[180:183], %[ppr]\n\t" \
    "ds_read_b128 v[184:187], %[ppr] offset:16\n\t" \
    "s_waitcnt lgkmcnt(0)\n\t" \
    "v_pk_add_f32 v[180:181], v[180:181], v[182:183]\n\t" \
    "v_pk_add_f32 v[184:185], v[184:185], v[186:187]\n\t" \
    "v_pk_add_f32 v[180:181], v[180:181], v[184:185]\n\t" \
    "v_add_f32 v180, v180, v181\n\t" \
    "s_nop 1\n\t" \
    "v_mov_b32_dpp v181, v180 quad_perm:[1,0,3,2] row_mask:0xf bank_mask:0xf\n\t" \
    "v_cvt_pk_bf16_f32 v180, v180, v181\n\t" \
    "v_min_u32 v180, -2, v180\n\t" \
    "buffer_store_dword v180, %[mo], %[srd], s46 offen sc1\n\t" \
    "s_branch zf_65_%=\n\t" \
    "z3_64_%=:\n\t" \
    "s_cmp_eq_u32 %[tsw], 0\n\t" \
    "s_cbranch_scc1 zf_65_%=\n\t" \
    "s_memrealtime s[58:59]\n\t" \
    "tg_67_%=:\n\t" \
    "s_waitcnt vmcnt(0)\n\t" \
    "v_cmp_eq_u32 vcc, -1, v207\n\t" \
    "s_cbranch_vccz tk_66_%=\n\t" \
    "buffer_load_dword v207, %[poft], %[srd], s49 offen sc1\n\t" \
    "s_add_u32 s53, s53, 1\n\t" \
    "s_cmp_gt_u32 s53, 131072\n\t" \
    "s_cbranch_scc1 ab_68_%=\n\t" \
    "s_and_b32 s54, s53, 63\n\t" \
    "s_cmp_lg_u32 s54, 0\n\t" \
    "s_cbranch_scc1 tg_67_%=\n\t" \
    "v_mov_b32 v208, 0\n\t" \
    "s_nop 0\n\t" \
    "buffer_load_dword v209, v208, %[srd], 0 offen sc1\n\t" \
    "s_waitcnt vmcnt(0)\n\t" \
    "v_readfirstlane_b32 s54, v209\n\t" \
    "s_nop 3\n\t" \
    "s_cmp_eq_u32 s54, 0\n\t" \
    "s_cbranch_scc1 tg_67_%=\n\t" \
    "s_branch z_end_%=\n\t" \
    "ab_68_%=:\n\t" \
    "v_mov_b32 v208, 0\n\t" \
    "v_mov_b32 v209, 1\n\t" \
    "s_nop 0\n\t" \
    "buffer_store_dword v209, v208, %[srd], 0 offen sc1\n\t" \
    "s_branch z_end_%=\n\t" \
    "tk_66_%=:\n\t" \
    "v_subrev_u32 v181, s43, v207\n\t" \
    "s_nop 1\n\t" \
    "v_max_i32_dpp v181, v181, v181 row_ror:8 row_mask:0xf bank_mask:0xf\n\t" \
    "s_nop 1\n\t" \
    "v_max_i32_dpp v181, v181, v181 row_ror:4 row_mask:0xf bank_mask:0xf\n\t" \
    "s_nop 1\n\t" \
    "v_max_i32_dpp v181, v181, v181 row_ror:2 row_mask:0xf bank_mask:0xf\n\t" \
    "s_nop 1\n\t" \
    "v_max_i32_dpp v181, v181, v181 row_ror:1 row_mask:0xf bank_mask:0xf\n\t" \
    "v_max_i32 v181, 0, v181\n\t" \
    "s_waitcnt lgkmcnt(0)\n\t" \
    "s_min_u32 s58, s58, -2\n\t" \
    "v_mov_b32 v180, s58\n\t" \
    "s_nop 0\n\t" \
    "buffer_store_dwordx2 v[180:181], %[tso], %[srd], s46 offen sc1\n\t" \
    "v_mov_b32 v182, s40\n\t" \
    "s_nop 0\n\t" \
    "buffer_store_dword v182, %[prgo], %[srd], 0 offen sc1\n\t" \
    "zf_65_%=:\n\t" \
    "buffer_load_dwordx4 v[144:147], %[guo], %[srd], s50 offen offset:0 sc1\n\t" \
    "buffer_load_dwordx4 v[148:151], %[guo], %[srd], s50 offen offset:1024 sc1\n\t" \
    "buffer_load_dwordx4 v[152:155], %[guo], %[srd], s50 offen offset:2048 sc1\n\t" \
    "buffer_load_dwordx4 v[156:159], %[guo], %[srd], s50 offen offset:3072 sc1\n\t" \
    "s_add_u32 s46, s46, 12288\n\t" \
    "s_add_u32 s47, s47, 12288\n\t" \
    "s_add_u32 s48, s48, 12288\n\t" \
    "s_add_u32 s49, s49, 22528\n\t" \
    "s_add_u32 s40, s40, 1\n\t" \
    "s_add_u32 s61, s61, 2\n\t" \
    "s_waitcnt lgkmcnt(0)\n\t" \
    "s_barrier\n\t" \
    "s_cmp_lt_u32 s40, 64\n\t" \
    "s_cbranch_scc1 zl_4_%=\n\t" \
    "ze_5_%=:\n\t" \
    "s_waitcnt vmcnt(0) lgkmcnt(0)\n\t" \
    "s_barrier\n\t" \
    "s_cmp_lg_u32 %[w], 0\n\t" \
    "s_cbranch_scc1 fz_69_%=\n\t" \
    "v_mov_b32 v206, 1\n\t" \
    "s_nop 0\n\t" \
    "buffer_atomic_add v206, %[fino], %[srd], 0 offen\n\t" \
    "fz_69_%=:\n\t" \
    "s_branch z_end_%=\n\t" \
    "zs_9_%=:\n\t" \
    "s_cmp_lg_u32 s62, 0\n\t" \
    "s_cbranch_scc1 zo_8_%=\n\t" \
    "s_mov_b32 s62, 1\n\t" \
    "s_waitcnt vmcnt(0)\n\t" \
    "sa_70_%=:\n\t" \
    "buffer_load_dwordx4 v[88:91], %[mlo], %[srd], s46 offen offset:0 sc1\n\t" \
    "s_nop 7\n\t" \
    "buffer_load_dwordx4 v[92:95], %[mlo], %[srd], s46 offen offset:1024 sc1\n\t" \
    "s_nop 7\n\t" \
    "buffer_load_dwordx4 v[96:99], %[mlo], %[srd], s46 offen offset:2048 sc1\n\t" \
    "s_nop 7\n\t" \
    "buffer_load_dwordx4 v[100:103], %[mlo], %[srd], s46 offen offset:3072 sc1\n\t" \
    "s_nop 7\n\t" \
    "buffer_load_dwordx4 v[104:107], %[mlo], %[srd], s47 offen offset:0 sc1\n\t" \
    "s_nop 7\n\t" \
    "buffer_load_dwordx4 v[108:111], %[mlo], %[srd], s47 offen offset:1024 sc1\n\t" \
    "s_nop 7\n\t" \
    "buffer_load_dwordx4 v[112:115], %[mlo], %[srd], s47 offen offset:2048 sc1\n\t" \
    "s_nop 7\n\t" \
    "buffer_load_dwordx4 v[116:119], %[mlo], %[srd], s47 offen offset:3072 sc1\n\t" \
    "s_nop 7\n\t" \
    "buffer_load_dwordx4 v[120:123], %[mlo], %[srd], s48 offen offset:0 sc1\n\t" \
    "s_nop 7\n\t" \
    "buffer_load_dwordx4 v[124:127], %[mlo], %[srd], s48 offen offset:1024 sc1\n\t" \
    "s_nop 7\n\t" \
    "buffer_load_dwordx2 v[204:205], %[tsa], %[srd], s46 offen sc1\n\t" \
    "s_waitcnt vmcnt(0)\n\t" \
    "v_max_u32 v208, v88, v89\n\t" \
    "v_max3_u32 v208, v208, v90, v91\n\t" \
    "v_max3_u32 v208, v208, v92, v93\n\t" \
    "v_max3_u32 v208, v208, v94, v95\n\t" \
    "v_max3_u32 v208, v208, v96, v97\n\t" \
    "v_max3_u32 v208, v208, v98, v99\n\t" \
    "v_max3_u32 v208, v208, v100, v101\n\t" \
    "v_max3_u32 v208, v208, v102, v103\n\t" \
    "v_max3_u32 v208, v208, v104, v105\n\t" \
    "v_max3_u32 v208, v208, v106, v107\n\t" \
    "v_max3_u32 v208, v208, v108, v109\n\t" \
    "v_max3_u32 v208, v208, v110, v111\n\t" \
    "v_max3_u32 v208, v208, v112, v113\n\t" \
    "v_max3_u32 v208, v208, v114, v115\n\t" \
    "v_max3_u32 v208, v208, v116, v117\n\t" \
    "v_max3_u32 v208, v208, v118, v119\n\t" \
    "v_max3_u32 v208, v208, v120, v121\n\t" \
    "v_max3_u32 v208, v208, v122, v123\n\t" \
    "v_max3_u32 v208, v208, v124, v125\n\t" \
    "v_max3_u32 v208, v208, v126, v127\n\t" \
    "v_cmp_eq_u32 vcc, -1, v208\n\t" \
    "s_cbranch_vccz zr_7_%=\n\t" \
    "s_add_u32 s53, s53, 1\n\t" \
    "s_cmp_gt_u32 s53, 131072\n\t" \
    "s_cbranch_scc1 ab_71_%=\n\t" \
    "s_and_b32 s54, s53, 63\n\t" \
    "s_cmp_lg_u32 s54, 0\n\t" \
    "s_cbranch_scc1 sa_70_%=\n\t" \
    "v_mov_b32 v210, 0\n\t" \
    "s_nop 0\n\t" \
    "buffer_load_dword v211, v210, %[srd], 0 offen sc1\n\t" \
    "s_waitcnt vmcnt(0)\n\t" \
    "v_readfirstlane_b32 s54, v211\n\t" \
    "s_nop 3\n\t" \
    "s_cmp_eq_u32 s54, 0\n\t" \
    "s_cbranch_scc1 sa_70_%=\n\t" \
    "s_branch z_end_%=\n\t" \
    "ab_71_%=:\n\t" \
    "v_mov_b32 v210, 0\n\t" \
    "v_mov_b32 v211, 1\n\t" \
    "s_nop 0\n\t" \
    "buffer_store_dword v211, v210, %[srd], 0 offen sc1\n\t" \
    "s_branch z_end_%=\n\t" \
    "zt_31_%=:\n\t" \
    "v_max_u32 v206, v180, v181\n\t" \
    "v_max3_u32 v206, v206, v182, v183\n\t" \
    "v_max3_u32 v206, v206, v184, v185\n\t" \
    "v_max3_u32 v206, v206, v186, v187\n\t" \
    "v_max3_u32 v206, v206, v188, v189\n\t" \
    "v_max3_u32 v206, v206, v190, v191\n\t" \
    "v_max3_u32 v206, v206, v192, v193\n\t" \
    "v_max3_u32 v206, v206, v194, v195\n\t" \
    "v_max3_u32 v206, v206, v196, v197\n\t" \
    "v_max3_u32 v206, v206, v198, v199\n\t" \
    "v_cmp_eq_u32 vcc, -1, v206\n\t" \
    "s_cbranch_vccz zp_30_%=\n\t" \
    "pa_72_%=:\n\t" \
    "buffer_load_dwordx4 v[180:183], %[pofx], %[srd], s49 offen offset:0 sc1\n\t" \
    "buffer_load_dwordx4 v[184:187], %[pofx], %[srd], s49 offen offset:1024 sc1\n\t" \
    "buffer_load_dwordx4 v[188:191], %[pofx], %[srd], s49 offen offset:2048 sc1\n\t" \
    "buffer_load_dwordx4 v[192:195], %[pofx], %[srd], s49 offen offset:3072 sc1\n\t" \
    "buffer_load_dwordx4 v[196:199], %[pofm], %[srd], s49 offen sc1\n\t" \
    "s_waitcnt vmcnt(0)\n\t" \
    "v_max_u32 v206, v180, v181\n\t" \
    "v_max3_u32 v206, v206, v182, v183\n\t" \
    "v_max3_u32 v206, v206, v184, v185\n\t" \
    "v_max3_u32 v206, v206, v186, v187\n\t" \
    "v_max3_u32 v206, v206, v188, v189\n\t" \
    "v_max3_u32 v206, v206, v190, v191\n\t" \
    "v_max3_u32 v206, v206, v192, v193\n\t" \
    "v_max3_u32 v206, v206, v194, v195\n\t" \
    "v_max3_u32 v206, v206, v196, v197\n\t" \
    "v_max3_u32 v206, v206, v198, v199\n\t" \
    "v_cmp_eq_u32 vcc, -1, v206\n\t" \
    "s_cbranch_vccz zq_29_%=\n\t" \
    "s_add_u32 s53, s53, 1\n\t" \
    "s_cmp_gt_u32 s53, 131072\n\t" \
    "s_cbranch_scc1 ab_73_%=\n\t" \
    "s_and_b32 s54, s53, 63\n\t" \
    "s_cmp_lg_u32 s54, 0\n\t" \
    "s_cbranch_scc1 pa_72_%=\n\t" \
    "v_mov_b32 v208, 0\n\t" \
    "s_nop 0\n\t" \
    "buffer_load_dword v209, v208, %[srd], 0 offen sc1\n\t" \
    "s_waitcnt vmcnt(0)\n\t" \
    "v_readfirstlane_b32 s54, v209\n\t" \
    "s_nop 3\n\t" \
    "s_cmp_eq_u32 s54, 0\n\t" \
    "s_cbranch_scc1 pa_72_%=\n\t" \
    "s_branch z_end_%=\n\t" \
    "ab_73_%=:\n\t" \
    "v_mov_b32 v208, 0\n\t" \
    "v_mov_b32 v209, 1\n\t" \
    "s_nop 0\n\t" \
    "buffer_store_dword v209, v208, %[srd], 0 offen sc1\n\t" \
    "s_branch z_end_%=\n\t" \
    "zs_41_%=:\n\t" \
    "s_cmp_lg_u32 s62, 0\n\t" \
    "s_cbranch_scc1 zo_40_%=\n\t" \
    "s_mov_b32 s62, 1\n\t" \
    "s_waitcnt vmcnt(0)\n\t" \
    "sa_74_%=:\n\t" \
    "buffer_load_dwordx4 v[88:91], %[mlo], %[srd], s46 offen offset:0 sc1\n\t" \
    "s_nop 7\n\t" \
    "buffer_load_dwordx4 v[92:95], %[mlo], %[srd], s46 offen offset:1024 sc1\n\t" \
    "s_nop 7\n\t" \
    "buffer_load_dwordx4 v[96:99], %[mlo], %[srd], s46 offen offset:2048 sc1\n\t" \
    "s_nop 7\n\t" \
    "buffer_load_dwordx4 v[100:103], %[mlo], %[srd], s46 offen offset:3072 sc1\n\t" \
    "s_nop 7\n\t" \
    "buffer_load_dwordx4 v[104:107], %[mlo], %[srd], s47 offen offset:0 sc1\n\t" \
    "s_nop 7\n\t" \
    "buffer_load_dwordx4 v[108:111], %[mlo], %[srd], s47 offen offset:1024 sc1\n\t" \
    "s_nop 7\n\t" \
    "buffer_load_dwordx4 v[112:115], %[mlo], %[srd], s47 offen offset:2048 sc1\n\t" \
    "s_nop 7\n\t" \
    "buffer_load_dwordx4 v[116:119], %[mlo], %[srd], s47 offen offset:3072 sc1\n\t" \
    "s_nop 7\n\t" \
    "buffer_load_dwordx4 v[120:123], %[mlo], %[srd], s48 offen offset:0 sc1\n\t" \
    "s_nop 7\n\t" \
    "buffer_load_dwordx4 v[124:127], %[mlo], %[srd], s48 offen offset:1024 sc1\n\t" \
    "s_nop 7\n\t" \
    "buffer_load_dwordx2 v[204:205], %[tsa], %[srd], s46 offen sc1\n\t" \
    "s_waitcnt vmcnt(0)\n\t" \
    "v_max_u32 v208, v88, v89\n\t" \
    "v_max3_u32 v208, v208, v90, v91\n\t" \
    "v_max3_u32 v208, v208, v92, v93\n\t" \
    "v_max3_u32 v208, v208, v94, v95\n\t" \
    "v_max3_u32 v208, v208, v96, v97\n\t" \
    "v_max3_u32 v208, v208, v98, v99\n\t" \
    "v_max3_u32 v208, v208, v100, v101\n\t" \
    "v_max3_u32 v208, v208, v102, v103\n\t" \
    "v_max3_u32 v208, v208, v104, v105\n\t" \
    "v_max3_u32 v208, v208, v106, v107\n\t" \
    "v_max3_u32 v208, v208, v108, v109\n\t" \
    "v_max3_u32 v208, v208, v110, v111\n\t" \
    "v_max3_u32 v208, v208, v112, v113\n\t" \
    "v_max3_u32 v208, v208, v114, v115\n\t" \
    "v_max3_u32 v208, v208, v116, v117\n\t" \
    "v_max3_u32 v208, v208, v118, v119\n\t" \
    "v_max3_u32 v208, v208, v120, v121\n\t" \
    "v_max3_u32 v208, v208, v122, v123\n\t" \
    "v_max3_u32 v208, v208, v124, v125\n\t" \
    "v_max3_u32 v208, v208, v126, v127\n\t" \
    "v_cmp_eq_u32 vcc, -1, v208\n\t" \
    "s_cbranch_vccz zr_39_%=\n\t" \
    "s_add_u32 s53, s53, 1\n\t" \
    "s_cmp_gt_u32 s53, 131072\n\t" \
    "s_cbranch_scc1 ab_75_%=\n\t" \
    "s_and_b32 s54, s53, 63\n\t" \
    "s_cmp_lg_u32 s54, 0\n\t" \
    "s_cbranch_scc1 sa_74_%=\n\t" \
    "v_mov_b32 v210, 0\n\t" \
    "s_nop 0\n\t" \
    "buffer_load_dword v211, v210, %[srd], 0 offen sc1\n\t" \
    "s_waitcnt vmcnt(0)\n\t" \
    "v_readfirstlane_b32 s54, v211\n\t" \
    "s_nop 3\n\t" \
    "s_cmp_eq_u32 s54, 0\n\t" \
    "s_cbranch_scc1 sa_74_%=\n\t" \
    "s_branch z_end_%=\n\t" \
    "ab_75_%=:\n\t" \
    "v_mov_b32 v210, 0\n\t" \
    "v_mov_b32 v211, 1\n\t" \
    "s_nop 0\n\t" \
    "buffer_store_dword v211, v210, %[srd], 0 offen sc1\n\t" \
    "s_branch z_end_%=\n\t" \
    "zt_63_%=:\n\t" \
    "v_max_u32 v206, v180, v181\n\t" \
    "v_max3_u32 v206, v206, v182, v183\n\t" \
    "v_max3_u32 v206, v206, v184, v185\n\t" \
    "v_max3_u32 v206, v206, v186, v187\n\t" \
    "v_max3_u32 v206, v206, v188, v189\n\t" \
    "v_max3_u32 v206, v206, v190, v191\n\t" \
    "v_max3_u32 v206, v206, v192, v193\n\t" \
    "v_max3_u32 v206, v206, v194, v195\n\t" \
    "v_max3_u32 v206, v206, v196, v197\n\t" \
    "v_max3_u32 v206, v206, v198, v199\n\t" \
    "v_cmp_eq_u32 vcc, -1, v206\n\t" \
    "s_cbranch_vccz zp_62_%=\n\t" \
    "pa_76_%=:\n\t" \
    "buffer_load_dwordx4 v[180:183], %[pofx], %[srd], s49 offen offset:0 sc1\n\t" \
    "buffer_load_dwordx4 v[184:187], %[pofx], %[srd], s49 offen offset:1024 sc1\n\t" \
    "buffer_load_dwordx4 v[188:191], %[pofx], %[srd], s49 offen offset:2048 sc1\n\t" \
    "buffer_load_dwordx4 v[192:195], %[pofx], %[srd], s49 offen offset:3072 sc1\n\t" \
    "buffer_load_dwordx4 v[196:199], %[pofm], %[srd], s49 offen sc1\n\t" \
    "s_waitcnt vmcnt(0)\n\t" \
    "v_max_u32 v206, v180, v181\n\t" \
    "v_max3_u32 v206, v206, v182, v183\n\t" \
    "v_max3_u32 v206, v206, v184, v185\n\t" \
    "v_max3_u32 v206, v206, v186, v187\n\t" \
    "v_max3_u32 v206, v206, v188, v189\n\t" \
    "v_max3_u32 v206, v206, v190, v191\n\t" \
    "v_max3_u32 v206, v206, v192, v193\n\t" \
    "v_max3_u32 v206, v206, v194, v195\n\t" \
    "v_max3_u32 v206, v206, v196, v197\n\t" \
    "v_max3_u32 v206, v206, v198, v199\n\t" \
    "v_cmp_eq_u32 vcc, -1, v206\n\t" \
    "s_cbranch_vccz zq_61_%=\n\t" \
    "s_add_u32 s53, s53, 1\n\t" \
    "s_cmp_gt_u32 s53, 131072\n\t" \
    "s_cbranch_scc1 ab_77_%=\n\t" \
    "s_and_b32 s54, s53, 63\n\t" \
    "s_cmp_lg_u32 s54, 0\n\t" \
    "s_cbranch_scc1 pa_76_%=\n\t" \
    "v_mov_b32 v208, 0\n\t" \
    "s_nop 0\n\t" \
    "buffer_load_dword v209, v208, %[srd], 0 offen sc1\n\t" \
    "s_waitcnt vmcnt(0)\n\t" \
    "v_readfirstlane_b32 s54, v209\n\t" \
    "s_nop 3\n\t" \
    "s_cmp_eq_u32 s54, 0\n\t" \
    "s_cbranch_scc1 pa_76_%=\n\t" \
    "s_branch z_end_%=\n\t" \
    "ab_77_%=:\n\t" \
    "v_mov_b32 v208, 0\n\t" \
    "v_mov_b32 v209, 1\n\t" \
    "s_nop 0\n\t" \
    "buffer_store_dword v209, v208, %[srd], 0 offen sc1\n\t" \
    "s_branch z_end_%=\n\t" \
    "z_end_%=:\n\t" \
    "s_waitcnt vmcnt(0) lgkmcnt(0)\n\t"

__global__ void __launch_bounds__(256, 1) k_mega(MegaArgs a_) {
  extern __shared__ __attribute__((aligned(16))) unsigned char lds[];
  LAS unsigned char* L = (LAS unsigned char*)lds;
  const int Lb = (int)(unsigned)(size_t)L;
  const int tid = threadIdx.x, w = __builtin_amdgcn_readfirstlane(tid >> 6);
  int lane = 0;
  const int b = vblock();
  { const MegaArgs a = kargs(); TL(0); }
  if (b >= NS + NZ) { worker(L, Lb, lane, w); { const MegaArgs a = kargs(); TL(1); } return; }
  {
    const MegaArgs a = kargs();
    if (b >= NS) {
      const int i = (b - NS) >> 3;
      unit_wvo(a, (b & 7) + 8 * (i / 5), i % 5, L, Lb, lane, w, mk_srd(a.ws, 0, (unsigned)WS_END));
      if (!wait_ge(a.ws, C_W, 160u, (LAS unsigned*)(L + 147456), w)) return;
    }
  }
  const MegaArgs a = kargs();
  unsigned char* ws = a.ws;
  TL(2);
  lane = get_lane();
  v4i srd;
  srd.x = (int)(unsigned)(size_t)ws; srd.y = (int)((unsigned)((size_t)ws >> 32) & 0xFFFFu); srd.z = (int)(WS_END + 16384); srd.w = 0x00020000;
  const int sidx = (lane & 2) ? 0xEEEE : 0x4444;
  const int g16 = lane >> 4, col = lane & 15;
  const int l16 = Lb + lane * 16;
  const int g16b = lane * 16;
  const int mlo = lane * 16;
  const int tsa = 10240 + col * 128;
  const int profo = (lane == 0) ? (int)WS_END + (b * 4 + w) * 32 : OOB;
  const int fino = (lane == 0) ? C_FINAL : OOB;
  if (b < NS) {
    const int h = b;
    const v4i srdq = mk_srd(a.Wq, 0, D_ * HE * 2), srdk = mk_srd(a.Wk, 0, D_ * HE * 2);
    const int wsrc = (lane >> 1) * (HE * 2) + (h * E_ + 16 * w + 8 * (lane & 1)) * 2, wsrc2 = wsrc + 32 * HE * 2 - 1024;
    const int lwm = Lb + L_W + w * 16 * 2048;
    const int q4 = col >> 2;
    const int trd = lwm + (16 * (g16 >> 1) + 4 * (g16 & 1) + (q4 & 1) + 8 * (q4 >> 1)) * 32 + (lane & 3) * 8;
    const int lw = Lb + L_W + w * 16 * 2048 + lane * 16;
    const int qkw = Lb + L_QK + (16 * w + col) * 40 + g16 * 4;
    const int qkr = Lb + L_QK + (8 * g16 + (col >> 2)) * 40 + (lane & 3) * 8;
    const float sc2 = 0.07216878364870322f * 1.4426950408889634f;
    const float mb = (g16 >= 2) ? 0.f : -__builtin_inff();
    const int pox = (col < 8) ? ((int)OFF_MSGP + h * 512 + (g16 >> 1) * 128 + col * 16 + (g16 & 1) * 8) : OOB;
    const int pom = (col < 8 && g16 >= 2) ? ((int)OFF_MSGP + 16384 + h * 128 + col * 16 + (g16 & 1) * 8) : OOB;
    const int kxo = h * 12288 + 2 * ((w + 3) & 3) * 1024 + lane * 16;
    const int kxl = Lb + L_PART + 4 * ((w + 3) & 3) * 1024 + lane * 16;
    const int kmo = (col >= 8) ? ((h * 8 + (col & 7)) * E_ + 8 * g16) * 2 : OOB;
    const int tso = (lane == 0) ? ((int)OFF_MSGP + 20480 + (h >> 1) * 128) : OOB;
    const int flw = Lb + L_FLAG + w * 4, flr = Lb + L_FLAG;
    const int tsw = __builtin_amdgcn_readfirstlane((w == 1 && (h & 1) == 0) ? 1 : 0);
    asm volatile(RK_ASM_S
                 :
                 : [l16] "v"(l16), [sidx] "v"(sidx), [flw] "v"(flw), [flr] "v"(flr),
                   [wsrc] "v"(wsrc), [wsrc2] "v"(wsrc2), [trd] "v"(trd), [mlo] "v"(mlo), [lw] "v"(lw), [qkw] "v"(qkw), [qkr] "v"(qkr), [pox] "v"(pox), [pom] "v"(pom), [mb] "v"(mb),
                   [kxo] "v"(kxo), [kxl] "v"(kxl), [kmo] "v"(kmo), [tsa] "v"(tsa), [tso] "v"(tso), [profo] "v"(profo), [fino] "v"(fino),
                   [srd] "s"(srd), [srdq] "s"(srdq), [srdk] "s"(srdk), [w] "s"(w), [lwm] "s"(RFL(lwm)), [tva] "s"(a.tva), [tvb] "s"(a.tvb), [pena] "s"(a.pena),
                   [decay] "s"(a.decay), [sc] "s"(sc2), [nsc] "s"(-sc2), [tsw] "s"(tsw),
                   [offm] "i"((int)OFF_MSGM), [offkx] "i"((int)OFF_KX), [offkm] "i"((int)OFF_KM)
                 : RK_CLOB);
  } else {
    const int cb = b - NS;
    const __amdgpu_buffer_rsrc_t rs = __builtin_amdgcn_make_buffer_rsrc(ws, 0, (int)MSG_END, 0x00020000);
    if (w == 0) {
      if (lane < 8) {
        v4u v = *(const v4u*)(a.mem0 + lane * D_ + cb * 8);
        v.x = min(v.x, 0xFFFFFFFEu); v.y = min(v.y, 0xFFFFFFFEu); v.z = min(v.z, 0xFFFFFFFEu); v.w = min(v.w, 0xFFFFFFFEu);
        __builtin_amdgcn_raw_buffer_store_b128(v, rs, (int)OFF_MSGM + (cb >> 1) * 256 + (lane >> 1) * 64 + (cb & 1) * 32 + (lane & 1) * 16, 0, 16);
      }
      if (cb % 5 == 0) {
        const unsigned ta = min(rt(), 0xFFFFFFFEu);
        if (lane < 8) __builtin_amdgcn_raw_buffer_store_b128(v4u{ta, 130u, 0u, 0u}, rs, (int)OFF_MSGM + 10240 + (cb / 5) * 128 + lane * 16, 0, 16);
      }
    }
    const int wsoff = (int)OFF_WVO + (cb * 4 + w) * 40 * 2048;
    const int offg = (int)OFF_G + cb * 20480;
    const int guo = 4 * w * 1024 + lane * 16, gzo = (16 + w) * 1024 + lane * 16;
    const int ztw = Lb + L_QK + w * 1024 + col * 16 + g16 * 4;
    const int ztr = Lb + L_QK + w * 1024 + lane * 16;
    const bool dblk = ((g16 >> 1) == (col >> 3));
    const int ppw = Lb + L_PART + (dblk ? (((col & 7) * 8 + 4 * (g16 & 1)) * 32 + (w * 2 + (g16 >> 1)) * 4) : (2048 + (lane & 7) * 4 + (lane >> 3) * 128));
    const int ppr = Lb + L_PART + lane * 32;
    const int mo = ((lane & 1) == 0) ? (MEMB + (cb >> 1) * 256 + (lane >> 4) * 64 + (cb & 1) * 32 + ((lane >> 3) & 1) * 16 + (lane & 7) * 2) : OOB;
    const int pofx = (int)OFF_MSGP + 4 * w * 1024 + lane * 16, pofm = (int)OFF_MSGP + (16 + w) * 1024 + lane * 16;
    const int poft = (w == 3) ? ((int)OFF_MSGP + 20480 + col * 128) : OOB;
    const int tso = (lane == 0) ? (MEMB + 10240 + (cb / 5) * 128) : OOB;
    const int prgo = (lane == 0 && cb == 0) ? C_PROG : OOB;
    const int tsw = __builtin_amdgcn_readfirstlane((w == 3 && cb % 5 == 0) ? 1 : 0);
    asm volatile(RK_ASM_Z
                 :
                 : [sidx] "v"(sidx),
                   [g16b] "v"(g16b), [mlo] "v"(mlo), [guo] "v"(guo), [gzo] "v"(gzo), [ztw] "v"(ztw), [ztr] "v"(ztr), [ppw] "v"(ppw), [ppr] "v"(ppr),
                   [mo] "v"(mo), [pofx] "v"(pofx), [pofm] "v"(pofm), [poft] "v"(poft), [tsa] "v"(tsa), [tso] "v"(tso), [profo] "v"(profo), [fino] "v"(fino), [prgo] "v"(prgo),
                   [srd] "s"(srd), [w] "s"(w), [wsoff] "s"(wsoff), [tva] "s"(a.tva), [tvb] "s"(a.tvb), [pena] "s"(a.pena),
                   [decay] "s"(a.decay), [zlag] "s"(a.zlag), [tsw] "s"(tsw), [offg] "s"(offg),
                   [offm] "i"((int)OFF_MSGM)
                 : RK_CLOB);
  }
}

}

#define RK_TVA 26
#define RK_TVB 40
#define RK_PENA 150
#define RK_DECAY 1
#define RK_ZLAG 24

extern "C" void kernel_launch(void* const* d_in, const int* in_sizes, int n_in, void* d_out, int out_size, void* d_ws, size_t ws_size,
                              hipStream_t stream) {
  using namespace rk;
  const u16* x = (const u16*)d_in[0];
  const u16* mem0 = (const u16*)d_in[1];
  const u16* Wq = (const u16*)d_in[2];
  const u16* Wk = (const u16*)d_in[3];
  const u16* Wv = (const u16*)d_in[4];
  const u16* Wo = (const u16*)d_in[5];
  const u16* w1 = (const u16*)d_in[8];
  const u16* b1 = (const u16*)d_in[9];
  const u16* w2 = (const u16*)d_in[10];
  const u16* b2 = (const u16*)d_in[11];
  unsigned char* ws = (unsigned char*)d_ws;
  u16* out = (u16*)d_out;

  (void)hipFuncSetAttribute((const void*)k_mega, hipFuncAttributeMaxDynamicSharedMemorySize, L_BYTES);
  k_prep<<<640, 256, 0, stream>>>(x, ws);
  MegaArgs ma;
  ma.ws = ws; ma.x = x; ma.mem0 = mem0; ma.Wq = Wq; ma.Wk = Wk; ma.Wv = Wv; ma.Wo = Wo; ma.w1 = w1; ma.b1 = b1; ma.w2 = w2; ma.b2 = b2; ma.out = out;
  ma.tva = RK_TVA; ma.tvb = RK_TVB; ma.pena = RK_PENA; ma.zlag = RK_ZLAG; ma.decay = RK_DECAY; ma.pad = 0;
  k_mega<<<256, 256, L_BYTES, stream>>>(ma);
}
```
